```python
import torch

def _softmax(x, axis=-1):
    return torch.softmax(x, dim=axis)
def _gelu(x):
    return torch.nn.functional.gelu(x, approximate="tanh")
def _silu(x):
    return torch.nn.functional.silu(x)
def _sigmoid(x):
    return torch.sigmoid(x)
def _layernorm(x, axis=-1, eps=1e-5):
    m = x.mean(dim=axis, keepdim=True)
    v = x.var(dim=axis, keepdim=True, unbiased=False)
    return (x - m) / torch.sqrt(v + eps)
def _scatter_add(tgt, idx, src, axis=0):
    return tgt.clone().index_add_(axis, idx, src)
def _segment_sum(src, seg, n):
    out = torch.zeros((n,) + src.shape[1:], dtype=src.dtype)
    return out.index_add_(0, seg, src)
def _pad(x, pw):
    flat = tuple(v for pair in reversed(pw) for v in pair)
    return torch.nn.functional.pad(x, flat)

kernel_name = 'sequential_preLN__sliding_block__mixer_channel_mlp__prompt'

def setup_inputs():

    _w = (lambda a: (lambda t: t.to(torch.bfloat16) if t.is_floating_point() else t)(torch.as_tensor(a)))
    def _A_setup():
        B, S, D = 1, 2048, 1024
        return {
            "x": _w((lambda *s: torch.randn(*s, dtype=torch.bfloat16) * 0.05)(B, S, D)),
            "wqkv": _w((lambda *s: torch.randn(*s, dtype=torch.bfloat16) * 0.05)(D, 3 * D)),
            "wo": _w((lambda *s: torch.randn(*s, dtype=torch.bfloat16) * 0.05)(D, D)),
        }
    _A_in = _A_setup()

    def _B_setup():
        B, S, D, F = 1, 2048, 1024, 3584
        return {
            "x": _w((lambda *s: torch.randn(*s, dtype=torch.bfloat16) * 0.05)(B, S, D)),
            "w1": _w((lambda *s: torch.randn(*s, dtype=torch.bfloat16) * 3.2)(D, F) * (D ** -0.5)),
            "b1": _w((lambda *s: torch.randn(*s, dtype=torch.bfloat16) * 1.6)(F)),
            "w2": _w((lambda *s: torch.randn(*s, dtype=torch.bfloat16) * 0.4)(F, D) * (F ** -0.5)),
            "b2": _w((lambda *s: torch.randn(*s, dtype=torch.bfloat16) * 0.8)(D)),
        }
    _B_in = _B_setup()

    return {
        "A_x": _A_in["x"],
        "A_wqkv": _A_in["wqkv"],
        "A_wo": _A_in["wo"],
        "B_x": _B_in["x"],
        "B_w1": _B_in["w1"],
        "B_b1": _B_in["b1"],
        "B_w2": _B_in["w2"],
        "B_b2": _B_in["b2"],
    }

def reference(A_x, A_wqkv, A_wo, B_x, B_w1, B_b1, B_w2, B_b2):
    def _A_ref(x, wqkv, wo):
        B, S, D = x.shape
        H = 32; Dh = D // H; W = 256
        qkv = (lambda x, *d: x.permute(*d))((x @ wqkv).reshape(B, S, 3, H, Dh), 2, 0, 3, 1, 4)
        q, k, v = qkv[0], qkv[1], qkv[2]
        scores = torch.einsum("bhqd,bhkd->bhqk", q, k) * (Dh ** -0.5)
        idx = torch.arange(S)
        mask = (idx[:, None] - idx[None, :] >= 0) & (idx[:, None] - idx[None, :] < W)
        scores = torch.where(mask[None, None], scores, -1e9)
        p = _softmax(scores, axis=-1)
        o = torch.einsum("bhqk,bhkd->bhqd", p, v)
        return (lambda x, *d: x.permute(*d))(o, 0, 2, 1, 3).reshape(B, S, D) @ wo

    def _B_ref(x, w1, b1, w2, b2):
        y = _layernorm(x, axis=-1)
        h = _gelu(torch.einsum("bsd,df->bsf", y, w1) + b1)
        return torch.einsum("bsf,fd->bsd", h, w2) + b2

    x = A_x
    h = x + _A_ref(_layernorm(x, axis=-1), A_wqkv, A_wo)
    h2 = h + _B_ref(_layernorm(h, axis=-1), B_w1, B_b1, B_w2, B_b2)
    return h2
```

```cpp
#include <hip/hip_runtime.h>
#include <hip/hip_bf16.h>
#include <stdint.h>

typedef unsigned short u16;
typedef __attribute__((ext_vector_type(8))) short bf16x8;
typedef __attribute__((ext_vector_type(4))) short s16x4;
typedef __attribute__((ext_vector_type(4))) float f32x4;
typedef __attribute__((ext_vector_type(4))) unsigned u32x4;
typedef __attribute__((ext_vector_type(2))) unsigned u32x2;
typedef __attribute__((address_space(3))) const char* lds_cptr;

constexpr int S_LEN = 2048, DM = 1024, NH = 32, DH = 32, WIN = 256, FF = 3584;

__device__ __forceinline__ float bf2f(u16 v) { return __uint_as_float(((unsigned)v) << 16); }
__device__ __forceinline__ unsigned pk2(float lo, float hi) {
  unsigned r;
  asm("v_cvt_pk_bf16_f32 %0, %1, %2" : "=v"(r) : "v"(lo), "v"(hi));
  return r;
}
__device__ __forceinline__ s16x4 vtr(lds_cptr p) {
  return __builtin_bit_cast(s16x4, __builtin_amdgcn_ds_read_tr16_b64_v4i16((__attribute__((address_space(3))) s16x4*)p));
}

__device__ __forceinline__ float wave_sum(float v) {
#pragma unroll
  for (int o = 32; o > 0; o >>= 1) v += __shfl_xor(v, o, 64);
  return v;
}

template <int NLN>
__global__ __launch_bounds__(256) void ln_kernel(const u16* __restrict__ in, u16* __restrict__ out) {
  const int lane = threadIdx.x & 63;
  const int row = blockIdx.x * 4 + (threadIdx.x >> 6);
  const u32x4* src = (const u32x4*)(in + (size_t)row * DM);
  u32x4 a = src[lane], b = src[lane + 64];
  float v[16];
#pragma unroll
  for (int i = 0; i < 4; ++i) {
    v[2 * i] = __uint_as_float(a[i] << 16);
    v[2 * i + 1] = __uint_as_float(a[i] & 0xffff0000u);
    v[8 + 2 * i] = __uint_as_float(b[i] << 16);
    v[8 + 2 * i + 1] = __uint_as_float(b[i] & 0xffff0000u);
  }
#pragma unroll
  for (int it = 0; it < NLN; ++it) {
    float s = 0.f;
#pragma unroll
    for (int i = 0; i < 16; ++i) s += v[i];
    const float mean = wave_sum(s) * (1.0f / DM);
    float q = 0.f;
#pragma unroll
    for (int i = 0; i < 16; ++i) { v[i] -= mean; q += v[i] * v[i]; }
    const float var = wave_sum(q) * (1.0f / DM);
    const float r = 1.0f / sqrtf(var + 1e-5f);
#pragma unroll
    for (int i = 0; i < 16; ++i) v[i] *= r;
  }
  u32x4 oa, ob;
#pragma unroll
  for (int i = 0; i < 4; ++i) {
    oa[i] = pk2(v[2 * i], v[2 * i + 1]);
    ob[i] = pk2(v[8 + 2 * i], v[8 + 2 * i + 1]);
  }
  u32x4* dst = (u32x4*)(out + (size_t)row * DM);
  dst[lane] = oa;
  dst[lane + 64] = ob;
}

enum { EPI_PLAIN = 0, EPI_RES = 1, EPI_GELU = 2, EPI_BIAS_RES = 3 };

template <int BN, int NBUF, int EPI>
__global__ __launch_bounds__(256, 1) void gemm_kernel(const u16* __restrict__ A, const u16* __restrict__ W,
                                                     u16* __restrict__ C, const u16* __restrict__ bias,
                                                     const u16* __restrict__ res, int N, int K) {
  constexpr int BM = 128, BK = 64;
  constexpr int WN = BN / 2, NT = WN / 16;
  constexpr int ABYTES = BM * BK * 2, WBYTES = BK * BN * 2, STAGE = ABYTES + WBYTES;
  constexpr int GA = ABYTES / 4096, GW = WBYTES / 4096;
  constexpr int G = GA + GW;
  extern __shared__ __attribute__((aligned(16))) char smem[];

  const int tid = threadIdx.x, lane = tid & 63;
  const int wid = __builtin_amdgcn_readfirstlane(tid >> 6);
  const int wm = wid >> 1, wn = wid & 1;
  const int ntn = N / BN;
  const int bm = blockIdx.x / ntn, bn = blockIdx.x % ntn;
  const int m0 = bm * BM, n0 = bn * BN;
  const int nk = K / BK;

  const u16* asrc[GA];
  const u16* wsrc[GW];
#pragma unroll
  for (int i = 0; i < GA; ++i) {
    const int p = (wid * GA + i) * 64 + lane;
    const int row = p >> 3, ch = (p & 7) ^ (row & 7);
    asrc[i] = A + (size_t)(m0 + row) * K + ch * 8;
  }
#pragma unroll
  for (int i = 0; i < GW; ++i) {
    const int p = (wid * GW + i) * 64 + lane;
    const int s = p >> 4, rs = (p >> 1) & 7, half = p & 1;
    const int kb = s / (BN / 16), nb = s % (BN / 16);
    const int k = kb * 8 + (rs ^ ((kb & 1) << 2));
    wsrc[i] = W + (size_t)k * N + n0 + nb * 16 + half * 8;
  }
  auto stage = [&](int t, int buf) {
    char* base = smem + buf * STAGE;
#pragma unroll
    for (int i = 0; i < GA; ++i)
      __builtin_amdgcn_global_load_lds((const unsigned*)(asrc[i] + (size_t)t * BK),
                                       (unsigned*)(base + (wid * GA + i) * 1024 + lane * 16), 16, 0, 0);
#pragma unroll
    for (int i = 0; i < GW; ++i)
      __builtin_amdgcn_global_load_lds((const unsigned*)(wsrc[i] + (size_t)t * BK * N),
                                       (unsigned*)(base + ABYTES + (wid * GW + i) * 1024 + lane * 16), 16, 0, 0);
  };

  const int fr = lane & 15, g = lane >> 4, q = (lane >> 2) & 3, pp = lane & 3;
  int aoff[2];
#pragma unroll
  for (int ks = 0; ks < 2; ++ks) aoff[ks] = (wm * 64 + fr) * 128 + (((ks * 4 + g) ^ (fr & 7)) << 4);
  int woff[2];
#pragma unroll
  for (int r = 0; r < 2; ++r)
    woff[r] = ABYTES + (g * (BN / 16) + wn * NT) * 256 + (((4 * r + q) ^ ((g & 1) << 2)) << 5) + pp * 8;

  f32x4 acc[4][NT];
#pragma unroll
  for (int i = 0; i < 4; ++i)
#pragma unroll
    for (int j = 0; j < NT; ++j) acc[i][j] = f32x4{0.f, 0.f, 0.f, 0.f};

  auto compute = [&](int buf) {
    const char* base = smem + buf * STAGE;
#pragma unroll
    for (int ks = 0; ks < 2; ++ks) {
      bf16x8 xf[4], wf[NT];
#pragma unroll
      for (int i = 0; i < 4; ++i) xf[i] = *(const bf16x8*)(base + aoff[ks] + i * 2048);
#pragma unroll
      for (int j = 0; j < NT; ++j) {
        s16x4 lo = vtr((lds_cptr)(base + woff[0] + (ks * 4 * (BN / 16) + j) * 256));
        s16x4 hi = vtr((lds_cptr)(base + woff[1] + (ks * 4 * (BN / 16) + j) * 256));
        wf[j] = __builtin_shufflevector(lo, hi, 0, 1, 2, 3, 4, 5, 6, 7);
      }
#pragma unroll
      for (int i = 0; i < 4; ++i)
#pragma unroll
        for (int j = 0; j < NT; ++j)
          acc[i][j] = __builtin_amdgcn_mfma_f32_16x16x32_bf16(wf[j], xf[i], acc[i][j], 0, 0, 0);
    }
  };

#pragma unroll
  for (int s = 0; s < NBUF - 1; ++s) stage(s, s);
  int buf = 0;
  for (int t = 0; t < nk; ++t) {
    if (t + NBUF - 2 < nk) {
      asm volatile("s_waitcnt vmcnt(%0) lgkmcnt(0)" ::"i"((NBUF - 2) * G) : "memory");
    } else {
      asm volatile("s_waitcnt vmcnt(0) lgkmcnt(0)" ::: "memory");
    }
    __builtin_amdgcn_s_barrier();
    if (t + NBUF - 1 < nk) {
      int nb = buf + NBUF - 1;
      if (nb >= NBUF) nb -= NBUF;
      stage(t + NBUF - 1, nb);
    }
    compute(buf);
    buf = (buf + 1 == NBUF) ? 0 : buf + 1;
  }

#pragma unroll
  for (int i = 0; i < 4; ++i) {
    const int m = m0 + wm * 64 + i * 16 + fr;
#pragma unroll
    for (int j = 0; j < NT; ++j) {
      const int n = n0 + wn * WN + j * 16 + g * 4;
      float v[4];
#pragma unroll
      for (int r = 0; r < 4; ++r) v[r] = acc[i][j][r];
      if (EPI == EPI_GELU || EPI == EPI_BIAS_RES) {
        u32x2 bb = *(const u32x2*)(bias + n);
        v[0] += __uint_as_float(bb[0] << 16);
        v[1] += __uint_as_float(bb[0] & 0xffff0000u);
        v[2] += __uint_as_float(bb[1] << 16);
        v[3] += __uint_as_float(bb[1] & 0xffff0000u);
      }
      if (EPI == EPI_GELU) {
#pragma unroll
        for (int r = 0; r < 4; ++r) {
          const float x = v[r];
          const float u = 0.7978845608028654f * (x + 0.044715f * x * x * x);
          const float e = __builtin_amdgcn_exp2f(-2.885390081777927f * u);
          v[r] = x * __builtin_amdgcn_rcpf(1.0f + e);
        }
      }
      if (EPI == EPI_RES || EPI == EPI_BIAS_RES) {
        u32x2 rr = *(const u32x2*)(res + (size_t)m * N + n);
        v[0] += __uint_as_float(rr[0] << 16);
        v[1] += __uint_as_float(rr[0] & 0xffff0000u);
        v[2] += __uint_as_float(rr[1] << 16);
        v[3] += __uint_as_float(rr[1] & 0xffff0000u);
      }
      u32x2 o;
      o[0] = pk2(v[0], v[1]);
      o[1] = pk2(v[2], v[3]);
      *(u32x2*)(C + (size_t)m * N + n) = o;
    }
  }
}

__global__ __launch_bounds__(256) void attn_kernel(const u16* __restrict__ qkv, u16* __restrict__ o) {
  constexpr int QB = 64, NKEY = WIN + QB;
  __shared__ __attribute__((aligned(16))) char vs[NKEY * 64];
  const int tid = threadIdx.x, lane = tid & 63;
  const int wid = __builtin_amdgcn_readfirstlane(tid >> 6);
  const int head = blockIdx.x & 31, qb = blockIdx.x >> 5;
  const int q0 = qb * QB, kbase = q0 - WIN;
  const int LD = 3 * DM;
#pragma unroll
  for (int it = 0; it < NKEY * 4 / 256; ++it) {
    const int idx = it * 256 + tid, key = idx >> 2, ch = idx & 3;
    int kj = kbase + key;
    kj = kj < 0 ? 0 : kj;
    u32x4 v = *(const u32x4*)(qkv + (size_t)kj * LD + 2 * DM + head * DH + ch * 8);
    *(u32x4*)(vs + key * 64 + ((ch ^ (((key >> 2) & 1) << 1)) << 4)) = v;
  }
  const int fr = lane & 15, g = lane >> 4;
  const int qw = q0 + wid * 16;
  bf16x8 qf = *(const bf16x8*)(qkv + (size_t)(qw + fr) * LD + head * DH + g * 8);
  f32x4 s[17];
#pragma unroll
  for (int t = 0; t < 17; ++t) {
    int kj = qw - WIN + t * 16 + fr;
    kj = kj < 0 ? 0 : kj;
    bf16x8 kf = *(const bf16x8*)(qkv + (size_t)kj * LD + DM + head * DH + g * 8);
    s[t] = __builtin_amdgcn_mfma_f32_16x16x32_bf16(kf, qf, f32x4{0.f, 0.f, 0.f, 0.f}, 0, 0, 0);
  }
  const float NEG = -1e30f;
  float mx = NEG;
#pragma unroll
  for (int t = 0; t < 17; ++t)
#pragma unroll
    for (int r = 0; r < 4; ++r) {
      const int rel = t * 16 + 4 * g + r;
      const int d = WIN + fr - rel;
      const bool ok = (d >= 0) && (d < WIN) && (qw - WIN + rel >= 0);
      s[t][r] = ok ? s[t][r] : NEG;
      mx = fmaxf(mx, s[t][r]);
    }
  mx = fmaxf(mx, __shfl_xor(mx, 16, 64));
  mx = fmaxf(mx, __shfl_xor(mx, 32, 64));
  const float c = 0.17677669529663687f * 1.4426950408889634f;
  const float mc = mx * c;
  float sum = 0.f;
  unsigned pk[18][2];
#pragma unroll
  for (int t = 0; t < 17; ++t) {
    float p[4];
#pragma unroll
    for (int r = 0; r < 4; ++r) {
      p[r] = __builtin_amdgcn_exp2f(s[t][r] * c - mc);
      sum += p[r];
    }
    pk[t][0] = pk2(p[0], p[1]);
    pk[t][1] = pk2(p[2], p[3]);
  }
  pk[17][0] = 0u;
  pk[17][1] = 0u;
  sum += __shfl_xor(sum, 16, 64);
  sum += __shfl_xor(sum, 32, 64);
  __syncthreads();
  const int q4 = (lane >> 2) & 3, pp = lane & 3;
  f32x4 oacc[2] = {f32x4{0.f, 0.f, 0.f, 0.f}, f32x4{0.f, 0.f, 0.f, 0.f}};
#pragma unroll
  for (int tp = 0; tp < 9; ++tp) {
    const int t0 = 2 * tp, t1 = (2 * tp + 1 < 17) ? 2 * tp + 1 : 16;
    const int key0 = (wid + t0) * 16 + 4 * g + q4, key1 = (wid + t1) * 16 + 4 * g + q4;
    u32x4 pb = {pk[2 * tp][0], pk[2 * tp][1], pk[2 * tp + 1][0], pk[2 * tp + 1][1]};
    bf16x8 pf = __builtin_bit_cast(bf16x8, pb);
#pragma unroll
    for (int dh = 0; dh < 2; ++dh) {
      s16x4 lo = vtr((lds_cptr)(vs + key0 * 64 + ((dh ^ ((key0 >> 2) & 1)) << 5) + pp * 8));
      s16x4 hi = vtr((lds_cptr)(vs + key1 * 64 + ((dh ^ ((key1 >> 2) & 1)) << 5) + pp * 8));
      bf16x8 vf = __builtin_shufflevector(lo, hi, 0, 1, 2, 3, 4, 5, 6, 7);
      oacc[dh] = __builtin_amdgcn_mfma_f32_16x16x32_bf16(vf, pf, oacc[dh], 0, 0, 0);
    }
  }
  const float inv = 1.0f / sum;
#pragma unroll
  for (int dh = 0; dh < 2; ++dh) {
    u32x2 w;
    w[0] = pk2(oacc[dh][0] * inv, oacc[dh][1] * inv);
    w[1] = pk2(oacc[dh][2] * inv, oacc[dh][3] * inv);
    *(u32x2*)(o + (size_t)(qw + fr) * DM + head * DH + dh * 16 + 4 * g) = w;
  }
}

template <int BN, int NBUF, int EPI>
static void launch_gemm(const u16* A, const u16* W, u16* C, const u16* bias, const u16* res, int N, int K,
                        hipStream_t stream) {
  constexpr int lds = NBUF * (128 * 64 * 2 + 64 * BN * 2);
  (void)hipFuncSetAttribute((const void*)gemm_kernel<BN, NBUF, EPI>, hipFuncAttributeMaxDynamicSharedMemorySize, lds);
  gemm_kernel<BN, NBUF, EPI><<<(S_LEN / 128) * (N / BN), 256, lds, stream>>>(A, W, C, bias, res, N, K);
}

extern "C" void kernel_launch(void* const* d_in, const int* in_sizes, int n_in, void* d_out, int out_size,
                              void* d_ws, size_t ws_size, hipStream_t stream) {
  const u16* x = (const u16*)d_in[0];
  const u16* wqkv = (const u16*)d_in[1];
  const u16* wo = (const u16*)d_in[2];
  const u16* w1 = (const u16*)d_in[4];
  const u16* b1 = (const u16*)d_in[5];
  const u16* w2 = (const u16*)d_in[6];
  const u16* b2 = (const u16*)d_in[7];
  u16* out = (u16*)d_out;
  char* ws = (char*)d_ws;
  u16* xn = (u16*)(ws);
  u16* qkv = (u16*)(ws + (4u << 20));
  u16* o = (u16*)(ws + (16u << 20));
  u16* h = (u16*)(ws + (20u << 20));
  u16* y = (u16*)(ws + (24u << 20));
  u16* hh = (u16*)(ws + (28u << 20));

  ln_kernel<1><<<S_LEN / 4, 256, 0, stream>>>(x, xn);
  launch_gemm<192, 3, EPI_PLAIN>(xn, wqkv, qkv, nullptr, nullptr, 3 * DM, DM, stream);
  attn_kernel<<<NH * (S_LEN / 64), 256, 0, stream>>>(qkv, o);
  launch_gemm<64, 4, EPI_RES>(o, wo, h, nullptr, x, DM, DM, stream);
  ln_kernel<2><<<S_LEN / 4, 256, 0, stream>>>(h, y);
  launch_gemm<224, 3, EPI_GELU>(y, w1, hh, b1, nullptr, FF, DM, stream);
  launch_gemm<64, 4, EPI_BIAS_RES>(hh, w2, out, b2, h, DM, FF, stream);
}
```

```cpp
#include <hip/hip_runtime.h>
#include <hip/hip_bf16.h>
#include <stdint.h>

typedef unsigned short u16;
typedef __attribute__((ext_vector_type(8))) short bf16x8;
typedef __attribute__((ext_vector_type(4))) short s16x4;
typedef __attribute__((ext_vector_type(4))) float f32x4;
typedef __attribute__((ext_vector_type(2))) float f32x2;
typedef __attribute__((ext_vector_type(4))) unsigned u32x4;
typedef __attribute__((ext_vector_type(2))) unsigned u32x2;
typedef __attribute__((ext_vector_type(2))) __bf16 bf16x2_t;
typedef __attribute__((address_space(3))) const char* lds_cptr;

#define STAMP(kid, i)
constexpr int S_LEN = 2048, DM = 1024, NH = 32, DH = 32, WIN = 256, FF = 3584;
constexpr float LN_EPS = 1e-5f;

__device__ __forceinline__ float bf2f(u16 v) { return __uint_as_float(((unsigned)v) << 16); }
__device__ __forceinline__ float bflo(unsigned v) { return __uint_as_float(v << 16); }
__device__ __forceinline__ float bfhi(unsigned v) { return __uint_as_float(v & 0xffff0000u); }
__device__ __forceinline__ unsigned pk2(float lo, float hi) {
  return __builtin_bit_cast(unsigned, __builtin_convertvector(f32x2{lo, hi}, bf16x2_t));
}
__device__ __forceinline__ void glds16(const void* sbase, unsigned voff, unsigned lds_addr) {
  unsigned sv;
  asm volatile("s_mov_b32 %0, m0\n\ts_mov_b32 m0, %3\n\ts_nop 0\n\tglobal_load_lds_dwordx4 %1, %2\n\ts_mov_b32 m0, %0"
               : "=&s"(sv) : "v"(voff), "s"(sbase), "s"(lds_addr) : "memory");
}
__device__ __forceinline__ s16x4 vtr(lds_cptr p) {
  return __builtin_bit_cast(s16x4, __builtin_amdgcn_ds_read_tr16_b64_v4i16((__attribute__((address_space(3))) s16x4*)p));
}
template <int CTRL>
__device__ __forceinline__ float dpp_add(float v) {
  return v + __int_as_float(__builtin_amdgcn_update_dpp(0, __float_as_int(v), CTRL, 0xf, 0xf, false));
}
__device__ __forceinline__ float sum8(float v) {
  v = dpp_add<0xB1>(v);
  v = dpp_add<0x4E>(v);
  return dpp_add<0x141>(v);
}
__device__ __forceinline__ float xsum16_32(float m) {
  auto a = __builtin_amdgcn_permlane32_swap(__float_as_uint(m), __float_as_uint(m), false, false);
  m = __uint_as_float(a[0]) + __uint_as_float(a[1]);
  auto b = __builtin_amdgcn_permlane16_swap(__float_as_uint(m), __float_as_uint(m), false, false);
  return __uint_as_float(b[0]) + __uint_as_float(b[1]);
}
__device__ __forceinline__ float xmax16_32(float m) {
  auto a = __builtin_amdgcn_permlane32_swap(__float_as_uint(m), __float_as_uint(m), false, false);
  m = fmaxf(__uint_as_float(a[0]), __uint_as_float(a[1]));
  auto b = __builtin_amdgcn_permlane16_swap(__float_as_uint(m), __float_as_uint(m), false, false);
  return fmaxf(__uint_as_float(b[0]), __uint_as_float(b[1]));
}
__device__ __forceinline__ float wave_sum(float v) {
  v = sum8(v);
  v = dpp_add<0x140>(v);
  return xsum16_32(v);
}

__global__ __launch_bounds__(256) void prep_kernel(const u16* __restrict__ in, u16* __restrict__ out,
                                                   const char* __restrict__ wqkv, unsigned* __restrict__ flags) {
  const int lane = threadIdx.x & 63;
  const int row = blockIdx.x * 4 + (threadIdx.x >> 6);
  const u32x4* src = (const u32x4*)(in + (size_t)row * DM);
  u32x4 a = src[lane], b = src[lane + 64];
  const int gi = blockIdx.x * 256 + threadIdx.x;
  unsigned w0 = 0;
  if (gi < 49152) w0 = *(const unsigned*)(wqkv + (size_t)gi * 128);
  if (gi < 1024) flags[gi] = 0u;
  float v[16];
#pragma unroll
  for (int i = 0; i < 4; ++i) {
    v[2 * i] = bflo(a[i]);
    v[2 * i + 1] = bfhi(a[i]);
    v[8 + 2 * i] = bflo(b[i]);
    v[8 + 2 * i + 1] = bfhi(b[i]);
  }
  float s = 0.f;
#pragma unroll
  for (int i = 0; i < 16; ++i) s += v[i];
  const float mean = wave_sum(s) * (1.0f / DM);
  float q = 0.f;
#pragma unroll
  for (int i = 0; i < 16; ++i) { v[i] -= mean; q += v[i] * v[i]; }
  const float var = wave_sum(q) * (1.0f / DM);
  const float r = 1.0f / sqrtf(var + LN_EPS);
  u32x4 oa, ob;
#pragma unroll
  for (int i = 0; i < 4; ++i) {
    oa[i] = pk2(v[2 * i] * r, v[2 * i + 1] * r);
    ob[i] = pk2(v[8 + 2 * i] * r, v[8 + 2 * i + 1] * r);
  }
  u32x4* dst = (u32x4*)(out + (size_t)row * DM);
  dst[lane] = oa;
  dst[lane + 64] = ob;
  asm volatile("" ::"v"(w0));
}

enum { EPI_QKV = 0, EPI_RES_STATS = 1, EPI_LN_GELU = 2, EPI_SPLIT_BIAS_RES = 3 };
constexpr int NLW = 4;

template <int CNT, int NBUF, int NPA, int STAGE, bool CS>
__device__ __forceinline__ void loader_loop(const char* Ab, const char* Wb, const unsigned* voff, int lw, int nk,
                                            unsigned wstep, unsigned lds0, const char* smem, float* csacc) {
  const int lane = threadIdx.x & 63;
  int ca0 = 0, ca1 = 0, cstep = 0;
  if (CS) {
    if (lw < 3) {
      const int k = lane >> 3, c = lane & 7;
      ca0 = NPA * 1024 + lw * 8192 + k * 128 + ((c ^ (k & 2)) << 4);
      ca1 = NPA * 1024 + lw * 8192 + (k + 8) * 128 + ((c ^ ((k & 2) | 4)) << 4);
      cstep = 2048;
    } else {
      const int k = lane >> 2, c = lane & 3;
      ca0 = NPA * 1024 + 3 * 8192 + k * 64 + ((c ^ ((k >> 2) & 2)) << 4);
      ca1 = ca0 + 1024;
      cstep = 2048;
    }
  }
  auto colsum = [&](int buf) {
    const char* base = smem + buf * STAGE;
    const int n = (lw < 3) ? 4 : 2;
#pragma unroll
    for (int i = 0; i < 4; ++i) {
      if (i < n) {
        const u32x4 u = *(const u32x4*)(base + ca0 + i * cstep);
        const u32x4 v = *(const u32x4*)(base + ca1 + i * cstep);
#pragma unroll
        for (int e = 0; e < 4; ++e) {
          csacc[2 * e] += bflo(u[e]) + bflo(v[e]);
          csacc[2 * e + 1] += bfhi(u[e]) + bfhi(v[e]);
        }
      }
    }
  };
  const char* sp[CNT];
  unsigned st[CNT];
#pragma unroll
  for (int i = 0; i < CNT; ++i) {
    const bool isA = (lw + i * NLW) < NPA;
    sp[i] = isA ? Ab : Wb;
    st[i] = isA ? 128u : wstep;
  }
  auto issue = [&](int buf) {
#pragma unroll
    for (int i = 0; i < CNT; ++i) {
      glds16(sp[i], voff[i], lds0 + buf * STAGE + (lw + i * NLW) * 1024);
      sp[i] += st[i];
    }
  };
  issue(0);
  issue(1);
  asm volatile("s_waitcnt vmcnt(%0)" ::"i"(CNT) : "memory");
  __builtin_amdgcn_s_barrier();
#pragma unroll
  for (int s = 2; s < NBUF; ++s) issue(s);
  if (CS) colsum(0);
  int buf = 0;
  for (int t = 0; t < nk; ++t) {
    if (t + NBUF <= nk) {
      asm volatile("s_waitcnt vmcnt(%0)" ::"i"((NBUF - 2) * CNT) : "memory");
    } else {
      asm volatile("s_waitcnt vmcnt(0)" ::: "memory");
    }
    __builtin_amdgcn_s_barrier();
    if (t + NBUF < nk) issue(buf);
    buf = (buf + 1 == NBUF) ? 0 : buf + 1;
    if (CS && t + 1 < nk) colsum(buf);
  }
}

template <int BN, int NBUF, int EPI, int XA>
__global__ __launch_bounds__(512, 1) void gemm_kernel(const u16* __restrict__ A, const u16* __restrict__ W,
                                                     u16* __restrict__ C, const u16* __restrict__ bias,
                                                     const u16* __restrict__ res, float* __restrict__ aux0,
                                                     float* __restrict__ aux1, unsigned* __restrict__ flags,
                                                     int N, int K) {
  constexpr int BM = 128;
  constexpr bool WIDE = BN > 64;
  constexpr bool SPLIT = (EPI == EPI_SPLIT_BIAS_RES);
  constexpr int NTHR = 512;
  constexpr int NG = BN / 64;
  constexpr bool HALF = (BN % 64) != 0;
  constexpr int NT = WIDE ? BN / 32 : 4;
  constexpr int ABYTES = BM * 128, WBYTES = 64 * BN * 2, STAGE = ABYTES + WBYTES;
  constexpr int NPA = ABYTES / 1024, NP = STAGE / 1024;
  constexpr int CMAX = (NP + NLW - 1) / NLW;
  constexpr int SIDE = NBUF * STAGE;
  extern __shared__ __attribute__((aligned(16))) char smem[];

  const int tid = threadIdx.x, lane = tid & 63;
  const int wid = __builtin_amdgcn_readfirstlane(tid >> 6);
  STAMP(EPI, 0);
  const int xc = blockIdx.x & 7, rr = blockIdx.x >> 3;
  int bm, bn, hk = 0;
  if (SPLIT) {
    hk = rr & 1;
    const int tt = rr >> 1;
    bm = (xc & 3) * 4 + (tt & 3);
    bn = (xc >> 2) * 4 + (tt >> 2);
  } else {
    constexpr int XB = 32 / XA, PM = 16 / XA;
    bm = (xc % PM) * XA + (rr % XA);
    bn = (xc / PM) * XB + (rr / XA);
  }
  const int m0 = bm * BM, n0 = bn * BN;
  const int nk = SPLIT ? K / 128 : K / 64;
  const int kbeg = hk * (K / 2);
  const int odd = HALF ? (bn & 1) : 0;
  const int cfull = odd ? 32 : 0, chalf = odd ? 0 : NG * 64;
  const unsigned lds0 = (unsigned)(uintptr_t)(__attribute__((address_space(3))) char*)smem;
  const int fr = lane & 15, g = lane >> 4;

  f32x4 acc[4][NT];
  const int wm = wid & 1, wx = (wid >> 1) & 1;
  int tcol[NT];

  if (wid >= 4) {
    const int lw = wid - 4;
    unsigned voff[CMAX];
#pragma unroll
    for (int i = 0; i < CMAX; ++i) {
      const int idx = lw + i * NLW;
      if (idx < NPA) {
        const int row = idx * 8 + (lane >> 3), ch = (lane & 7) ^ (row & 7);
        voff[i] = (unsigned)(((m0 + row) * K + ch * 8) * 2);
      } else if (idx < NPA + NG * 8) {
        const int w = idx - NPA, G = w >> 3, k = (w & 7) * 8 + (lane >> 3);
        const int ch = (lane & 7) ^ ((k & 2) | ((k >> 1) & 4));
        voff[i] = (unsigned)((k * N + n0 + cfull + G * 64 + ch * 8) * 2);
      } else {
        const int w = idx - NPA - NG * 8, k = w * 16 + (lane >> 2);
        const int ch = (lane & 3) ^ ((k >> 2) & 2);
        voff[i] = (unsigned)((k * N + n0 + chalf + ch * 8) * 2);
      }
    }
    const unsigned wstep = (unsigned)(64 * N * 2);
    const char* Ab = (const char*)(A + kbeg);
    const char* Wb = (const char*)(W + (size_t)kbeg * N);
    constexpr bool CS = false;
    float csacc[8];
#pragma unroll
    for (int e = 0; e < 8; ++e) csacc[e] = 0.f;
    if (lw + (CMAX - 1) * NLW < NP)
      loader_loop<CMAX, NBUF, NPA, STAGE, CS>(Ab, Wb, voff, lw, nk, wstep, lds0, smem, csacc);
    else
      loader_loop<CMAX - 1, NBUF, NPA, STAGE, CS>(Ab, Wb, voff, lw, nk, wstep, lds0, smem, csacc);
    if (CS) {
#pragma unroll
      for (int e = 0; e < 8; ++e) {
        float t = csacc[e];
        const float t4 = dpp_add<0x124>(t);
        t = (lw == 3) ? t4 : t;
        t = dpp_add<0x128>(t);
        csacc[e] = xsum16_32(t);
      }
      const int nl = (lw < 3) ? 8 : 4;
      if (lane < nl) {
        const int col = ((lw < 3) ? cfull + lw * 64 : chalf) + lane * 8;
#pragma unroll
        for (int e = 0; e < 8; ++e) *(float*)(smem + SIDE + 1024 + (col + e) * 8) = csacc[e];
      }
    }
  } else {
    if (EPI == EPI_LN_GELU) {
      if (tid < 128) {
        const f32x4* sp = (const f32x4*)(aux0 + (size_t)(m0 + tid) * 32);
        f32x4 p[8];
#pragma unroll
        for (int i = 0; i < 8; ++i) p[i] = sp[i];
        float mean = 0.f;
#pragma unroll
        for (int i = 0; i < 8; ++i) mean += p[i][0] + p[i][2];
        mean *= (1.0f / 16.0f);
        float m2 = 0.f;
#pragma unroll
        for (int i = 0; i < 8; ++i) {
          const float d0 = p[i][0] - mean, d1 = p[i][2] - mean;
          m2 += p[i][1] + p[i][3] + 64.0f * (d0 * d0 + d1 * d1);
        }
        const float var = m2 * (1.0f / DM);
        const float r1 = 1.0f / sqrtf(var + LN_EPS);
        const float r2 = 1.0f / sqrtf(var * r1 * r1 + LN_EPS);
        *(f32x2*)(smem + SIDE + tid * 8) = f32x2{mean, r1 * r2};
      } else if (tid - 128 < BN / 2) {
        const int c = (tid - 128) * 2;
        f32x2 cs = {0.f, 0.f};
#pragma unroll
        for (int kc = 0; kc < 32; ++kc) cs += *(const f32x2*)(aux1 + (size_t)kc * FF + n0 + c);
        const unsigned bb = *(const unsigned*)(bias + n0 + c);
        *(f32x4*)(smem + SIDE + 1024 + c * 8) = f32x4{cs[0], bflo(bb), cs[1], bfhi(bb)};
      }
    }
    const int q = (lane >> 2) & 3, pp = lane & 3;
    const int wk = WIDE ? 0 : wx;
    int aoff[2];
#pragma unroll
    for (int ks = 0; ks < 2; ++ks) aoff[ks] = (wm * 64 + fr) * 128 + (((ks * 4 + g) ^ (fr & 7)) << 4);
    int tb[NT];
    const int f1 = (q & 2) | ((g & 1) << 2);
#pragma unroll
    for (int j = 0; j < NT; ++j) {
      int G, tp;
      if (!WIDE) { G = 0; tp = j; }
      else if (j < 4) { G = wx * (NG - 1); tp = j; }
      else { G = 1; tp = wx * 2 + (j - 4); }
      if (WIDE && HALF && j == NT - 1) {
        tb[j] = ABYTES + NG * 8192 + (8 * g + q) * 64 + (((2 * wx + (pp >> 1)) ^ ((g & 1) << 1)) << 4) + (pp & 1) * 8;
        tcol[j] = chalf + wx * 16;
      } else {
        tb[j] = ABYTES + G * 8192 + (8 * g + q) * 128 + (((2 * tp + (pp >> 1)) ^ f1) << 4) + (pp & 1) * 8;
        tcol[j] = cfull + G * 64 + tp * 16;
      }
    }
#pragma unroll
    for (int i = 0; i < 4; ++i)
#pragma unroll
      for (int j = 0; j < NT; ++j) acc[i][j] = f32x4{0.f, 0.f, 0.f, 0.f};

    bf16x8 xa[4], wa[NT], xb[4], wb[NT];
    auto rd = [&](bf16x8* xf, bf16x8* wf, int buf, int ks) {
      const char* base = smem + buf * STAGE;
#pragma unroll
      for (int j = 0; j < NT; ++j) {
        const int rs = (WIDE && HALF && j == NT - 1) ? 64 : 128;
        s16x4 lo = vtr((lds_cptr)(base + tb[j] + ks * 32 * rs));
        s16x4 hi = vtr((lds_cptr)(base + tb[j] + ks * 32 * rs + 4 * rs));
        wf[j] = __builtin_shufflevector(lo, hi, 0, 1, 2, 3, 4, 5, 6, 7);
      }
#pragma unroll
      for (int i = 0; i < 4; ++i) xf[i] = *(const bf16x8*)(base + aoff[ks] + i * 2048);
    };
    auto mm = [&](const bf16x8* xf, const bf16x8* wf) {
#pragma unroll
      for (int i = 0; i < 4; ++i)
#pragma unroll
        for (int j = 0; j < NT; ++j)
          acc[i][j] = __builtin_amdgcn_mfma_f32_16x16x32_bf16(wf[j], xf[i], acc[i][j], 0, 0, 0);
    };
#define INTERLEAVE() do { _Pragma("unroll") for (int z_ = 0; z_ < 2 * NT + 4; ++z_) { __builtin_amdgcn_sched_group_barrier(0x8, 1, 0); __builtin_amdgcn_sched_group_barrier(0x100, 1, 0); } __builtin_amdgcn_sched_group_barrier(0x8, 4 * NT - (2 * NT + 4), 0); } while (0)
    __builtin_amdgcn_s_waitcnt(0xC07F);
    __builtin_amdgcn_s_barrier();
    STAMP(EPI, 1);
    if (WIDE) {
      rd(xa, wa, 0, 0);
      int b0 = 0;
      for (int t = 0; t < nk; ++t) {
        rd(xb, wb, b0, 1);
        b0 = (b0 + 1 == NBUF) ? 0 : b0 + 1;
        mm(xa, wa);
        INTERLEAVE();
        __builtin_amdgcn_sched_barrier(0);
        __builtin_amdgcn_s_waitcnt(0xC07F);
        __builtin_amdgcn_s_barrier();
        rd(xa, wa, b0, 0);
        mm(xb, wb);
        INTERLEAVE();
        __builtin_amdgcn_sched_barrier(0);
      }
    } else {
      rd(xa, wa, 0, wk);
      int b1 = 1;
      for (int t = 0; t < nk; t += 2) {
        __builtin_amdgcn_s_waitcnt(0xC07F);
        __builtin_amdgcn_s_barrier();
        rd(xb, wb, b1, wk);
        b1 = (b1 + 1 == NBUF) ? 0 : b1 + 1;
        mm(xa, wa);
        INTERLEAVE();
        __builtin_amdgcn_sched_barrier(0);
        __builtin_amdgcn_s_waitcnt(0xC07F);
        __builtin_amdgcn_s_barrier();
        rd(xa, wa, b1, wk);
        b1 = (b1 + 1 == NBUF) ? 0 : b1 + 1;
        mm(xb, wb);
        INTERLEAVE();
        __builtin_amdgcn_sched_barrier(0);
      }
    }
    __builtin_amdgcn_s_waitcnt(0xC07F);
    STAMP(EPI, 2);
    if (SPLIT) {
      const int tile = bm * 8 + bn;
      const auto rsrc = __builtin_amdgcn_make_buffer_rsrc(aux0, 0, 128 * 2 * 2 * 16384, 0x00020000);
      if (wm != hk) {
        const int slot = ((tile * 2 + hk) * 2 + wx);
#pragma unroll
        for (int i = 0; i < 4; ++i)
#pragma unroll
          for (int j = 0; j < NT; ++j)
            __builtin_amdgcn_raw_buffer_store_b128(__builtin_bit_cast(u32x4, acc[i][j]), rsrc,
                                                   slot * 16384 + (i * NT + j) * 1024 + lane * 16, 0, 16);
        asm volatile("s_waitcnt vmcnt(0)" ::: "memory");
        if (lane == 0) __hip_atomic_store(flags + slot, 1u, __ATOMIC_RELAXED, __HIP_MEMORY_SCOPE_AGENT);
      } else {
        const int slot = ((tile * 2 + (hk ^ 1)) * 2 + wx);
        for (int spin = 0; spin < (1 << 24); ++spin) {
          if (__hip_atomic_load(flags + slot, __ATOMIC_RELAXED, __HIP_MEMORY_SCOPE_AGENT) != 0u) break;
          __builtin_amdgcn_s_sleep(2);
        }
        asm volatile("" ::: "memory");
#pragma unroll
        for (int i = 0; i < 4; ++i) {
          u32x4 t[NT];
#pragma unroll
          for (int j = 0; j < NT; ++j)
            t[j] = __builtin_amdgcn_raw_buffer_load_b128(rsrc, slot * 16384 + (i * NT + j) * 1024 + lane * 16, 0, 16);
#pragma unroll
          for (int j = 0; j < NT; ++j) acc[i][j] += __builtin_bit_cast(f32x4, t[j]);
        }
      }
    }
  }
  __syncthreads();
  STAMP(EPI, 3);

  constexpr int CPR = BN / 8;
  if (EPI == EPI_QKV) {
    constexpr int RS = BN * 2 + 16;
    if (wid < 4) {
#pragma unroll
      for (int j = 0; j < NT; ++j)
#pragma unroll
        for (int i = 0; i < 4; ++i) {
          u32x2 o;
          o[0] = pk2(acc[i][j][0], acc[i][j][1]);
          o[1] = pk2(acc[i][j][2], acc[i][j][3]);
          *(u32x2*)(smem + (wm * 64 + i * 16 + fr) * RS + (tcol[j] + g * 4) * 2) = o;
        }
    }
    __syncthreads();
#pragma unroll
    for (int c = tid; c < BM * CPR; c += NTHR) {
      const int hs = c >> 9, row = (c >> 2) & 127, c4 = c & 3;
      u32x4 v = *(const u32x4*)(smem + row * RS + (hs * 4 + c4) * 16);
      *(u32x4*)(C + ((size_t)((n0 >> 5) + hs) * S_LEN + m0 + row) * 32 + c4 * 8) = v;
    }
  } else if (EPI == EPI_LN_GELU) {
    constexpr int RS = BN * 4 + 16;
    if (wid < 4) {
#pragma unroll
      for (int j = 0; j < NT; ++j)
#pragma unroll
        for (int i = 0; i < 4; ++i)
          *(f32x4*)(smem + (wm * 64 + i * 16 + fr) * RS + (tcol[j] + g * 4) * 4) = acc[i][j];
    }
    __syncthreads();
#pragma unroll
    for (int c = tid; c < BM * CPR; c += NTHR) {
      const int row = c / CPR, ch = c % CPR;
      const f32x4 a0 = *(const f32x4*)(smem + row * RS + ch * 32);
      const f32x4 a1 = *(const f32x4*)(smem + row * RS + ch * 32 + 16);
      const f32x2 rs = *(const f32x2*)(smem + SIDE + row * 8);
      float v[8];
#pragma unroll
      for (int r = 0; r < 4; ++r) {
        const f32x4 cb = *(const f32x4*)(smem + SIDE + 1024 + ch * 64 + r * 16);
        const float x0 = (r < 2) ? a0[2 * r] : a1[2 * r - 4];
        const float x1 = (r < 2) ? a0[2 * r + 1] : a1[2 * r - 3];
        v[2 * r] = (x0 - rs[0] * cb[0]) * rs[1] + cb[1];
        v[2 * r + 1] = (x1 - rs[0] * cb[2]) * rs[1] + cb[3];
      }
      u32x4 o;
#pragma unroll
      for (int r = 0; r < 4; ++r) {
        const f32x2 x = {v[2 * r], v[2 * r + 1]};
        const f32x2 x2 = x * x;
        const f32x2 w = x2 * f32x2{-0.10294324f, -0.10294324f} + f32x2{-2.302208198f, -2.302208198f};
        const f32x2 z = x * w;
        f32x2 e = {__builtin_amdgcn_exp2f(z[0]), __builtin_amdgcn_exp2f(z[1])};
        e = e + f32x2{1.0f, 1.0f};
        const f32x2 y = x * f32x2{__builtin_amdgcn_rcpf(e[0]), __builtin_amdgcn_rcpf(e[1])};
        o[r] = pk2(y[0], y[1]);
      }
      *(u32x4*)(C + (size_t)(m0 + row) * N + n0 + ch * 8) = o;
    }
  } else if (EPI == EPI_RES_STATS) {
    constexpr int RS = BN * 4 + 16;
    constexpr int PART = BM * RS;
    if (wid < 4) {
#pragma unroll
      for (int i = 0; i < 4; ++i)
#pragma unroll
        for (int j = 0; j < NT; ++j)
          *(f32x4*)(smem + wx * PART + (wm * 64 + i * 16 + fr) * RS + (j * 16 + g * 4) * 4) = acc[i][j];
    }
    __syncthreads();
#pragma unroll
    for (int c = tid; c < BM * CPR; c += NTHR) {
      const int row = c / CPR, ch = c % CPR;
      const f32x4 a0 = *(const f32x4*)(smem + row * RS + ch * 32);
      const f32x4 a1 = *(const f32x4*)(smem + row * RS + ch * 32 + 16);
      const f32x4 b0 = *(const f32x4*)(smem + PART + row * RS + ch * 32);
      const f32x4 b1 = *(const f32x4*)(smem + PART + row * RS + ch * 32 + 16);
      const u32x4 rv = *(const u32x4*)(res + (size_t)(m0 + row) * N + n0 + ch * 8);
      u32x4 o;
      o[0] = pk2(a0[0] + b0[0] + bflo(rv[0]), a0[1] + b0[1] + bfhi(rv[0]));
      o[1] = pk2(a0[2] + b0[2] + bflo(rv[1]), a0[3] + b0[3] + bfhi(rv[1]));
      o[2] = pk2(a1[0] + b1[0] + bflo(rv[2]), a1[1] + b1[1] + bfhi(rv[2]));
      o[3] = pk2(a1[2] + b1[2] + bflo(rv[3]), a1[3] + b1[3] + bfhi(rv[3]));
      *(u32x4*)(C + (size_t)(m0 + row) * N + n0 + ch * 8) = o;
      float s = 0.f;
#pragma unroll
      for (int r = 0; r < 4; ++r) s += bflo(o[r]) + bfhi(o[r]);
      const float mean = sum8(s) * (1.0f / 64.0f);
      float m2 = 0.f;
#pragma unroll
      for (int r = 0; r < 4; ++r) {
        const float d0 = bflo(o[r]) - mean, d1 = bfhi(o[r]) - mean;
        m2 += d0 * d0 + d1 * d1;
      }
      m2 = sum8(m2);
      if (ch == 0) *(f32x2*)(aux0 + ((size_t)(m0 + row) * 16 + bn) * 2) = f32x2{mean, m2};
    }
  } else {
    constexpr int RS = BN * 4 + 16;
    if (wid < 4 && wm == hk) {
#pragma unroll
      for (int i = 0; i < 4; ++i)
#pragma unroll
        for (int j = 0; j < NT; ++j)
          *(f32x4*)(smem + (i * 16 + fr) * RS + (tcol[j] + g * 4) * 4) = acc[i][j];
    }
    __syncthreads();
#pragma unroll
    for (int c = tid; c < 64 * CPR; c += NTHR) {
      const int row = c / CPR, ch = c % CPR;
      const f32x4 a0 = *(const f32x4*)(smem + row * RS + ch * 32);
      const f32x4 a1 = *(const f32x4*)(smem + row * RS + ch * 32 + 16);
      const u32x4 bb = *(const u32x4*)(bias + n0 + ch * 8);
      const size_t go = (size_t)(m0 + hk * 64 + row) * N + n0 + ch * 8;
      const u32x4 rv = *(const u32x4*)(res + go);
      u32x4 o;
      o[0] = pk2(a0[0] + bflo(bb[0]) + bflo(rv[0]), a0[1] + bfhi(bb[0]) + bfhi(rv[0]));
      o[1] = pk2(a0[2] + bflo(bb[1]) + bflo(rv[1]), a0[3] + bfhi(bb[1]) + bfhi(rv[1]));
      o[2] = pk2(a1[0] + bflo(bb[2]) + bflo(rv[2]), a1[1] + bfhi(bb[2]) + bfhi(rv[2]));
      o[3] = pk2(a1[2] + bflo(bb[3]) + bflo(rv[3]), a1[3] + bfhi(bb[3]) + bfhi(rv[3]));
      *(u32x4*)(C + go) = o;
    }
  }
}

template <bool EARLY>
__device__ __forceinline__ void attn_body(const char* ks, const char* vs, bf16x8 qf, int wid, int lane, int tmin,
                                          u16* __restrict__ op) {
  const int fr = lane & 15, g = lane >> 4;
  const float NEG = -1e30f;
  f32x4 s[17];
  const char* kp = ks + (wid * 16 + fr) * 64 + ((g ^ ((4 - ((fr >> 2) & 3)) & 3)) << 4);
#pragma unroll
  for (int t = 0; t < 17; ++t) {
    bf16x8 kf = *(const bf16x8*)(kp + t * 1024);
    s[t] = __builtin_amdgcn_mfma_f32_16x16x32_bf16(kf, qf, f32x4{0.f, 0.f, 0.f, 0.f}, 0, 0, 0);
  }
#pragma unroll
  for (int r = 0; r < 4; ++r) {
    s[0][r] = (4 * g + r > fr) ? s[0][r] : NEG;
    s[16][r] = (4 * g + r <= fr) ? s[16][r] : NEG;
  }
  if (EARLY) {
#pragma unroll
    for (int t = 0; t < 16; ++t)
      if (t < tmin) s[t] = f32x4{NEG, NEG, NEG, NEG};
  }
  float mx = NEG;
#pragma unroll
  for (int t = 0; t < 17; ++t) mx = fmaxf(fmaxf(mx, fmaxf(s[t][0], s[t][1])), fmaxf(s[t][2], s[t][3]));
  mx = xmax16_32(mx);
  const float c = 0.17677669529663687f * 1.4426950408889634f;
  const float nmc = -mx * c;
  float sum = 0.f;
  unsigned pk[18][2];
#pragma unroll
  for (int t = 0; t < 17; ++t) {
    float p[4];
#pragma unroll
    for (int r = 0; r < 4; ++r) {
      p[r] = __builtin_amdgcn_exp2f(__builtin_fmaf(s[t][r], c, nmc));
      sum += p[r];
    }
    pk[t][0] = pk2(p[0], p[1]);
    pk[t][1] = pk2(p[2], p[3]);
  }
  pk[17][0] = 0u;
  pk[17][1] = 0u;
  sum = xsum16_32(sum);
  const int q4 = (lane >> 2) & 3, pp = lane & 3;
  const char* vp0 = vs + (wid * 16 + 4 * g + q4) * 64 + ((g & 1) << 5) + pp * 8;
  const char* vp1 = vs + (wid * 16 + 4 * g + q4) * 64 + (((g & 1) ^ 1) << 5) + pp * 8;
  f32x4 o0 = {0.f, 0.f, 0.f, 0.f}, o1 = {0.f, 0.f, 0.f, 0.f};
#pragma unroll
  for (int tp = 0; tp < 9; ++tp) {
    const int t0 = 2 * tp, t1 = (2 * tp + 1 < 17) ? 2 * tp + 1 : 16;
    u32x4 pb = {pk[2 * tp][0], pk[2 * tp][1], pk[2 * tp + 1][0], pk[2 * tp + 1][1]};
    bf16x8 pf = __builtin_bit_cast(bf16x8, pb);
    s16x4 a0 = vtr((lds_cptr)(vp0 + t0 * 1024)), a1 = vtr((lds_cptr)(vp0 + t1 * 1024));
    s16x4 b0 = vtr((lds_cptr)(vp1 + t0 * 1024)), b1 = vtr((lds_cptr)(vp1 + t1 * 1024));
    o0 = __builtin_amdgcn_mfma_f32_16x16x32_bf16(__builtin_shufflevector(a0, a1, 0, 1, 2, 3, 4, 5, 6, 7), pf, o0, 0, 0, 0);
    o1 = __builtin_amdgcn_mfma_f32_16x16x32_bf16(__builtin_shufflevector(b0, b1, 0, 1, 2, 3, 4, 5, 6, 7), pf, o1, 0, 0, 0);
  }
  const float inv = 1.0f / sum;
  u32x2 w0, w1;
  w0[0] = pk2(o0[0] * inv, o0[1] * inv);
  w0[1] = pk2(o0[2] * inv, o0[3] * inv);
  w1[0] = pk2(o1[0] * inv, o1[1] * inv);
  w1[1] = pk2(o1[2] * inv, o1[3] * inv);
  *(u32x2*)(op + 4 * g) = w0;
  *(u32x2*)(op + 16 + 4 * g) = w1;
}

__global__ __launch_bounds__(512) void attn_kernel(const u16* __restrict__ qkv, u16* __restrict__ o,
                                                   const char* __restrict__ wo, const u16* __restrict__ w1,
                                                   const char* __restrict__ w2, float* __restrict__ cspart) {
  constexpr int QB = 128, NKEY = WIN + QB;
  __shared__ __attribute__((aligned(16))) char smem[2 * NKEY * 64];
  const int tid = threadIdx.x, lane = tid & 63;
  const int wid = __builtin_amdgcn_readfirstlane(tid >> 6);
  const int bid = blockIdx.x;
  const int head = bid & 31, qb = bid >> 5;
  const int q0 = qb * QB, kbase = q0 - WIN;
  const u16* qh = qkv + (size_t)head * S_LEN * 32;
  const u16* kh = qkv + (size_t)(NH + head) * S_LEN * 32;
  const u16* vh = qkv + (size_t)(2 * NH + head) * S_LEN * 32;
  const unsigned lds0 = (unsigned)(uintptr_t)(__attribute__((address_space(3))) char*)smem;
#pragma unroll
  for (int i = 0; i < 3; ++i) {
    const int piece = wid * 3 + i;
    const int key = piece * 16 + (lane >> 2), slot = lane & 3;
    int kj = kbase + key;
    kj = kj < 0 ? 0 : kj;
    const int ck = slot ^ ((4 - ((key >> 2) & 3)) & 3);
    const int cv = slot ^ (((key >> 2) & 1) << 1);
    glds16(kh, (unsigned)((kj * 32 + ck * 8) * 2), lds0 + piece * 1024);
    glds16(vh, (unsigned)((kj * 32 + cv * 8) * 2), lds0 + NKEY * 64 + piece * 1024);
  }
  const int fr = lane & 15, g = lane >> 4;
  const int qw = q0 + wid * 16;
  bf16x8 qf = *(const bf16x8*)(qh + (size_t)(qw + fr) * 32 + g * 8);
  __builtin_amdgcn_s_waitcnt(0x0F70);
  asm volatile("" ::: "memory");
  unsigned touch = 0;
  if (wid < 3) {
    const int line = (bid * 3 + wid) * 64 + lane;
    if (line < 73728) {
      const char* p = line < 16384 ? wo + (size_t)line * 128 : w2 + (size_t)(line - 16384) * 128;
      asm volatile("global_load_dword %0, %1, off" : "+v"(touch) : "v"(p) : "memory");
    }
  }
  const int cb = bid < 448 ? bid : 447;
  const int cg = cb % 14, kc = cb / 14;
  const u16* cp = w1 + (size_t)(kc * 32 + (tid >> 5) * 2) * FF + cg * 256 + (tid & 31) * 8;
  const u32x4 cw0 = *(const u32x4*)cp;
  const u32x4 cw1 = *(const u32x4*)(cp + FF);
  __builtin_amdgcn_s_barrier();
  u16* op = o + (size_t)(qw + fr) * DM + head * DH;
  if (q0 < WIN) {
    const int tmin = (WIN - qw) >> 4;
    attn_body<true>(smem, smem + NKEY * 64, qf, wid, lane, tmin, op);
  } else {
    attn_body<false>(smem, smem + NKEY * 64, qf, wid, lane, 0, op);
  }
  if (bid < 448) {
    __syncthreads();
    float* red = (float*)smem;
    f32x4 lo, hi;
#pragma unroll
    for (int e = 0; e < 2; ++e) {
      lo[2 * e] = bflo(cw0[e]) + bflo(cw1[e]);
      lo[2 * e + 1] = bfhi(cw0[e]) + bfhi(cw1[e]);
      hi[2 * e] = bflo(cw0[2 + e]) + bflo(cw1[2 + e]);
      hi[2 * e + 1] = bfhi(cw0[2 + e]) + bfhi(cw1[2 + e]);
    }
    *(f32x4*)(red + (tid >> 5) * 256 + (tid & 31) * 8) = lo;
    *(f32x4*)(red + (tid >> 5) * 256 + (tid & 31) * 8 + 4) = hi;
    __syncthreads();
    if (tid < 256) {
      float t = 0.f;
#pragma unroll
      for (int i = 0; i < 16; ++i) t += red[i * 256 + tid];
      cspart[(size_t)kc * FF + cg * 256 + tid] = t;
    }
  }
  asm volatile("s_waitcnt vmcnt(0)" : "+v"(touch)::"memory");
}

template <int BN, int NBUF, int EPI, int XA>
static void launch_gemm(const u16* A, const u16* W, u16* C, const u16* bias, const u16* res, float* aux0,
                        float* aux1, unsigned* flags, int N, int K, hipStream_t stream) {
  constexpr int lds = NBUF * (128 * 64 * 2 + 64 * BN * 2) + (EPI == EPI_LN_GELU ? 1024 + BN * 8 : 0);
  auto kern = gemm_kernel<BN, NBUF, EPI, XA>;
  (void)hipFuncSetAttribute((const void*)kern, hipFuncAttributeMaxDynamicSharedMemorySize, lds);
  kern<<<256, 512, lds, stream>>>(A, W, C, bias, res, aux0, aux1, flags, N, K);
}

extern "C" void kernel_launch(void* const* d_in, const int* in_sizes, int n_in, void* d_out, int out_size,
                              void* d_ws, size_t ws_size, hipStream_t stream) {
  const u16* x = (const u16*)d_in[0];
  const u16* wqkv = (const u16*)d_in[1];
  const u16* wo = (const u16*)d_in[2];
  const u16* w1 = (const u16*)d_in[4];
  const u16* b1 = (const u16*)d_in[5];
  const u16* w2 = (const u16*)d_in[6];
  const u16* b2 = (const u16*)d_in[7];
  u16* out = (u16*)d_out;
  char* ws = (char*)d_ws;
  u16* xn = (u16*)(ws);
  u16* qkv = (u16*)(ws + (4u << 20));
  u16* o = (u16*)(ws + (16u << 20));
  u16* h = (u16*)(ws + (20u << 20));
  u16* hh = (u16*)(ws + (28u << 20));
  float* stats = (float*)(ws + (44u << 20));
  float* cspart = (float*)(ws + (45u << 20));
  unsigned* flags = (unsigned*)(ws + (46u << 20));
  float* xchg = (float*)(ws + (48u << 20));

  prep_kernel<<<S_LEN / 4, 256, 0, stream>>>(x, xn, (const char*)wqkv, flags);
  launch_gemm<192, 4, EPI_QKV, 8>(xn, wqkv, qkv, nullptr, nullptr, nullptr, nullptr, nullptr, 3 * DM, DM, stream);
  attn_kernel<<<NH * (S_LEN / 128), 512, 0, stream>>>(qkv, o, (const char*)wo, w1, (const char*)w2, cspart);
  launch_gemm<64, 6, EPI_RES_STATS, 4>(o, wo, h, nullptr, x, stats, nullptr, nullptr, DM, DM, stream);
  launch_gemm<224, 3, EPI_LN_GELU, 8>(h, w1, hh, b1, nullptr, stats, cspart, nullptr, FF, DM, stream);
  launch_gemm<128, 4, EPI_SPLIT_BIAS_RES, 4>(hh, w2, out, b2, h, xchg, nullptr, flags, DM, FF, stream);
}
```

```cpp
#include <hip/hip_runtime.h>
#include <hip/hip_bf16.h>
#include <stdint.h>

typedef unsigned short u16;
typedef __attribute__((ext_vector_type(8))) short bf16x8;
typedef __attribute__((ext_vector_type(4))) short s16x4;
typedef __attribute__((ext_vector_type(4))) float f32x4;
typedef __attribute__((ext_vector_type(2))) float f32x2;
typedef __attribute__((ext_vector_type(4))) unsigned u32x4;
typedef __attribute__((ext_vector_type(2))) unsigned u32x2;
typedef __attribute__((ext_vector_type(2))) __bf16 bf16x2_t;
typedef __attribute__((address_space(3))) const char* lds_cptr;

#define STAMP(kid, i)
__device__ __forceinline__ void st16_sc1(void* p, __attribute__((ext_vector_type(4))) unsigned v) { asm volatile("global_store_dwordx4 %0, %1, off sc1\n\ts_nop 1" :: "v"(p), "v"(v) : "memory"); }
#define ST16(p, v) st16_sc1((void*)(p), (v))
constexpr int S_LEN = 2048, DM = 1024, NH = 32, DH = 32, WIN = 256, FF = 3584;
constexpr float LN_EPS = 1e-5f;

__device__ __forceinline__ float bf2f(u16 v) { return __uint_as_float(((unsigned)v) << 16); }
__device__ __forceinline__ float bflo(unsigned v) { return __uint_as_float(v << 16); }
__device__ __forceinline__ float bfhi(unsigned v) { return __uint_as_float(v & 0xffff0000u); }
__device__ __forceinline__ unsigned pk2(float lo, float hi) {
  return __builtin_bit_cast(unsigned, __builtin_convertvector(f32x2{lo, hi}, bf16x2_t));
}
__device__ __forceinline__ void glds16(const void* sbase, unsigned voff, unsigned lds_addr) {
  unsigned sv;
  asm volatile("s_mov_b32 %0, m0\n\ts_mov_b32 m0, %3\n\ts_nop 0\n\tglobal_load_lds_dwordx4 %1, %2\n\ts_mov_b32 m0, %0"
               : "=&s"(sv) : "v"(voff), "s"(sbase), "s"(lds_addr) : "memory");
}
__device__ __forceinline__ s16x4 vtr(lds_cptr p) {
  return __builtin_bit_cast(s16x4, __builtin_amdgcn_ds_read_tr16_b64_v4i16((__attribute__((address_space(3))) s16x4*)p));
}
template <int CTRL>
__device__ __forceinline__ float dpp_add(float v) {
  return v + __int_as_float(__builtin_amdgcn_update_dpp(0, __float_as_int(v), CTRL, 0xf, 0xf, false));
}
__device__ __forceinline__ float sum8(float v) {
  v = dpp_add<0xB1>(v);
  v = dpp_add<0x4E>(v);
  return dpp_add<0x141>(v);
}
__device__ __forceinline__ float xsum16_32(float m) {
  auto a = __builtin_amdgcn_permlane32_swap(__float_as_uint(m), __float_as_uint(m), false, false);
  m = __uint_as_float(a[0]) + __uint_as_float(a[1]);
  auto b = __builtin_amdgcn_permlane16_swap(__float_as_uint(m), __float_as_uint(m), false, false);
  return __uint_as_float(b[0]) + __uint_as_float(b[1]);
}
__device__ __forceinline__ float xmax16_32(float m) {
  auto a = __builtin_amdgcn_permlane32_swap(__float_as_uint(m), __float_as_uint(m), false, false);
  m = fmaxf(__uint_as_float(a[0]), __uint_as_float(a[1]));
  auto b = __builtin_amdgcn_permlane16_swap(__float_as_uint(m), __float_as_uint(m), false, false);
  return fmaxf(__uint_as_float(b[0]), __uint_as_float(b[1]));
}
__device__ __forceinline__ float wave_sum(float v) {
  v = sum8(v);
  v = dpp_add<0x140>(v);
  return xsum16_32(v);
}

__global__ __launch_bounds__(256) void prep_kernel(const u16* __restrict__ in, u16* __restrict__ out,
                                                   const char* __restrict__ wqkv, unsigned* __restrict__ flags) {
  const int lane = threadIdx.x & 63;
  const int row = blockIdx.x * 4 + (threadIdx.x >> 6);
  const u32x4* src = (const u32x4*)(in + (size_t)row * DM);
  u32x4 a = src[lane], b = src[lane + 64];
  const int gi = blockIdx.x * 256 + threadIdx.x;
  unsigned w0 = 0;
  if (gi < 49152) w0 = *(const unsigned*)(wqkv + (size_t)gi * 128);
  if (gi < 1024) flags[gi] = 0u;
  float v[16];
#pragma unroll
  for (int i = 0; i < 4; ++i) {
    v[2 * i] = bflo(a[i]);
    v[2 * i + 1] = bfhi(a[i]);
    v[8 + 2 * i] = bflo(b[i]);
    v[8 + 2 * i + 1] = bfhi(b[i]);
  }
  float s = 0.f;
#pragma unroll
  for (int i = 0; i < 16; ++i) s += v[i];
  const float mean = wave_sum(s) * (1.0f / DM);
  float q = 0.f;
#pragma unroll
  for (int i = 0; i < 16; ++i) { v[i] -= mean; q += v[i] * v[i]; }
  const float var = wave_sum(q) * (1.0f / DM);
  const float r = 1.0f / sqrtf(var + LN_EPS);
  u32x4 oa, ob;
#pragma unroll
  for (int i = 0; i < 4; ++i) {
    oa[i] = pk2(v[2 * i] * r, v[2 * i + 1] * r);
    ob[i] = pk2(v[8 + 2 * i] * r, v[8 + 2 * i + 1] * r);
  }
  u32x4* dst = (u32x4*)(out + (size_t)row * DM);
  dst[lane] = oa;
  dst[lane + 64] = ob;
  asm volatile("" ::"v"(w0));
}

enum { EPI_QKV = 0, EPI_RES_STATS = 1, EPI_LN_GELU = 2, EPI_SPLIT_BIAS_RES = 3 };
constexpr int NLW = 4;

template <int CNT, int NBUF, int NPA, int STAGE, bool CS>
__device__ __forceinline__ void loader_loop(const char* Ab, const char* Wb, const unsigned* voff, int lw, int nk,
                                            unsigned wstep, unsigned lds0, const char* smem, float* csacc) {
  const int lane = threadIdx.x & 63;
  int ca0 = 0, ca1 = 0, cstep = 0;
  if (CS) {
    if (lw < 3) {
      const int k = lane >> 3, c = lane & 7;
      ca0 = NPA * 1024 + lw * 8192 + k * 128 + ((c ^ (k & 2)) << 4);
      ca1 = NPA * 1024 + lw * 8192 + (k + 8) * 128 + ((c ^ ((k & 2) | 4)) << 4);
      cstep = 2048;
    } else {
      const int k = lane >> 2, c = lane & 3;
      ca0 = NPA * 1024 + 3 * 8192 + k * 64 + ((c ^ ((k >> 2) & 2)) << 4);
      ca1 = ca0 + 1024;
      cstep = 2048;
    }
  }
  auto colsum = [&](int buf) {
    const char* base = smem + buf * STAGE;
    const int n = (lw < 3) ? 4 : 2;
#pragma unroll
    for (int i = 0; i < 4; ++i) {
      if (i < n) {
        const u32x4 u = *(const u32x4*)(base + ca0 + i * cstep);
        const u32x4 v = *(const u32x4*)(base + ca1 + i * cstep);
#pragma unroll
        for (int e = 0; e < 4; ++e) {
          csacc[2 * e] += bflo(u[e]) + bflo(v[e]);
          csacc[2 * e + 1] += bfhi(u[e]) + bfhi(v[e]);
        }
      }
    }
  };
  const char* sp[CNT];
  unsigned st[CNT];
#pragma unroll
  for (int i = 0; i < CNT; ++i) {
    const bool isA = (lw + i * NLW) < NPA;
    sp[i] = isA ? Ab : Wb;
    st[i] = isA ? 128u : wstep;
  }
  auto issue = [&](int buf) {
#pragma unroll
    for (int i = 0; i < CNT; ++i) {
      glds16(sp[i], voff[i], lds0 + buf * STAGE + (lw + i * NLW) * 1024);
      sp[i] += st[i];
    }
  };
  issue(0);
  issue(1);
  asm volatile("s_waitcnt vmcnt(%0)" ::"i"(CNT) : "memory");
  __builtin_amdgcn_s_barrier();
#pragma unroll
  for (int s = 2; s < NBUF; ++s) issue(s);
  if (CS) colsum(0);
  int buf = 0;
  for (int t = 0; t < nk; ++t) {
    if (t + NBUF <= nk) {
      asm volatile("s_waitcnt vmcnt(%0)" ::"i"((NBUF - 2) * CNT) : "memory");
    } else {
      asm volatile("s_waitcnt vmcnt(0)" ::: "memory");
    }
    __builtin_amdgcn_s_barrier();
    if (t + NBUF < nk) issue(buf);
    buf = (buf + 1 == NBUF) ? 0 : buf + 1;
    if (CS && t + 1 < nk) colsum(buf);
  }
}

template <int BN, int NBUF, int EPI, int XA>
__global__ __launch_bounds__(512, 1) void gemm_kernel(const u16* __restrict__ A, const u16* __restrict__ W,
                                                     u16* __restrict__ C, const u16* __restrict__ bias,
                                                     const u16* __restrict__ res, float* __restrict__ aux0,
                                                     float* __restrict__ aux1, unsigned* __restrict__ flags,
                                                     int N, int K) {
  constexpr int BM = 128;
  constexpr bool WIDE = BN > 64;
  constexpr bool SPLIT = (EPI == EPI_SPLIT_BIAS_RES);
  constexpr int NTHR = 512;
  constexpr int NG = BN / 64;
  constexpr bool HALF = (BN % 64) != 0;
  constexpr int NT = WIDE ? BN / 32 : 4;
  constexpr int ABYTES = BM * 128, WBYTES = 64 * BN * 2, STAGE = ABYTES + WBYTES;
  constexpr int NPA = ABYTES / 1024, NP = STAGE / 1024;
  constexpr int CMAX = (NP + NLW - 1) / NLW;
  constexpr int SIDE = NBUF * STAGE;
  extern __shared__ __attribute__((aligned(16))) char smem[];

  const int tid = threadIdx.x, lane = tid & 63;
  const int wid = __builtin_amdgcn_readfirstlane(tid >> 6);
  STAMP(EPI, 0);
  const int xc = blockIdx.x & 7, rr = blockIdx.x >> 3;
  int bm, bn, hk = 0;
  if (SPLIT) {
    hk = rr & 1;
    const int tt = rr >> 1;
    bm = (xc & 3) * 4 + (tt & 3);
    bn = (xc >> 2) * 4 + (tt >> 2);
  } else {
    constexpr int XB = 32 / XA, PM = 16 / XA;
    bm = (xc % PM) * XA + (rr % XA);
    bn = (xc / PM) * XB + (rr / XA);
  }
  const int m0 = bm * BM, n0 = bn * BN;
  const int nk = SPLIT ? K / 128 : K / 64;
  const int kbeg = hk * (K / 2);
  const int odd = HALF ? (bn & 1) : 0;
  const int cfull = odd ? 32 : 0, chalf = odd ? 0 : NG * 64;
  const unsigned lds0 = (unsigned)(uintptr_t)(__attribute__((address_space(3))) char*)smem;
  const int fr = lane & 15, g = lane >> 4;

  f32x4 acc[4][NT];
  const int wm = wid & 1, wx = (wid >> 1) & 1;
  int tcol[NT];

  if (wid >= 4) {
    const int lw = wid - 4;
    unsigned voff[CMAX];
#pragma unroll
    for (int i = 0; i < CMAX; ++i) {
      const int idx = lw + i * NLW;
      if (idx < NPA) {
        const int row = idx * 8 + (lane >> 3), ch = (lane & 7) ^ (row & 7);
        voff[i] = (unsigned)(((m0 + row) * K + ch * 8) * 2);
      } else if (idx < NPA + NG * 8) {
        const int w = idx - NPA, G = w >> 3, k = (w & 7) * 8 + (lane >> 3);
        const int ch = (lane & 7) ^ ((k & 2) | ((k >> 1) & 4));
        voff[i] = (unsigned)((k * N + n0 + cfull + G * 64 + ch * 8) * 2);
      } else {
        const int w = idx - NPA - NG * 8, k = w * 16 + (lane >> 2);
        const int ch = (lane & 3) ^ ((k >> 2) & 2);
        voff[i] = (unsigned)((k * N + n0 + chalf + ch * 8) * 2);
      }
    }
    const unsigned wstep = (unsigned)(64 * N * 2);
    const char* Ab = (const char*)(A + kbeg);
    const char* Wb = (const char*)(W + (size_t)kbeg * N);
    constexpr bool CS = false;
    float csacc[8];
#pragma unroll
    for (int e = 0; e < 8; ++e) csacc[e] = 0.f;
    if (lw + (CMAX - 1) * NLW < NP)
      loader_loop<CMAX, NBUF, NPA, STAGE, CS>(Ab, Wb, voff, lw, nk, wstep, lds0, smem, csacc);
    else
      loader_loop<CMAX - 1, NBUF, NPA, STAGE, CS>(Ab, Wb, voff, lw, nk, wstep, lds0, smem, csacc);
    if (CS) {
#pragma unroll
      for (int e = 0; e < 8; ++e) {
        float t = csacc[e];
        const float t4 = dpp_add<0x124>(t);
        t = (lw == 3) ? t4 : t;
        t = dpp_add<0x128>(t);
        csacc[e] = xsum16_32(t);
      }
      const int nl = (lw < 3) ? 8 : 4;
      if (lane < nl) {
        const int col = ((lw < 3) ? cfull + lw * 64 : chalf) + lane * 8;
#pragma unroll
        for (int e = 0; e < 8; ++e) *(float*)(smem + SIDE + 1024 + (col + e) * 8) = csacc[e];
      }
    }
  } else {
    if (EPI == EPI_LN_GELU) {
      if (tid < 128) {
        const f32x4* sp = (const f32x4*)(aux0 + (size_t)(m0 + tid) * 32);
        f32x4 p[8];
#pragma unroll
        for (int i = 0; i < 8; ++i) p[i] = sp[i];
        float mean = 0.f;
#pragma unroll
        for (int i = 0; i < 8; ++i) mean += p[i][0] + p[i][2];
        mean *= (1.0f / 16.0f);
        float m2 = 0.f;
#pragma unroll
        for (int i = 0; i < 8; ++i) {
          const float d0 = p[i][0] - mean, d1 = p[i][2] - mean;
          m2 += p[i][1] + p[i][3] + 64.0f * (d0 * d0 + d1 * d1);
        }
        const float var = m2 * (1.0f / DM);
        const float r1 = 1.0f / sqrtf(var + LN_EPS);
        const float r2 = 1.0f / sqrtf(var * r1 * r1 + LN_EPS);
        *(f32x2*)(smem + SIDE + tid * 8) = f32x2{mean, r1 * r2};
      } else if (tid - 128 < BN / 2) {
        const int c = (tid - 128) * 2;
        f32x2 cs = {0.f, 0.f};
#pragma unroll
        for (int kc = 0; kc < 32; ++kc) cs += *(const f32x2*)(aux1 + (size_t)kc * FF + n0 + c);
        const unsigned bb = *(const unsigned*)(bias + n0 + c);
        *(f32x4*)(smem + SIDE + 1024 + c * 8) = f32x4{cs[0], bflo(bb), cs[1], bfhi(bb)};
      }
    }
    const int q = (lane >> 2) & 3, pp = lane & 3;
    const int wk = WIDE ? 0 : wx;
    int aoff[2];
#pragma unroll
    for (int ks = 0; ks < 2; ++ks) aoff[ks] = (wm * 64 + fr) * 128 + (((ks * 4 + g) ^ (fr & 7)) << 4);
    int tb[NT];
    const int f1 = (q & 2) | ((g & 1) << 2);
#pragma unroll
    for (int j = 0; j < NT; ++j) {
      int G, tp;
      if (!WIDE) { G = 0; tp = j; }
      else if (j < 4) { G = wx * (NG - 1); tp = j; }
      else { G = 1; tp = wx * 2 + (j - 4); }
      if (WIDE && HALF && j == NT - 1) {
        tb[j] = ABYTES + NG * 8192 + (8 * g + q) * 64 + (((2 * wx + (pp >> 1)) ^ ((g & 1) << 1)) << 4) + (pp & 1) * 8;
        tcol[j] = chalf + wx * 16;
      } else {
        tb[j] = ABYTES + G * 8192 + (8 * g + q) * 128 + (((2 * tp + (pp >> 1)) ^ f1) << 4) + (pp & 1) * 8;
        tcol[j] = cfull + G * 64 + tp * 16;
      }
    }
#pragma unroll
    for (int i = 0; i < 4; ++i)
#pragma unroll
      for (int j = 0; j < NT; ++j) acc[i][j] = f32x4{0.f, 0.f, 0.f, 0.f};

    bf16x8 xa[4], wa[NT], xb[4], wb[NT];
    auto rd = [&](bf16x8* xf, bf16x8* wf, int buf, int ks) {
      const char* base = smem + buf * STAGE;
#pragma unroll
      for (int j = 0; j < NT; ++j) {
        const int rs = (WIDE && HALF && j == NT - 1) ? 64 : 128;
        s16x4 lo = vtr((lds_cptr)(base + tb[j] + ks * 32 * rs));
        s16x4 hi = vtr((lds_cptr)(base + tb[j] + ks * 32 * rs + 4 * rs));
        wf[j] = __builtin_shufflevector(lo, hi, 0, 1, 2, 3, 4, 5, 6, 7);
      }
#pragma unroll
      for (int i = 0; i < 4; ++i) xf[i] = *(const bf16x8*)(base + aoff[ks] + i * 2048);
    };
    auto mm = [&](const bf16x8* xf, const bf16x8* wf) {
#pragma unroll
      for (int i = 0; i < 4; ++i)
#pragma unroll
        for (int j = 0; j < NT; ++j)
          acc[i][j] = __builtin_amdgcn_mfma_f32_16x16x32_bf16(wf[j], xf[i], acc[i][j], 0, 0, 0);
    };
#define INTERLEAVE() do { _Pragma("unroll") for (int z_ = 0; z_ < 2 * NT + 4; ++z_) { __builtin_amdgcn_sched_group_barrier(0x8, 1, 0); __builtin_amdgcn_sched_group_barrier(0x100, 1, 0); } __builtin_amdgcn_sched_group_barrier(0x8, 4 * NT - (2 * NT + 4), 0); } while (0)
    __builtin_amdgcn_s_waitcnt(0xC07F);
    __builtin_amdgcn_s_barrier();
    STAMP(EPI, 1);
    if (WIDE) {
      rd(xa, wa, 0, 0);
      int b0 = 0;
      for (int t = 0; t < nk; ++t) {
        rd(xb, wb, b0, 1);
        b0 = (b0 + 1 == NBUF) ? 0 : b0 + 1;
        mm(xa, wa);
        INTERLEAVE();
        __builtin_amdgcn_sched_barrier(0);
        __builtin_amdgcn_s_waitcnt(0xC07F);
        __builtin_amdgcn_s_barrier();
        rd(xa, wa, b0, 0);
        mm(xb, wb);
        INTERLEAVE();
        __builtin_amdgcn_sched_barrier(0);
      }
    } else {
      rd(xa, wa, 0, wk);
      int b1 = 1;
      for (int t = 0; t < nk; t += 2) {
        __builtin_amdgcn_s_waitcnt(0xC07F);
        __builtin_amdgcn_s_barrier();
        rd(xb, wb, b1, wk);
        b1 = (b1 + 1 == NBUF) ? 0 : b1 + 1;
        mm(xa, wa);
        INTERLEAVE();
        __builtin_amdgcn_sched_barrier(0);
        __builtin_amdgcn_s_waitcnt(0xC07F);
        __builtin_amdgcn_s_barrier();
        rd(xa, wa, b1, wk);
        b1 = (b1 + 1 == NBUF) ? 0 : b1 + 1;
        mm(xb, wb);
        INTERLEAVE();
        __builtin_amdgcn_sched_barrier(0);
      }
    }
    __builtin_amdgcn_s_waitcnt(0xC07F);
    STAMP(EPI, 2);
    if (SPLIT) {
      const int tile = bm * 8 + bn;
      const auto rsrc = __builtin_amdgcn_make_buffer_rsrc(aux0, 0, 128 * 2 * 2 * 16384, 0x00020000);
      if (wm != hk) {
        const int slot = ((tile * 2 + hk) * 2 + wx);
#pragma unroll
        for (int i = 0; i < 4; ++i)
#pragma unroll
          for (int j = 0; j < NT; ++j)
            __builtin_amdgcn_raw_buffer_store_b128(__builtin_bit_cast(u32x4, acc[i][j]), rsrc,
                                                   slot * 16384 + (i * NT + j) * 1024 + lane * 16, 0, 16);
        asm volatile("s_waitcnt vmcnt(0)" ::: "memory");
        if (lane == 0) __hip_atomic_store(flags + slot, 1u, __ATOMIC_RELAXED, __HIP_MEMORY_SCOPE_AGENT);
      } else {
        const int slot = ((tile * 2 + (hk ^ 1)) * 2 + wx);
        for (int spin = 0; spin < (1 << 24); ++spin) {
          if (__hip_atomic_load(flags + slot, __ATOMIC_RELAXED, __HIP_MEMORY_SCOPE_AGENT) != 0u) break;
          __builtin_amdgcn_s_sleep(2);
        }
        asm volatile("" ::: "memory");
#pragma unroll
        for (int i = 0; i < 4; ++i) {
          u32x4 t[NT];
#pragma unroll
          for (int j = 0; j < NT; ++j)
            t[j] = __builtin_amdgcn_raw_buffer_load_b128(rsrc, slot * 16384 + (i * NT + j) * 1024 + lane * 16, 0, 16);
#pragma unroll
          for (int j = 0; j < NT; ++j) acc[i][j] += __builtin_bit_cast(f32x4, t[j]);
        }
      }
    }
  }
  __syncthreads();
  STAMP(EPI, 3);

  constexpr int CPR = BN / 8;
  if (EPI == EPI_QKV) {
    constexpr int RS = BN * 2 + 16;
    if (wid < 4) {
#pragma unroll
      for (int j = 0; j < NT; ++j)
#pragma unroll
        for (int i = 0; i < 4; ++i) {
          u32x2 o;
          o[0] = pk2(acc[i][j][0], acc[i][j][1]);
          o[1] = pk2(acc[i][j][2], acc[i][j][3]);
          *(u32x2*)(smem + (wm * 64 + i * 16 + fr) * RS + (tcol[j] + g * 4) * 2) = o;
        }
    }
    __syncthreads();
#pragma unroll
    for (int c = tid; c < BM * CPR; c += NTHR) {
      const int hs = c >> 9, row = (c >> 2) & 127, c4 = c & 3;
      u32x4 v = *(const u32x4*)(smem + row * RS + (hs * 4 + c4) * 16);
      ST16(C + ((size_t)((n0 >> 5) + hs) * S_LEN + m0 + row) * 32 + c4 * 8, v);
    }
  } else if (EPI == EPI_LN_GELU) {
    constexpr int RS = BN * 4 + 16;
    if (wid < 4) {
#pragma unroll
      for (int j = 0; j < NT; ++j)
#pragma unroll
        for (int i = 0; i < 4; ++i)
          *(f32x4*)(smem + (wm * 64 + i * 16 + fr) * RS + (tcol[j] + g * 4) * 4) = acc[i][j];
    }
    __syncthreads();
#pragma unroll
    for (int c = tid; c < BM * CPR; c += NTHR) {
      const int row = c / CPR, ch = c % CPR;
      const f32x4 a0 = *(const f32x4*)(smem + row * RS + ch * 32);
      const f32x4 a1 = *(const f32x4*)(smem + row * RS + ch * 32 + 16);
      const f32x2 rs = *(const f32x2*)(smem + SIDE + row * 8);
      float v[8];
#pragma unroll
      for (int r = 0; r < 4; ++r) {
        const f32x4 cb = *(const f32x4*)(smem + SIDE + 1024 + ch * 64 + r * 16);
        const float x0 = (r < 2) ? a0[2 * r] : a1[2 * r - 4];
        const float x1 = (r < 2) ? a0[2 * r + 1] : a1[2 * r - 3];
        v[2 * r] = (x0 - rs[0] * cb[0]) * rs[1] + cb[1];
        v[2 * r + 1] = (x1 - rs[0] * cb[2]) * rs[1] + cb[3];
      }
      u32x4 o;
#pragma unroll
      for (int r = 0; r < 4; ++r) {
        const f32x2 x = {v[2 * r], v[2 * r + 1]};
        const f32x2 x2 = x * x;
        const f32x2 w = x2 * f32x2{-0.10294324f, -0.10294324f} + f32x2{-2.302208198f, -2.302208198f};
        const f32x2 z = x * w;
        f32x2 e = {__builtin_amdgcn_exp2f(z[0]), __builtin_amdgcn_exp2f(z[1])};
        e = e + f32x2{1.0f, 1.0f};
        const f32x2 y = x * f32x2{__builtin_amdgcn_rcpf(e[0]), __builtin_amdgcn_rcpf(e[1])};
        o[r] = pk2(y[0], y[1]);
      }
      ST16(C + (size_t)(m0 + row) * N + n0 + ch * 8, o);
    }
  } else if (EPI == EPI_RES_STATS) {
    constexpr int RS = BN * 4 + 16;
    constexpr int PART = BM * RS;
    if (wid < 4) {
#pragma unroll
      for (int i = 0; i < 4; ++i)
#pragma unroll
        for (int j = 0; j < NT; ++j)
          *(f32x4*)(smem + wx * PART + (wm * 64 + i * 16 + fr) * RS + (j * 16 + g * 4) * 4) = acc[i][j];
    }
    __syncthreads();
#pragma unroll
    for (int c = tid; c < BM * CPR; c += NTHR) {
      const int row = c / CPR, ch = c % CPR;
      const f32x4 a0 = *(const f32x4*)(smem + row * RS + ch * 32);
      const f32x4 a1 = *(const f32x4*)(smem + row * RS + ch * 32 + 16);
      const f32x4 b0 = *(const f32x4*)(smem + PART + row * RS + ch * 32);
      const f32x4 b1 = *(const f32x4*)(smem + PART + row * RS + ch * 32 + 16);
      const u32x4 rv = *(const u32x4*)(res + (size_t)(m0 + row) * N + n0 + ch * 8);
      u32x4 o;
      o[0] = pk2(a0[0] + b0[0] + bflo(rv[0]), a0[1] + b0[1] + bfhi(rv[0]));
      o[1] = pk2(a0[2] + b0[2] + bflo(rv[1]), a0[3] + b0[3] + bfhi(rv[1]));
      o[2] = pk2(a1[0] + b1[0] + bflo(rv[2]), a1[1] + b1[1] + bfhi(rv[2]));
      o[3] = pk2(a1[2] + b1[2] + bflo(rv[3]), a1[3] + b1[3] + bfhi(rv[3]));
      ST16(C + (size_t)(m0 + row) * N + n0 + ch * 8, o);
      float s = 0.f;
#pragma unroll
      for (int r = 0; r < 4; ++r) s += bflo(o[r]) + bfhi(o[r]);
      const float mean = sum8(s) * (1.0f / 64.0f);
      float m2 = 0.f;
#pragma unroll
      for (int r = 0; r < 4; ++r) {
        const float d0 = bflo(o[r]) - mean, d1 = bfhi(o[r]) - mean;
        m2 += d0 * d0 + d1 * d1;
      }
      m2 = sum8(m2);
      if (ch == 0) *(f32x2*)(aux0 + ((size_t)(m0 + row) * 16 + bn) * 2) = f32x2{mean, m2};
    }
  } else {
    constexpr int RS = BN * 4 + 16;
    if (wid < 4 && wm == hk) {
#pragma unroll
      for (int i = 0; i < 4; ++i)
#pragma unroll
        for (int j = 0; j < NT; ++j)
          *(f32x4*)(smem + (i * 16 + fr) * RS + (tcol[j] + g * 4) * 4) = acc[i][j];
    }
    __syncthreads();
#pragma unroll
    for (int c = tid; c < 64 * CPR; c += NTHR) {
      const int row = c / CPR, ch = c % CPR;
      const f32x4 a0 = *(const f32x4*)(smem + row * RS + ch * 32);
      const f32x4 a1 = *(const f32x4*)(smem + row * RS + ch * 32 + 16);
      const u32x4 bb = *(const u32x4*)(bias + n0 + ch * 8);
      const size_t go = (size_t)(m0 + hk * 64 + row) * N + n0 + ch * 8;
      const u32x4 rv = *(const u32x4*)(res + go);
      u32x4 o;
      o[0] = pk2(a0[0] + bflo(bb[0]) + bflo(rv[0]), a0[1] + bfhi(bb[0]) + bfhi(rv[0]));
      o[1] = pk2(a0[2] + bflo(bb[1]) + bflo(rv[1]), a0[3] + bfhi(bb[1]) + bfhi(rv[1]));
      o[2] = pk2(a1[0] + bflo(bb[2]) + bflo(rv[2]), a1[1] + bfhi(bb[2]) + bfhi(rv[2]));
      o[3] = pk2(a1[2] + bflo(bb[3]) + bflo(rv[3]), a1[3] + bfhi(bb[3]) + bfhi(rv[3]));
      ST16(C + go, o);
    }
  }
}

template <bool EARLY>
__device__ __forceinline__ void attn_body(const char* ks, const char* vs, bf16x8 qf, int wid, int lane, int tmin,
                                          u16* __restrict__ op) {
  const int fr = lane & 15, g = lane >> 4;
  const float NEG = -1e30f;
  f32x4 s[17];
  const char* kp = ks + (wid * 16 + fr) * 64 + ((g ^ ((4 - ((fr >> 2) & 3)) & 3)) << 4);
#pragma unroll
  for (int t = 0; t < 17; ++t) {
    bf16x8 kf = *(const bf16x8*)(kp + t * 1024);
    s[t] = __builtin_amdgcn_mfma_f32_16x16x32_bf16(kf, qf, f32x4{0.f, 0.f, 0.f, 0.f}, 0, 0, 0);
  }
#pragma unroll
  for (int r = 0; r < 4; ++r) {
    s[0][r] = (4 * g + r > fr) ? s[0][r] : NEG;
    s[16][r] = (4 * g + r <= fr) ? s[16][r] : NEG;
  }
  if (EARLY) {
#pragma unroll
    for (int t = 0; t < 16; ++t)
      if (t < tmin) s[t] = f32x4{NEG, NEG, NEG, NEG};
  }
  float mx = NEG;
#pragma unroll
  for (int t = 0; t < 17; ++t) mx = fmaxf(fmaxf(mx, fmaxf(s[t][0], s[t][1])), fmaxf(s[t][2], s[t][3]));
  mx = xmax16_32(mx);
  const float c = 0.17677669529663687f * 1.4426950408889634f;
  const float nmc = -mx * c;
  float sum = 0.f;
  unsigned pk[18][2];
#pragma unroll
  for (int t = 0; t < 17; ++t) {
    float p[4];
#pragma unroll
    for (int r = 0; r < 4; ++r) {
      p[r] = __builtin_amdgcn_exp2f(__builtin_fmaf(s[t][r], c, nmc));
      sum += p[r];
    }
    pk[t][0] = pk2(p[0], p[1]);
    pk[t][1] = pk2(p[2], p[3]);
  }
  pk[17][0] = 0u;
  pk[17][1] = 0u;
  sum = xsum16_32(sum);
  const int q4 = (lane >> 2) & 3, pp = lane & 3;
  const char* vp0 = vs + (wid * 16 + 4 * g + q4) * 64 + ((g & 1) << 5) + pp * 8;
  const char* vp1 = vs + (wid * 16 + 4 * g + q4) * 64 + (((g & 1) ^ 1) << 5) + pp * 8;
  f32x4 o0 = {0.f, 0.f, 0.f, 0.f}, o1 = {0.f, 0.f, 0.f, 0.f};
#pragma unroll
  for (int tp = 0; tp < 9; ++tp) {
    const int t0 = 2 * tp, t1 = (2 * tp + 1 < 17) ? 2 * tp + 1 : 16;
    u32x4 pb = {pk[2 * tp][0], pk[2 * tp][1], pk[2 * tp + 1][0], pk[2 * tp + 1][1]};
    bf16x8 pf = __builtin_bit_cast(bf16x8, pb);
    s16x4 a0 = vtr((lds_cptr)(vp0 + t0 * 1024)), a1 = vtr((lds_cptr)(vp0 + t1 * 1024));
    s16x4 b0 = vtr((lds_cptr)(vp1 + t0 * 1024)), b1 = vtr((lds_cptr)(vp1 + t1 * 1024));
    o0 = __builtin_amdgcn_mfma_f32_16x16x32_bf16(__builtin_shufflevector(a0, a1, 0, 1, 2, 3, 4, 5, 6, 7), pf, o0, 0, 0, 0);
    o1 = __builtin_amdgcn_mfma_f32_16x16x32_bf16(__builtin_shufflevector(b0, b1, 0, 1, 2, 3, 4, 5, 6, 7), pf, o1, 0, 0, 0);
  }
  const float inv = 1.0f / sum;
  u32x2 w0, w1;
  w0[0] = pk2(o0[0] * inv, o0[1] * inv);
  w0[1] = pk2(o0[2] * inv, o0[3] * inv);
  w1[0] = pk2(o1[0] * inv, o1[1] * inv);
  w1[1] = pk2(o1[2] * inv, o1[3] * inv);
  *(u32x2*)(op + 4 * g) = w0;
  *(u32x2*)(op + 16 + 4 * g) = w1;
}

__global__ __launch_bounds__(512) void attn_kernel(const u16* __restrict__ qkv, u16* __restrict__ o,
                                                   const char* __restrict__ wo, const u16* __restrict__ w1,
                                                   const char* __restrict__ w2, float* __restrict__ cspart) {
  constexpr int QB = 128, NKEY = WIN + QB;
  __shared__ __attribute__((aligned(16))) char smem[2 * NKEY * 64];
  const int tid = threadIdx.x, lane = tid & 63;
  const int wid = __builtin_amdgcn_readfirstlane(tid >> 6);
  const int bid = blockIdx.x;
  const int head = bid & 31, qb = bid >> 5;
  const int q0 = qb * QB, kbase = q0 - WIN;
  const u16* qh = qkv + (size_t)head * S_LEN * 32;
  const u16* kh = qkv + (size_t)(NH + head) * S_LEN * 32;
  const u16* vh = qkv + (size_t)(2 * NH + head) * S_LEN * 32;
  const unsigned lds0 = (unsigned)(uintptr_t)(__attribute__((address_space(3))) char*)smem;
#pragma unroll
  for (int i = 0; i < 3; ++i) {
    const int piece = wid * 3 + i;
    const int key = piece * 16 + (lane >> 2), slot = lane & 3;
    int kj = kbase + key;
    kj = kj < 0 ? 0 : kj;
    const int ck = slot ^ ((4 - ((key >> 2) & 3)) & 3);
    const int cv = slot ^ (((key >> 2) & 1) << 1);
    glds16(kh, (unsigned)((kj * 32 + ck * 8) * 2), lds0 + piece * 1024);
    glds16(vh, (unsigned)((kj * 32 + cv * 8) * 2), lds0 + NKEY * 64 + piece * 1024);
  }
  const int fr = lane & 15, g = lane >> 4;
  const int qw = q0 + wid * 16;
  bf16x8 qf = *(const bf16x8*)(qh + (size_t)(qw + fr) * 32 + g * 8);
  __builtin_amdgcn_s_waitcnt(0x0F70);
  asm volatile("" ::: "memory");
  unsigned touch = 0;
  if (wid < 3) {
    const int line = (bid * 3 + wid) * 64 + lane;
    if (line < 73728) {
      const char* p = line < 16384 ? wo + (size_t)line * 128 : w2 + (size_t)(line - 16384) * 128;
      asm volatile("global_load_dword %0, %1, off" : "+v"(touch) : "v"(p) : "memory");
    }
  }
  const int cb = bid < 448 ? bid : 447;
  const int cg = cb % 14, kc = cb / 14;
  const u16* cp = w1 + (size_t)(kc * 32 + (tid >> 5) * 2) * FF + cg * 256 + (tid & 31) * 8;
  const u32x4 cw0 = *(const u32x4*)cp;
  const u32x4 cw1 = *(const u32x4*)(cp + FF);
  __builtin_amdgcn_s_barrier();
  u16* op = o + (size_t)(qw + fr) * DM + head * DH;
  if (q0 < WIN) {
    const int tmin = (WIN - qw) >> 4;
    attn_body<true>(smem, smem + NKEY * 64, qf, wid, lane, tmin, op);
  } else {
    attn_body<false>(smem, smem + NKEY * 64, qf, wid, lane, 0, op);
  }
  if (bid < 448) {
    __syncthreads();
    float* red = (float*)smem;
    f32x4 lo, hi;
#pragma unroll
    for (int e = 0; e < 2; ++e) {
      lo[2 * e] = bflo(cw0[e]) + bflo(cw1[e]);
      lo[2 * e + 1] = bfhi(cw0[e]) + bfhi(cw1[e]);
      hi[2 * e] = bflo(cw0[2 + e]) + bflo(cw1[2 + e]);
      hi[2 * e + 1] = bfhi(cw0[2 + e]) + bfhi(cw1[2 + e]);
    }
    *(f32x4*)(red + (tid >> 5) * 256 + (tid & 31) * 8) = lo;
    *(f32x4*)(red + (tid >> 5) * 256 + (tid & 31) * 8 + 4) = hi;
    __syncthreads();
    if (tid < 256) {
      float t = 0.f;
#pragma unroll
      for (int i = 0; i < 16; ++i) t += red[i * 256 + tid];
      cspart[(size_t)kc * FF + cg * 256 + tid] = t;
    }
  }
  asm volatile("s_waitcnt vmcnt(0)" : "+v"(touch)::"memory");
}

template <int BN, int NBUF, int EPI, int XA>
static void launch_gemm(const u16* A, const u16* W, u16* C, const u16* bias, const u16* res, float* aux0,
                        float* aux1, unsigned* flags, int N, int K, hipStream_t stream) {
  constexpr int lds = NBUF * (128 * 64 * 2 + 64 * BN * 2) + (EPI == EPI_LN_GELU ? 1024 + BN * 8 : 0);
  auto kern = gemm_kernel<BN, NBUF, EPI, XA>;
  (void)hipFuncSetAttribute((const void*)kern, hipFuncAttributeMaxDynamicSharedMemorySize, lds);
  kern<<<256, 512, lds, stream>>>(A, W, C, bias, res, aux0, aux1, flags, N, K);
}

extern "C" void kernel_launch(void* const* d_in, const int* in_sizes, int n_in, void* d_out, int out_size,
                              void* d_ws, size_t ws_size, hipStream_t stream) {
  const u16* x = (const u16*)d_in[0];
  const u16* wqkv = (const u16*)d_in[1];
  const u16* wo = (const u16*)d_in[2];
  const u16* w1 = (const u16*)d_in[4];
  const u16* b1 = (const u16*)d_in[5];
  const u16* w2 = (const u16*)d_in[6];
  const u16* b2 = (const u16*)d_in[7];
  u16* out = (u16*)d_out;
  char* ws = (char*)d_ws;
  u16* xn = (u16*)(ws);
  u16* qkv = (u16*)(ws + (4u << 20));
  u16* o = (u16*)(ws + (16u << 20));
  u16* h = (u16*)(ws + (20u << 20));
  u16* hh = (u16*)(ws + (28u << 20));
  float* stats = (float*)(ws + (44u << 20));
  float* cspart = (float*)(ws + (45u << 20));
  unsigned* flags = (unsigned*)(ws + (46u << 20));
  float* xchg = (float*)(ws + (48u << 20));

  prep_kernel<<<S_LEN / 4, 256, 0, stream>>>(x, xn, (const char*)wqkv, flags);
  launch_gemm<192, 4, EPI_QKV, 8>(xn, wqkv, qkv, nullptr, nullptr, nullptr, nullptr, nullptr, 3 * DM, DM, stream);
  attn_kernel<<<NH * (S_LEN / 128), 512, 0, stream>>>(qkv, o, (const char*)wo, w1, (const char*)w2, cspart);
  launch_gemm<64, 6, EPI_RES_STATS, 4>(o, wo, h, nullptr, x, stats, nullptr, nullptr, DM, DM, stream);
  launch_gemm<224, 3, EPI_LN_GELU, 8>(h, w1, hh, b1, nullptr, stats, cspart, nullptr, FF, DM, stream);
  launch_gemm<128, 4, EPI_SPLIT_BIAS_RES, 4>(hh, w2, out, b2, h, xchg, nullptr, flags, DM, FF, stream);
}
```

```cpp
#include <hip/hip_runtime.h>
#include <hip/hip_bf16.h>
#include <stdint.h>

typedef unsigned short u16;
typedef __attribute__((ext_vector_type(8))) short bf16x8;
typedef __attribute__((ext_vector_type(4))) short s16x4;
typedef __attribute__((ext_vector_type(4))) float f32x4;
typedef __attribute__((ext_vector_type(2))) float f32x2;
typedef __attribute__((ext_vector_type(4))) unsigned u32x4;
typedef __attribute__((ext_vector_type(2))) unsigned u32x2;
typedef __attribute__((ext_vector_type(2))) __bf16 bf16x2_t;
typedef __attribute__((address_space(3))) const char* lds_cptr;

#define STAMP(kid, i)
__device__ __forceinline__ void st16_sc1(void* p, __attribute__((ext_vector_type(4))) unsigned v) { asm volatile("global_store_dwordx4 %0, %1, off sc1\n\ts_nop 1" :: "v"(p), "v"(v) : "memory"); }
#define ST16(p, v) st16_sc1((void*)(p), (v))
constexpr int S_LEN = 2048, DM = 1024, NH = 32, DH = 32, WIN = 256, FF = 3584;
constexpr float LN_EPS = 1e-5f;

__device__ __forceinline__ float bf2f(u16 v) { return __uint_as_float(((unsigned)v) << 16); }
__device__ __forceinline__ float bflo(unsigned v) { return __uint_as_float(v << 16); }
__device__ __forceinline__ float bfhi(unsigned v) { return __uint_as_float(v & 0xffff0000u); }
__device__ __forceinline__ unsigned pk2(float lo, float hi) {
  return __builtin_bit_cast(unsigned, __builtin_convertvector(f32x2{lo, hi}, bf16x2_t));
}
__device__ __forceinline__ void glds16(const void* sbase, unsigned voff, unsigned lds_addr) {
  unsigned sv;
  asm volatile("s_mov_b32 %0, m0\n\ts_mov_b32 m0, %3\n\ts_nop 0\n\tglobal_load_lds_dwordx4 %1, %2\n\ts_mov_b32 m0, %0"
               : "=&s"(sv) : "v"(voff), "s"(sbase), "s"(lds_addr) : "memory");
}
__device__ __forceinline__ s16x4 vtr(lds_cptr p) {
  return __builtin_bit_cast(s16x4, __builtin_amdgcn_ds_read_tr16_b64_v4i16((__attribute__((address_space(3))) s16x4*)p));
}
template <int CTRL>
__device__ __forceinline__ float dpp_add(float v) {
  return v + __int_as_float(__builtin_amdgcn_update_dpp(0, __float_as_int(v), CTRL, 0xf, 0xf, false));
}
__device__ __forceinline__ float sum8(float v) {
  v = dpp_add<0xB1>(v);
  v = dpp_add<0x4E>(v);
  return dpp_add<0x141>(v);
}
__device__ __forceinline__ float xsum16_32(float m) {
  auto a = __builtin_amdgcn_permlane32_swap(__float_as_uint(m), __float_as_uint(m), false, false);
  m = __uint_as_float(a[0]) + __uint_as_float(a[1]);
  auto b = __builtin_amdgcn_permlane16_swap(__float_as_uint(m), __float_as_uint(m), false, false);
  return __uint_as_float(b[0]) + __uint_as_float(b[1]);
}
__device__ __forceinline__ float xmax16_32(float m) {
  auto a = __builtin_amdgcn_permlane32_swap(__float_as_uint(m), __float_as_uint(m), false, false);
  m = fmaxf(__uint_as_float(a[0]), __uint_as_float(a[1]));
  auto b = __builtin_amdgcn_permlane16_swap(__float_as_uint(m), __float_as_uint(m), false, false);
  return fmaxf(__uint_as_float(b[0]), __uint_as_float(b[1]));
}
__device__ __forceinline__ float wave_sum(float v) {
  v = sum8(v);
  v = dpp_add<0x140>(v);
  return xsum16_32(v);
}

__global__ __launch_bounds__(256) void prep_kernel(const u16* __restrict__ in, u16* __restrict__ out,
                                                   const char* __restrict__ wqkv, unsigned* __restrict__ flags) {
  const int lane = threadIdx.x & 63;
  const int row = blockIdx.x * 4 + (threadIdx.x >> 6);
  const u32x4* src = (const u32x4*)(in + (size_t)row * DM);
  u32x4 a = src[lane], b = src[lane + 64];
  const int gi = blockIdx.x * 256 + threadIdx.x;
  unsigned w0 = 0;
  if (gi < 49152) w0 = *(const unsigned*)(wqkv + (size_t)gi * 128);
  if (gi < 1024) flags[gi] = 0u;
  unsigned w1c = 0;
  if (blockIdx.x < 48 && threadIdx.x < 64) {
    unsigned long long pc;
    asm volatile("s_getpc_b64 %0" : "=s"(pc));
    const int line = (blockIdx.x >> 3) * 64 + lane;
    if (line < 360) w1c = *(const unsigned*)((pc & ~127ull) + (size_t)line * 128);
  }
  float v[16];
#pragma unroll
  for (int i = 0; i < 4; ++i) {
    v[2 * i] = bflo(a[i]);
    v[2 * i + 1] = bfhi(a[i]);
    v[8 + 2 * i] = bflo(b[i]);
    v[8 + 2 * i + 1] = bfhi(b[i]);
  }
  float s = 0.f;
#pragma unroll
  for (int i = 0; i < 16; ++i) s += v[i];
  const float mean = wave_sum(s) * (1.0f / DM);
  float q = 0.f;
#pragma unroll
  for (int i = 0; i < 16; ++i) { v[i] -= mean; q += v[i] * v[i]; }
  const float var = wave_sum(q) * (1.0f / DM);
  const float r = 1.0f / sqrtf(var + LN_EPS);
  u32x4 oa, ob;
#pragma unroll
  for (int i = 0; i < 4; ++i) {
    oa[i] = pk2(v[2 * i] * r, v[2 * i + 1] * r);
    ob[i] = pk2(v[8 + 2 * i] * r, v[8 + 2 * i + 1] * r);
  }
  u32x4* dst = (u32x4*)(out + (size_t)row * DM);
  dst[lane] = oa;
  dst[lane + 64] = ob;
  asm volatile("" ::"v"(w0), "v"(w1c));
}

enum { EPI_QKV = 0, EPI_RES_STATS = 1, EPI_LN_GELU = 2, EPI_SPLIT_BIAS_RES = 3 };
constexpr int NLW = 4;

template <int CNT, int NBUF, int NPA, int STAGE, bool CS>
__device__ __forceinline__ void loader_loop(const char* Ab, const char* Wb, const unsigned* voff, int lw, int nk,
                                            unsigned wstep, unsigned lds0, const char* smem, float* csacc) {
  const int lane = threadIdx.x & 63;
  int ca0 = 0, ca1 = 0, cstep = 0;
  if (CS) {
    if (lw < 3) {
      const int k = lane >> 3, c = lane & 7;
      ca0 = NPA * 1024 + lw * 8192 + k * 128 + ((c ^ (k & 2)) << 4);
      ca1 = NPA * 1024 + lw * 8192 + (k + 8) * 128 + ((c ^ ((k & 2) | 4)) << 4);
      cstep = 2048;
    } else {
      const int k = lane >> 2, c = lane & 3;
      ca0 = NPA * 1024 + 3 * 8192 + k * 64 + ((c ^ ((k >> 2) & 2)) << 4);
      ca1 = ca0 + 1024;
      cstep = 2048;
    }
  }
  auto colsum = [&](int buf) {
    const char* base = smem + buf * STAGE;
    const int n = (lw < 3) ? 4 : 2;
#pragma unroll
    for (int i = 0; i < 4; ++i) {
      if (i < n) {
        const u32x4 u = *(const u32x4*)(base + ca0 + i * cstep);
        const u32x4 v = *(const u32x4*)(base + ca1 + i * cstep);
#pragma unroll
        for (int e = 0; e < 4; ++e) {
          csacc[2 * e] += bflo(u[e]) + bflo(v[e]);
          csacc[2 * e + 1] += bfhi(u[e]) + bfhi(v[e]);
        }
      }
    }
  };
  const char* sp[CNT];
  unsigned st[CNT];
#pragma unroll
  for (int i = 0; i < CNT; ++i) {
    const bool isA = (lw + i * NLW) < NPA;
    sp[i] = isA ? Ab : Wb;
    st[i] = isA ? 128u : wstep;
  }
  auto issue = [&](int buf) {
#pragma unroll
    for (int i = 0; i < CNT; ++i) {
      glds16(sp[i], voff[i], lds0 + buf * STAGE + (lw + i * NLW) * 1024);
      sp[i] += st[i];
    }
  };
  issue(0);
  issue(1);
  asm volatile("s_waitcnt vmcnt(%0)" ::"i"(CNT) : "memory");
  __builtin_amdgcn_s_barrier();
#pragma unroll
  for (int s = 2; s < NBUF; ++s) issue(s);
  if (CS) colsum(0);
  int buf = 0;
  for (int t = 0; t < nk; ++t) {
    if (t + NBUF <= nk) {
      asm volatile("s_waitcnt vmcnt(%0)" ::"i"((NBUF - 2) * CNT) : "memory");
    } else {
      asm volatile("s_waitcnt vmcnt(0)" ::: "memory");
    }
    __builtin_amdgcn_s_barrier();
    if (t + NBUF < nk) issue(buf);
    buf = (buf + 1 == NBUF) ? 0 : buf + 1;
    if (CS && t + 1 < nk) colsum(buf);
  }
}

template <int BN, int NBUF, int EPI, int XA>
__global__ __launch_bounds__(512, 1) void gemm_kernel(const u16* __restrict__ A, const u16* __restrict__ W,
                                                     u16* __restrict__ C, const u16* __restrict__ bias,
                                                     const u16* __restrict__ res, float* __restrict__ aux0,
                                                     float* __restrict__ aux1, unsigned* __restrict__ flags,
                                                     int N, int K) {
  constexpr int BM = 128;
  constexpr bool WIDE = BN > 64;
  constexpr bool SPLIT = (EPI == EPI_SPLIT_BIAS_RES);
  constexpr int NTHR = 512;
  constexpr int NG = BN / 64;
  constexpr bool HALF = (BN % 64) != 0;
  constexpr int NT = WIDE ? BN / 32 : 4;
  constexpr int ABYTES = BM * 128, WBYTES = 64 * BN * 2, STAGE = ABYTES + WBYTES;
  constexpr int NPA = ABYTES / 1024, NP = STAGE / 1024;
  constexpr int CMAX = (NP + NLW - 1) / NLW;
  constexpr int SIDE = NBUF * STAGE;
  extern __shared__ __attribute__((aligned(16))) char smem[];

  const int tid = threadIdx.x, lane = tid & 63;
  const int wid = __builtin_amdgcn_readfirstlane(tid >> 6);
  STAMP(EPI, 0);
  const int xc = blockIdx.x & 7, rr = blockIdx.x >> 3;
  int bm, bn, hk = 0;
  if (SPLIT) {
    hk = rr & 1;
    const int tt = rr >> 1;
    bm = (xc & 3) * 4 + (tt & 3);
    bn = (xc >> 2) * 4 + (tt >> 2);
  } else {
    constexpr int XB = 32 / XA, PM = 16 / XA;
    bm = (xc % PM) * XA + (rr % XA);
    bn = (xc / PM) * XB + (rr / XA);
  }
  const int m0 = bm * BM, n0 = bn * BN;
  const int nk = SPLIT ? K / 128 : K / 64;
  const int kbeg = hk * (K / 2);
  const int odd = HALF ? (bn & 1) : 0;
  const int cfull = odd ? 32 : 0, chalf = odd ? 0 : NG * 64;
  const unsigned lds0 = (unsigned)(uintptr_t)(__attribute__((address_space(3))) char*)smem;
  const int fr = lane & 15, g = lane >> 4;

  f32x4 acc[4][NT];
  const int wm = wid & 1, wx = (wid >> 1) & 1;
  int tcol[NT];

  if (wid >= 4) {
    const int lw = wid - 4;
    unsigned voff[CMAX];
#pragma unroll
    for (int i = 0; i < CMAX; ++i) {
      const int idx = lw + i * NLW;
      if (idx < NPA) {
        const int row = idx * 8 + (lane >> 3), ch = (lane & 7) ^ (row & 7);
        voff[i] = (unsigned)(((m0 + row) * K + ch * 8) * 2);
      } else if (idx < NPA + NG * 8) {
        const int w = idx - NPA, G = w >> 3, k = (w & 7) * 8 + (lane >> 3);
        const int ch = (lane & 7) ^ ((k & 2) | ((k >> 1) & 4));
        voff[i] = (unsigned)((k * N + n0 + cfull + G * 64 + ch * 8) * 2);
      } else {
        const int w = idx - NPA - NG * 8, k = w * 16 + (lane >> 2);
        const int ch = (lane & 3) ^ ((k >> 2) & 2);
        voff[i] = (unsigned)((k * N + n0 + chalf + ch * 8) * 2);
      }
    }
    const unsigned wstep = (unsigned)(64 * N * 2);
    const char* Ab = (const char*)(A + kbeg);
    const char* Wb = (const char*)(W + (size_t)kbeg * N);
    constexpr bool CS = false;
    float csacc[8];
#pragma unroll
    for (int e = 0; e < 8; ++e) csacc[e] = 0.f;
    if (lw + (CMAX - 1) * NLW < NP)
      loader_loop<CMAX, NBUF, NPA, STAGE, CS>(Ab, Wb, voff, lw, nk, wstep, lds0, smem, csacc);
    else
      loader_loop<CMAX - 1, NBUF, NPA, STAGE, CS>(Ab, Wb, voff, lw, nk, wstep, lds0, smem, csacc);
    if (CS) {
#pragma unroll
      for (int e = 0; e < 8; ++e) {
        float t = csacc[e];
        const float t4 = dpp_add<0x124>(t);
        t = (lw == 3) ? t4 : t;
        t = dpp_add<0x128>(t);
        csacc[e] = xsum16_32(t);
      }
      const int nl = (lw < 3) ? 8 : 4;
      if (lane < nl) {
        const int col = ((lw < 3) ? cfull + lw * 64 : chalf) + lane * 8;
#pragma unroll
        for (int e = 0; e < 8; ++e) *(float*)(smem + SIDE + 1024 + (col + e) * 8) = csacc[e];
      }
    }
  } else {
    if (EPI == EPI_LN_GELU) {
      if (tid < 128) {
        const f32x4* sp = (const f32x4*)(aux0 + (size_t)(m0 + tid) * 32);
        f32x4 p[8];
#pragma unroll
        for (int i = 0; i < 8; ++i) p[i] = sp[i];
        float mean = 0.f;
#pragma unroll
        for (int i = 0; i < 8; ++i) mean += p[i][0] + p[i][2];
        mean *= (1.0f / 16.0f);
        float m2 = 0.f;
#pragma unroll
        for (int i = 0; i < 8; ++i) {
          const float d0 = p[i][0] - mean, d1 = p[i][2] - mean;
          m2 += p[i][1] + p[i][3] + 64.0f * (d0 * d0 + d1 * d1);
        }
        const float var = m2 * (1.0f / DM);
        const float r1 = 1.0f / sqrtf(var + LN_EPS);
        const float r2 = 1.0f / sqrtf(var * r1 * r1 + LN_EPS);
        *(f32x2*)(smem + SIDE + tid * 8) = f32x2{mean, r1 * r2};
      } else if (tid - 128 < BN / 2) {
        const int c = (tid - 128) * 2;
        f32x2 cs = {0.f, 0.f};
#pragma unroll
        for (int kc = 0; kc < 32; ++kc) cs += *(const f32x2*)(aux1 + (size_t)kc * FF + n0 + c);
        const unsigned bb = *(const unsigned*)(bias + n0 + c);
        *(f32x4*)(smem + SIDE + 1024 + c * 8) = f32x4{cs[0], bflo(bb), cs[1], bfhi(bb)};
      }
    }
    const int q = (lane >> 2) & 3, pp = lane & 3;
    const int wk = WIDE ? 0 : wx;
    int aoff[2];
#pragma unroll
    for (int ks = 0; ks < 2; ++ks) aoff[ks] = (wm * 64 + fr) * 128 + (((ks * 4 + g) ^ (fr & 7)) << 4);
    int tb[NT];
    const int f1 = (q & 2) | ((g & 1) << 2);
#pragma unroll
    for (int j = 0; j < NT; ++j) {
      int G, tp;
      if (!WIDE) { G = 0; tp = j; }
      else if (j < 4) { G = wx * (NG - 1); tp = j; }
      else { G = 1; tp = wx * 2 + (j - 4); }
      if (WIDE && HALF && j == NT - 1) {
        tb[j] = ABYTES + NG * 8192 + (8 * g + q) * 64 + (((2 * wx + (pp >> 1)) ^ ((g & 1) << 1)) << 4) + (pp & 1) * 8;
        tcol[j] = chalf + wx * 16;
      } else {
        tb[j] = ABYTES + G * 8192 + (8 * g + q) * 128 + (((2 * tp + (pp >> 1)) ^ f1) << 4) + (pp & 1) * 8;
        tcol[j] = cfull + G * 64 + tp * 16;
      }
    }
#pragma unroll
    for (int i = 0; i < 4; ++i)
#pragma unroll
      for (int j = 0; j < NT; ++j) acc[i][j] = f32x4{0.f, 0.f, 0.f, 0.f};

    bf16x8 xa[4], wa[NT], xb[4], wb[NT];
    auto rd = [&](bf16x8* xf, bf16x8* wf, int buf, int ks) {
      const char* base = smem + buf * STAGE;
#pragma unroll
      for (int j = 0; j < NT; ++j) {
        const int rs = (WIDE && HALF && j == NT - 1) ? 64 : 128;
        s16x4 lo = vtr((lds_cptr)(base + tb[j] + ks * 32 * rs));
        s16x4 hi = vtr((lds_cptr)(base + tb[j] + ks * 32 * rs + 4 * rs));
        wf[j] = __builtin_shufflevector(lo, hi, 0, 1, 2, 3, 4, 5, 6, 7);
      }
#pragma unroll
      for (int i = 0; i < 4; ++i) xf[i] = *(const bf16x8*)(base + aoff[ks] + i * 2048);
    };
    auto mm = [&](const bf16x8* xf, const bf16x8* wf) {
#pragma unroll
      for (int i = 0; i < 4; ++i)
#pragma unroll
        for (int j = 0; j < NT; ++j)
          acc[i][j] = __builtin_amdgcn_mfma_f32_16x16x32_bf16(wf[j], xf[i], acc[i][j], 0, 0, 0);
    };
#define INTERLEAVE() do { _Pragma("unroll") for (int z_ = 0; z_ < 2 * NT + 4; ++z_) { __builtin_amdgcn_sched_group_barrier(0x8, 1, 0); __builtin_amdgcn_sched_group_barrier(0x100, 1, 0); } __builtin_amdgcn_sched_group_barrier(0x8, 4 * NT - (2 * NT + 4), 0); } while (0)
    __builtin_amdgcn_s_waitcnt(0xC07F);
    __builtin_amdgcn_s_barrier();
    STAMP(EPI, 1);
    if (WIDE) {
      rd(xa, wa, 0, 0);
      int b0 = 0;
      for (int t = 0; t < nk; ++t) {
        rd(xb, wb, b0, 1);
        b0 = (b0 + 1 == NBUF) ? 0 : b0 + 1;
        mm(xa, wa);
        INTERLEAVE();
        __builtin_amdgcn_sched_barrier(0);
        __builtin_amdgcn_s_waitcnt(0xC07F);
        __builtin_amdgcn_s_barrier();
        rd(xa, wa, b0, 0);
        mm(xb, wb);
        INTERLEAVE();
        __builtin_amdgcn_sched_barrier(0);
      }
    } else {
      rd(xa, wa, 0, wk);
      int b1 = 1;
      for (int t = 0; t < nk; t += 2) {
        __builtin_amdgcn_s_waitcnt(0xC07F);
        __builtin_amdgcn_s_barrier();
        rd(xb, wb, b1, wk);
        b1 = (b1 + 1 == NBUF) ? 0 : b1 + 1;
        mm(xa, wa);
        INTERLEAVE();
        __builtin_amdgcn_sched_barrier(0);
        __builtin_amdgcn_s_waitcnt(0xC07F);
        __builtin_amdgcn_s_barrier();
        rd(xa, wa, b1, wk);
        b1 = (b1 + 1 == NBUF) ? 0 : b1 + 1;
        mm(xb, wb);
        INTERLEAVE();
        __builtin_amdgcn_sched_barrier(0);
      }
    }
    __builtin_amdgcn_s_waitcnt(0xC07F);
    STAMP(EPI, 2);
    if (SPLIT) {
      const int tile = bm * 8 + bn;
      const auto rsrc = __builtin_amdgcn_make_buffer_rsrc(aux0, 0, 128 * 2 * 2 * 16384, 0x00020000);
      if (wm != hk) {
        const int slot = ((tile * 2 + hk) * 2 + wx);
#pragma unroll
        for (int i = 0; i < 4; ++i)
#pragma unroll
          for (int j = 0; j < NT; ++j)
            __builtin_amdgcn_raw_buffer_store_b128(__builtin_bit_cast(u32x4, acc[i][j]), rsrc,
                                                   slot * 16384 + (i * NT + j) * 1024 + lane * 16, 0, 16);
        asm volatile("s_waitcnt vmcnt(0)" ::: "memory");
        if (lane == 0) __hip_atomic_store(flags + slot, 1u, __ATOMIC_RELAXED, __HIP_MEMORY_SCOPE_AGENT);
      } else {
        const int slot = ((tile * 2 + (hk ^ 1)) * 2 + wx);
        for (int spin = 0; spin < (1 << 24); ++spin) {
          if (__hip_atomic_load(flags + slot, __ATOMIC_RELAXED, __HIP_MEMORY_SCOPE_AGENT) != 0u) break;
          __builtin_amdgcn_s_sleep(2);
        }
        asm volatile("" ::: "memory");
#pragma unroll
        for (int i = 0; i < 4; ++i) {
          u32x4 t[NT];
#pragma unroll
          for (int j = 0; j < NT; ++j)
            t[j] = __builtin_amdgcn_raw_buffer_load_b128(rsrc, slot * 16384 + (i * NT + j) * 1024 + lane * 16, 0, 16);
#pragma unroll
          for (int j = 0; j < NT; ++j) acc[i][j] += __builtin_bit_cast(f32x4, t[j]);
        }
      }
    }
  }
  __syncthreads();
  STAMP(EPI, 3);

  constexpr int CPR = BN / 8;
  if (EPI == EPI_QKV) {
    constexpr int RS = BN * 2 + 16;
    if (wid < 4) {
#pragma unroll
      for (int j = 0; j < NT; ++j)
#pragma unroll
        for (int i = 0; i < 4; ++i) {
          u32x2 o;
          o[0] = pk2(acc[i][j][0], acc[i][j][1]);
          o[1] = pk2(acc[i][j][2], acc[i][j][3]);
          *(u32x2*)(smem + (wm * 64 + i * 16 + fr) * RS + (tcol[j] + g * 4) * 2) = o;
        }
    }
    __syncthreads();
#pragma unroll
    for (int c = tid; c < BM * CPR; c += NTHR) {
      const int hs = c >> 9, row = (c >> 2) & 127, c4 = c & 3;
      u32x4 v = *(const u32x4*)(smem + row * RS + (hs * 4 + c4) * 16);
      ST16(C + ((size_t)((n0 >> 5) + hs) * S_LEN + m0 + row) * 32 + c4 * 8, v);
    }
  } else if (EPI == EPI_LN_GELU) {
    constexpr int RS = BN * 4 + 16;
    if (wid < 4) {
#pragma unroll
      for (int j = 0; j < NT; ++j)
#pragma unroll
        for (int i = 0; i < 4; ++i)
          *(f32x4*)(smem + (wm * 64 + i * 16 + fr) * RS + (tcol[j] + g * 4) * 4) = acc[i][j];
    }
    __syncthreads();
#pragma unroll
    for (int c = tid; c < BM * CPR; c += NTHR) {
      const int row = c / CPR, ch = c % CPR;
      const f32x4 a0 = *(const f32x4*)(smem + row * RS + ch * 32);
      const f32x4 a1 = *(const f32x4*)(smem + row * RS + ch * 32 + 16);
      const f32x2 rs = *(const f32x2*)(smem + SIDE + row * 8);
      float v[8];
#pragma unroll
      for (int r = 0; r < 4; ++r) {
        const f32x4 cb = *(const f32x4*)(smem + SIDE + 1024 + ch * 64 + r * 16);
        const float x0 = (r < 2) ? a0[2 * r] : a1[2 * r - 4];
        const float x1 = (r < 2) ? a0[2 * r + 1] : a1[2 * r - 3];
        v[2 * r] = (x0 - rs[0] * cb[0]) * rs[1] + cb[1];
        v[2 * r + 1] = (x1 - rs[0] * cb[2]) * rs[1] + cb[3];
      }
      u32x4 o;
#pragma unroll
      for (int r = 0; r < 4; ++r) {
        const f32x2 x = {v[2 * r], v[2 * r + 1]};
        const f32x2 x2 = x * x;
        const f32x2 w = x2 * f32x2{-0.10294324f, -0.10294324f} + f32x2{-2.302208198f, -2.302208198f};
        const f32x2 z = x * w;
        f32x2 e = {__builtin_amdgcn_exp2f(z[0]), __builtin_amdgcn_exp2f(z[1])};
        e = e + f32x2{1.0f, 1.0f};
        const f32x2 y = x * f32x2{__builtin_amdgcn_rcpf(e[0]), __builtin_amdgcn_rcpf(e[1])};
        o[r] = pk2(y[0], y[1]);
      }
      ST16(C + (size_t)(m0 + row) * N + n0 + ch * 8, o);
    }
  } else if (EPI == EPI_RES_STATS) {
    constexpr int RS = BN * 4 + 16;
    constexpr int PART = BM * RS;
    if (wid < 4) {
#pragma unroll
      for (int i = 0; i < 4; ++i)
#pragma unroll
        for (int j = 0; j < NT; ++j)
          *(f32x4*)(smem + wx * PART + (wm * 64 + i * 16 + fr) * RS + (j * 16 + g * 4) * 4) = acc[i][j];
    }
    __syncthreads();
#pragma unroll
    for (int c = tid; c < BM * CPR; c += NTHR) {
      const int row = c / CPR, ch = c % CPR;
      const f32x4 a0 = *(const f32x4*)(smem + row * RS + ch * 32);
      const f32x4 a1 = *(const f32x4*)(smem + row * RS + ch * 32 + 16);
      const f32x4 b0 = *(const f32x4*)(smem + PART + row * RS + ch * 32);
      const f32x4 b1 = *(const f32x4*)(smem + PART + row * RS + ch * 32 + 16);
      const u32x4 rv = *(const u32x4*)(res + (size_t)(m0 + row) * N + n0 + ch * 8);
      u32x4 o;
      o[0] = pk2(a0[0] + b0[0] + bflo(rv[0]), a0[1] + b0[1] + bfhi(rv[0]));
      o[1] = pk2(a0[2] + b0[2] + bflo(rv[1]), a0[3] + b0[3] + bfhi(rv[1]));
      o[2] = pk2(a1[0] + b1[0] + bflo(rv[2]), a1[1] + b1[1] + bfhi(rv[2]));
      o[3] = pk2(a1[2] + b1[2] + bflo(rv[3]), a1[3] + b1[3] + bfhi(rv[3]));
      ST16(C + (size_t)(m0 + row) * N + n0 + ch * 8, o);
      float s = 0.f;
#pragma unroll
      for (int r = 0; r < 4; ++r) s += bflo(o[r]) + bfhi(o[r]);
      const float mean = sum8(s) * (1.0f / 64.0f);
      float m2 = 0.f;
#pragma unroll
      for (int r = 0; r < 4; ++r) {
        const float d0 = bflo(o[r]) - mean, d1 = bfhi(o[r]) - mean;
        m2 += d0 * d0 + d1 * d1;
      }
      m2 = sum8(m2);
      if (ch == 0) *(f32x2*)(aux0 + ((size_t)(m0 + row) * 16 + bn) * 2) = f32x2{mean, m2};
    }
  } else {
    constexpr int RS = BN * 4 + 16;
    if (wid < 4 && wm == hk) {
#pragma unroll
      for (int i = 0; i < 4; ++i)
#pragma unroll
        for (int j = 0; j < NT; ++j)
          *(f32x4*)(smem + (i * 16 + fr) * RS + (tcol[j] + g * 4) * 4) = acc[i][j];
    }
    __syncthreads();
#pragma unroll
    for (int c = tid; c < 64 * CPR; c += NTHR) {
      const int row = c / CPR, ch = c % CPR;
      const f32x4 a0 = *(const f32x4*)(smem + row * RS + ch * 32);
      const f32x4 a1 = *(const f32x4*)(smem + row * RS + ch * 32 + 16);
      const u32x4 bb = *(const u32x4*)(bias + n0 + ch * 8);
      const size_t go = (size_t)(m0 + hk * 64 + row) * N + n0 + ch * 8;
      const u32x4 rv = *(const u32x4*)(res + go);
      u32x4 o;
      o[0] = pk2(a0[0] + bflo(bb[0]) + bflo(rv[0]), a0[1] + bfhi(bb[0]) + bfhi(rv[0]));
      o[1] = pk2(a0[2] + bflo(bb[1]) + bflo(rv[1]), a0[3] + bfhi(bb[1]) + bfhi(rv[1]));
      o[2] = pk2(a1[0] + bflo(bb[2]) + bflo(rv[2]), a1[1] + bfhi(bb[2]) + bfhi(rv[2]));
      o[3] = pk2(a1[2] + bflo(bb[3]) + bflo(rv[3]), a1[3] + bfhi(bb[3]) + bfhi(rv[3]));
      ST16(C + go, o);
    }
  }
}

template <bool EARLY>
__device__ __forceinline__ void attn_body(const char* ks, const char* vs, bf16x8 qf, int wid, int lane, int tmin,
                                          u16* __restrict__ op) {
  const int fr = lane & 15, g = lane >> 4;
  const float NEG = -1e30f;
  f32x4 s[17];
  const char* kp = ks + (wid * 16 + fr) * 64 + ((g ^ ((4 - ((fr >> 2) & 3)) & 3)) << 4);
#pragma unroll
  for (int t = 0; t < 17; ++t) {
    bf16x8 kf = *(const bf16x8*)(kp + t * 1024);
    s[t] = __builtin_amdgcn_mfma_f32_16x16x32_bf16(kf, qf, f32x4{0.f, 0.f, 0.f, 0.f}, 0, 0, 0);
  }
#pragma unroll
  for (int r = 0; r < 4; ++r) {
    s[0][r] = (4 * g + r > fr) ? s[0][r] : NEG;
    s[16][r] = (4 * g + r <= fr) ? s[16][r] : NEG;
  }
  if (EARLY) {
#pragma unroll
    for (int t = 0; t < 16; ++t)
      if (t < tmin) s[t] = f32x4{NEG, NEG, NEG, NEG};
  }
  float mx = NEG;
#pragma unroll
  for (int t = 0; t < 17; ++t) mx = fmaxf(fmaxf(mx, fmaxf(s[t][0], s[t][1])), fmaxf(s[t][2], s[t][3]));
  mx = xmax16_32(mx);
  const float c = 0.17677669529663687f * 1.4426950408889634f;
  const float nmc = -mx * c;
  float sum = 0.f;
  unsigned pk[18][2];
#pragma unroll
  for (int t = 0; t < 17; ++t) {
    float p[4];
#pragma unroll
    for (int r = 0; r < 4; ++r) {
      p[r] = __builtin_amdgcn_exp2f(__builtin_fmaf(s[t][r], c, nmc));
      sum += p[r];
    }
    pk[t][0] = pk2(p[0], p[1]);
    pk[t][1] = pk2(p[2], p[3]);
  }
  pk[17][0] = 0u;
  pk[17][1] = 0u;
  sum = xsum16_32(sum);
  const int q4 = (lane >> 2) & 3, pp = lane & 3;
  const char* vp0 = vs + (wid * 16 + 4 * g + q4) * 64 + ((g & 1) << 5) + pp * 8;
  const char* vp1 = vs + (wid * 16 + 4 * g + q4) * 64 + (((g & 1) ^ 1) << 5) + pp * 8;
  f32x4 o0 = {0.f, 0.f, 0.f, 0.f}, o1 = {0.f, 0.f, 0.f, 0.f};
#pragma unroll
  for (int tp = 0; tp < 9; ++tp) {
    const int t0 = 2 * tp, t1 = (2 * tp + 1 < 17) ? 2 * tp + 1 : 16;
    u32x4 pb = {pk[2 * tp][0], pk[2 * tp][1], pk[2 * tp + 1][0], pk[2 * tp + 1][1]};
    bf16x8 pf = __builtin_bit_cast(bf16x8, pb);
    s16x4 a0 = vtr((lds_cptr)(vp0 + t0 * 1024)), a1 = vtr((lds_cptr)(vp0 + t1 * 1024));
    s16x4 b0 = vtr((lds_cptr)(vp1 + t0 * 1024)), b1 = vtr((lds_cptr)(vp1 + t1 * 1024));
    o0 = __builtin_amdgcn_mfma_f32_16x16x32_bf16(__builtin_shufflevector(a0, a1, 0, 1, 2, 3, 4, 5, 6, 7), pf, o0, 0, 0, 0);
    o1 = __builtin_amdgcn_mfma_f32_16x16x32_bf16(__builtin_shufflevector(b0, b1, 0, 1, 2, 3, 4, 5, 6, 7), pf, o1, 0, 0, 0);
  }
  const float inv = 1.0f / sum;
  u32x2 w0, w1;
  w0[0] = pk2(o0[0] * inv, o0[1] * inv);
  w0[1] = pk2(o0[2] * inv, o0[3] * inv);
  w1[0] = pk2(o1[0] * inv, o1[1] * inv);
  w1[1] = pk2(o1[2] * inv, o1[3] * inv);
  *(u32x2*)(op + 4 * g) = w0;
  *(u32x2*)(op + 16 + 4 * g) = w1;
}

__global__ __launch_bounds__(512) void attn_kernel(const u16* __restrict__ qkv, u16* __restrict__ o,
                                                   const char* __restrict__ wo, const u16* __restrict__ w1,
                                                   const char* __restrict__ w2, float* __restrict__ cspart) {
  constexpr int QB = 128, NKEY = WIN + QB;
  __shared__ __attribute__((aligned(16))) char smem[2 * NKEY * 64];
  const int tid = threadIdx.x, lane = tid & 63;
  const int wid = __builtin_amdgcn_readfirstlane(tid >> 6);
  const int bid = blockIdx.x;
  const int head = bid & 31, qb = bid >> 5;
  const int q0 = qb * QB, kbase = q0 - WIN;
  const u16* qh = qkv + (size_t)head * S_LEN * 32;
  const u16* kh = qkv + (size_t)(NH + head) * S_LEN * 32;
  const u16* vh = qkv + (size_t)(2 * NH + head) * S_LEN * 32;
  const unsigned lds0 = (unsigned)(uintptr_t)(__attribute__((address_space(3))) char*)smem;
#pragma unroll
  for (int i = 0; i < 3; ++i) {
    const int piece = wid * 3 + i;
    const int key = piece * 16 + (lane >> 2), slot = lane & 3;
    int kj = kbase + key;
    kj = kj < 0 ? 0 : kj;
    const int ck = slot ^ ((4 - ((key >> 2) & 3)) & 3);
    const int cv = slot ^ (((key >> 2) & 1) << 1);
    glds16(kh, (unsigned)((kj * 32 + ck * 8) * 2), lds0 + piece * 1024);
    glds16(vh, (unsigned)((kj * 32 + cv * 8) * 2), lds0 + NKEY * 64 + piece * 1024);
  }
  const int fr = lane & 15, g = lane >> 4;
  const int qw = q0 + wid * 16;
  bf16x8 qf = *(const bf16x8*)(qh + (size_t)(qw + fr) * 32 + g * 8);
  __builtin_amdgcn_s_waitcnt(0x0F70);
  asm volatile("" ::: "memory");
  unsigned touch = 0;
  if (wid < 3) {
    const int line = (bid * 3 + wid) * 64 + lane;
    if (line < 73728) {
      const char* p = line < 16384 ? wo + (size_t)line * 128 : w2 + (size_t)(line - 16384) * 128;
      asm volatile("global_load_dword %0, %1, off" : "+v"(touch) : "v"(p) : "memory");
    }
  }
  const int cb = bid < 448 ? bid : 447;
  const int cg = cb % 14, kc = cb / 14;
  const u16* cp = w1 + (size_t)(kc * 32 + (tid >> 5) * 2) * FF + cg * 256 + (tid & 31) * 8;
  const u32x4 cw0 = *(const u32x4*)cp;
  const u32x4 cw1 = *(const u32x4*)(cp + FF);
  __builtin_amdgcn_s_barrier();
  u16* op = o + (size_t)(qw + fr) * DM + head * DH;
  if (q0 < WIN) {
    const int tmin = (WIN - qw) >> 4;
    attn_body<true>(smem, smem + NKEY * 64, qf, wid, lane, tmin, op);
  } else {
    attn_body<false>(smem, smem + NKEY * 64, qf, wid, lane, 0, op);
  }
  if (bid < 448) {
    __syncthreads();
    float* red = (float*)smem;
    f32x4 lo, hi;
#pragma unroll
    for (int e = 0; e < 2; ++e) {
      lo[2 * e] = bflo(cw0[e]) + bflo(cw1[e]);
      lo[2 * e + 1] = bfhi(cw0[e]) + bfhi(cw1[e]);
      hi[2 * e] = bflo(cw0[2 + e]) + bflo(cw1[2 + e]);
      hi[2 * e + 1] = bfhi(cw0[2 + e]) + bfhi(cw1[2 + e]);
    }
    *(f32x4*)(red + (tid >> 5) * 256 + (tid & 31) * 8) = lo;
    *(f32x4*)(red + (tid >> 5) * 256 + (tid & 31) * 8 + 4) = hi;
    __syncthreads();
    if (tid < 256) {
      float t = 0.f;
#pragma unroll
      for (int i = 0; i < 16; ++i) t += red[i * 256 + tid];
      cspart[(size_t)kc * FF + cg * 256 + tid] = t;
    }
  }
  asm volatile("s_waitcnt vmcnt(0)" : "+v"(touch)::"memory");
}

template <int BN, int NBUF, int EPI, int XA>
static void launch_gemm(const u16* A, const u16* W, u16* C, const u16* bias, const u16* res, float* aux0,
                        float* aux1, unsigned* flags, int N, int K, hipStream_t stream) {
  constexpr int lds = NBUF * (128 * 64 * 2 + 64 * BN * 2) + (EPI == EPI_LN_GELU ? 1024 + BN * 8 : 0);
  auto kern = gemm_kernel<BN, NBUF, EPI, XA>;
  (void)hipFuncSetAttribute((const void*)kern, hipFuncAttributeMaxDynamicSharedMemorySize, lds);
  kern<<<256, 512, lds, stream>>>(A, W, C, bias, res, aux0, aux1, flags, N, K);
}

extern "C" void kernel_launch(void* const* d_in, const int* in_sizes, int n_in, void* d_out, int out_size,
                              void* d_ws, size_t ws_size, hipStream_t stream) {
  const u16* x = (const u16*)d_in[0];
  const u16* wqkv = (const u16*)d_in[1];
  const u16* wo = (const u16*)d_in[2];
  const u16* w1 = (const u16*)d_in[4];
  const u16* b1 = (const u16*)d_in[5];
  const u16* w2 = (const u16*)d_in[6];
  const u16* b2 = (const u16*)d_in[7];
  u16* out = (u16*)d_out;
  char* ws = (char*)d_ws;
  u16* xn = (u16*)(ws);
  u16* qkv = (u16*)(ws + (4u << 20));
  u16* o = (u16*)(ws + (16u << 20));
  u16* h = (u16*)(ws + (20u << 20));
  u16* hh = (u16*)(ws + (28u << 20));
  float* stats = (float*)(ws + (44u << 20));
  float* cspart = (float*)(ws + (45u << 20));
  unsigned* flags = (unsigned*)(ws + (46u << 20));
  float* xchg = (float*)(ws + (48u << 20));

  prep_kernel<<<S_LEN / 4, 256, 0, stream>>>(x, xn, (const char*)wqkv, flags);
  launch_gemm<192, 4, EPI_QKV, 8>(xn, wqkv, qkv, nullptr, nullptr, nullptr, nullptr, nullptr, 3 * DM, DM, stream);
  attn_kernel<<<NH * (S_LEN / 128), 512, 0, stream>>>(qkv, o, (const char*)wo, w1, (const char*)w2, cspart);
  launch_gemm<64, 6, EPI_RES_STATS, 4>(o, wo, h, nullptr, x, stats, nullptr, nullptr, DM, DM, stream);
  launch_gemm<224, 3, EPI_LN_GELU, 8>(h, w1, hh, b1, nullptr, stats, cspart, nullptr, FF, DM, stream);
  launch_gemm<128, 4, EPI_SPLIT_BIAS_RES, 4>(hh, w2, out, b2, h, xchg, nullptr, flags, DM, FF, stream);
}
```

```cpp
#include <hip/hip_runtime.h>
#include <hip/hip_bf16.h>
#include <stdint.h>

typedef unsigned short u16;
typedef __attribute__((ext_vector_type(8))) short bf16x8;
typedef __attribute__((ext_vector_type(4))) short s16x4;
typedef __attribute__((ext_vector_type(4))) float f32x4;
typedef __attribute__((ext_vector_type(2))) float f32x2;
typedef __attribute__((ext_vector_type(4))) unsigned u32x4;
typedef __attribute__((ext_vector_type(2))) unsigned u32x2;
typedef __attribute__((ext_vector_type(2))) __bf16 bf16x2_t;
typedef __attribute__((address_space(3))) const char* lds_cptr;

#define STAMP(kid, i)
__device__ __forceinline__ void st16_sc1(void* p, __attribute__((ext_vector_type(4))) unsigned v) { asm volatile("global_store_dwordx4 %0, %1, off sc1\n\ts_nop 1" :: "v"(p), "v"(v) : "memory"); }
#define ST16(p, v) st16_sc1((void*)(p), (v))
constexpr int S_LEN = 2048, DM = 1024, NH = 32, DH = 32, WIN = 256, FF = 3584;
constexpr float LN_EPS = 1e-5f;

__device__ __forceinline__ float bf2f(u16 v) { return __uint_as_float(((unsigned)v) << 16); }
__device__ __forceinline__ float bflo(unsigned v) { return __uint_as_float(v << 16); }
__device__ __forceinline__ float bfhi(unsigned v) { return __uint_as_float(v & 0xffff0000u); }
__device__ __forceinline__ unsigned pk2(float lo, float hi) {
  return __builtin_bit_cast(unsigned, __builtin_convertvector(f32x2{lo, hi}, bf16x2_t));
}
__device__ __forceinline__ void glds16(const void* sbase, unsigned voff, unsigned lds_addr) {
  unsigned sv;
  asm volatile("s_mov_b32 %0, m0\n\ts_mov_b32 m0, %3\n\ts_nop 0\n\tglobal_load_lds_dwordx4 %1, %2\n\ts_mov_b32 m0, %0"
               : "=&s"(sv) : "v"(voff), "s"(sbase), "s"(lds_addr) : "memory");
}
__device__ __forceinline__ s16x4 vtr(lds_cptr p) {
  return __builtin_bit_cast(s16x4, __builtin_amdgcn_ds_read_tr16_b64_v4i16((__attribute__((address_space(3))) s16x4*)p));
}
template <int CTRL>
__device__ __forceinline__ float dpp_add(float v) {
  return v + __int_as_float(__builtin_amdgcn_update_dpp(0, __float_as_int(v), CTRL, 0xf, 0xf, false));
}
__device__ __forceinline__ float sum8(float v) {
  v = dpp_add<0xB1>(v);
  v = dpp_add<0x4E>(v);
  return dpp_add<0x141>(v);
}
__device__ __forceinline__ float xsum16_32(float m) {
  auto a = __builtin_amdgcn_permlane32_swap(__float_as_uint(m), __float_as_uint(m), false, false);
  m = __uint_as_float(a[0]) + __uint_as_float(a[1]);
  auto b = __builtin_amdgcn_permlane16_swap(__float_as_uint(m), __float_as_uint(m), false, false);
  return __uint_as_float(b[0]) + __uint_as_float(b[1]);
}
__device__ __forceinline__ float xmax16_32(float m) {
  auto a = __builtin_amdgcn_permlane32_swap(__float_as_uint(m), __float_as_uint(m), false, false);
  m = fmaxf(__uint_as_float(a[0]), __uint_as_float(a[1]));
  auto b = __builtin_amdgcn_permlane16_swap(__float_as_uint(m), __float_as_uint(m), false, false);
  return fmaxf(__uint_as_float(b[0]), __uint_as_float(b[1]));
}
__device__ __forceinline__ float wave_sum(float v) {
  v = sum8(v);
  v = dpp_add<0x140>(v);
  return xsum16_32(v);
}

__global__ __launch_bounds__(256) void prep_kernel(const u16* __restrict__ in, u16* __restrict__ out,
                                                   const char* __restrict__ wqkv, const char* __restrict__ b1,
                                                   const char* __restrict__ b2, unsigned* __restrict__ flags) {
  const int lane = threadIdx.x & 63;
  const int row = blockIdx.x * 4 + (threadIdx.x >> 6);
  const u32x4* src = (const u32x4*)(in + (size_t)row * DM);
  u32x4 a = src[lane], b = src[lane + 64];
  const int gi = blockIdx.x * 256 + threadIdx.x;
  unsigned w0 = 0;
  if (gi < 49152) w0 = *(const unsigned*)(wqkv + (size_t)gi * 128);
  else if (gi < 49152 + 56) w0 = *(const unsigned*)(b1 + (size_t)(gi - 49152) * 128);
  else if (gi < 49152 + 72) w0 = *(const unsigned*)(b2 + (size_t)(gi - 49152 - 56) * 128);
  if (gi < 512) flags[gi * 32] = 0u;
  unsigned w1c = 0;
  if (threadIdx.x < 64 && blockIdx.x < 128) {
    unsigned long long pc, pe;
    asm volatile("s_getpc_b64 %0" : "=s"(pc));
    asm volatile("s_getpc_b64 s[40:41]\n\ts_add_u32 s40, s40, zz_text_end@rel32@lo+4\n\t"
                 "s_addc_u32 s41, s41, zz_text_end@rel32@hi+12\n\ts_mov_b64 %0, s[40:41]"
                 : "=s"(pe) : : "s40", "s41", "scc");
    const unsigned long long a = (pc & ~127ull) + (unsigned long long)((blockIdx.x >> 3) * 64 + lane) * 128;
    if (a + 4 <= pe) w1c = *(const unsigned*)a;
  }
  float v[16];
#pragma unroll
  for (int i = 0; i < 4; ++i) {
    v[2 * i] = bflo(a[i]);
    v[2 * i + 1] = bfhi(a[i]);
    v[8 + 2 * i] = bflo(b[i]);
    v[8 + 2 * i + 1] = bfhi(b[i]);
  }
  float s = 0.f;
#pragma unroll
  for (int i = 0; i < 16; ++i) s += v[i];
  const float mean = wave_sum(s) * (1.0f / DM);
  float q = 0.f;
#pragma unroll
  for (int i = 0; i < 16; ++i) { v[i] -= mean; q += v[i] * v[i]; }
  const float var = wave_sum(q) * (1.0f / DM);
  const float r = 1.0f / sqrtf(var + LN_EPS);
  u32x4 oa, ob;
#pragma unroll
  for (int i = 0; i < 4; ++i) {
    oa[i] = pk2(v[2 * i] * r, v[2 * i + 1] * r);
    ob[i] = pk2(v[8 + 2 * i] * r, v[8 + 2 * i + 1] * r);
  }
  u32x4* dst = (u32x4*)(out + (size_t)row * DM);
  dst[lane] = oa;
  dst[lane + 64] = ob;
  asm volatile("" ::"v"(w0), "v"(w1c));
}

enum { EPI_QKV = 0, EPI_RES_STATS = 1, EPI_LN_GELU = 2, EPI_SPLIT_BIAS_RES = 3 };
constexpr int NLW = 4;

template <int CNT, int NBUF, int NPA, int STAGE, bool CS>
__device__ __forceinline__ void loader_loop(const char* Ab, const char* Wb, const unsigned* voff, int lw, int nk,
                                            unsigned wstep, unsigned lds0, const char* smem, float* csacc) {
  const int lane = threadIdx.x & 63;
  int ca0 = 0, ca1 = 0, cstep = 0;
  if (CS) {
    if (lw < 3) {
      const int k = lane >> 3, c = lane & 7;
      ca0 = NPA * 1024 + lw * 8192 + k * 128 + ((c ^ (k & 2)) << 4);
      ca1 = NPA * 1024 + lw * 8192 + (k + 8) * 128 + ((c ^ ((k & 2) | 4)) << 4);
      cstep = 2048;
    } else {
      const int k = lane >> 2, c = lane & 3;
      ca0 = NPA * 1024 + 3 * 8192 + k * 64 + ((c ^ ((k >> 2) & 2)) << 4);
      ca1 = ca0 + 1024;
      cstep = 2048;
    }
  }
  auto colsum = [&](int buf) {
    const char* base = smem + buf * STAGE;
    const int n = (lw < 3) ? 4 : 2;
#pragma unroll
    for (int i = 0; i < 4; ++i) {
      if (i < n) {
        const u32x4 u = *(const u32x4*)(base + ca0 + i * cstep);
        const u32x4 v = *(const u32x4*)(base + ca1 + i * cstep);
#pragma unroll
        for (int e = 0; e < 4; ++e) {
          csacc[2 * e] += bflo(u[e]) + bflo(v[e]);
          csacc[2 * e + 1] += bfhi(u[e]) + bfhi(v[e]);
        }
      }
    }
  };
  const char* sp[CNT];
  unsigned st[CNT];
#pragma unroll
  for (int i = 0; i < CNT; ++i) {
    const bool isA = (lw + i * NLW) < NPA;
    sp[i] = isA ? Ab : Wb;
    st[i] = isA ? 128u : wstep;
  }
  auto issue = [&](int buf) {
#pragma unroll
    for (int i = 0; i < CNT; ++i) {
      glds16(sp[i], voff[i], lds0 + buf * STAGE + (lw + i * NLW) * 1024);
      sp[i] += st[i];
    }
  };
  issue(0);
  issue(1);
  asm volatile("s_waitcnt vmcnt(%0)" ::"i"(CNT) : "memory");
  __builtin_amdgcn_s_barrier();
#pragma unroll
  for (int s = 2; s < NBUF; ++s) issue(s);
  if (CS) colsum(0);
  int buf = 0;
  for (int t = 0; t < nk; ++t) {
    if (t + NBUF <= nk) {
      asm volatile("s_waitcnt vmcnt(%0)" ::"i"((NBUF - 2) * CNT) : "memory");
    } else {
      asm volatile("s_waitcnt vmcnt(0)" ::: "memory");
    }
    __builtin_amdgcn_s_barrier();
    if (t + NBUF < nk) issue(buf);
    buf = (buf + 1 == NBUF) ? 0 : buf + 1;
    if (CS && t + 1 < nk) colsum(buf);
  }
}

template <int BN, int NBUF, int EPI, int XA>
__device__ __forceinline__ void gemm_body(const u16* __restrict__ A, const u16* __restrict__ W,
                                          u16* __restrict__ C, const u16* __restrict__ bias,
                                          const u16* __restrict__ res, float* __restrict__ aux0,
                                          float* __restrict__ aux1, unsigned* __restrict__ flags,
                                          const int N, const int K) {
  constexpr int BM = 128;
  constexpr bool WIDE = BN > 64;
  constexpr bool SPLIT = (EPI == EPI_SPLIT_BIAS_RES);
  constexpr int NTHR = 512;
  constexpr int NG = BN / 64;
  constexpr bool HALF = (BN % 64) != 0;
  constexpr int NT = WIDE ? BN / 32 : 4;
  constexpr int ABYTES = BM * 128, WBYTES = 64 * BN * 2, STAGE = ABYTES + WBYTES;
  constexpr int NPA = ABYTES / 1024, NP = STAGE / 1024;
  constexpr int CMAX = (NP + NLW - 1) / NLW;
  constexpr int SIDE = NBUF * STAGE;
  extern __shared__ __attribute__((aligned(16))) char smem[];

  const int tid = threadIdx.x, lane = tid & 63;
  const int wid = __builtin_amdgcn_readfirstlane(tid >> 6);
  STAMP(EPI, 0);
  const int xc = blockIdx.x & 7, rr = blockIdx.x >> 3;
  int bm, bn, hk = 0;
  if (SPLIT) {
    hk = rr & 1;
    const int tt = rr >> 1;
    bm = (xc & 3) * 4 + (tt & 3);
    bn = (xc >> 2) * 4 + (tt >> 2);
  } else {
    constexpr int XB = 32 / XA, PM = 16 / XA;
    bm = (xc % PM) * XA + (rr % XA);
    bn = (xc / PM) * XB + (rr / XA);
  }
  const int m0 = bm * BM, n0 = bn * BN;
  const int nk = SPLIT ? K / 128 : K / 64;
  const int kbeg = hk * (K / 2);
  const int odd = HALF ? (bn & 1) : 0;
  const int cfull = odd ? 32 : 0, chalf = odd ? 0 : NG * 64;
  const unsigned lds0 = (unsigned)(uintptr_t)(__attribute__((address_space(3))) char*)smem;
  const int fr = lane & 15, g = lane >> 4;

  f32x4 acc[4][NT];
  const int wm = wid & 1, wx = (wid >> 1) & 1;
  int tcol[NT];

  if (wid >= 4) {
    const int lw = wid - 4;
    unsigned voff[CMAX];
#pragma unroll
    for (int i = 0; i < CMAX; ++i) {
      const int idx = lw + i * NLW;
      if (idx < NPA) {
        const int row = idx * 8 + (lane >> 3), ch = (lane & 7) ^ (row & 7);
        voff[i] = (unsigned)(((m0 + row) * K + ch * 8) * 2);
      } else if (idx < NPA + NG * 8) {
        const int w = idx - NPA, G = w >> 3, k = (w & 7) * 8 + (lane >> 3);
        const int ch = (lane & 7) ^ ((k & 2) | ((k >> 1) & 4));
        voff[i] = (unsigned)((k * N + n0 + cfull + G * 64 + ch * 8) * 2);
      } else {
        const int w = idx - NPA - NG * 8, k = w * 16 + (lane >> 2);
        const int ch = (lane & 3) ^ ((k >> 2) & 2);
        voff[i] = (unsigned)((k * N + n0 + chalf + ch * 8) * 2);
      }
    }
    const unsigned wstep = (unsigned)(64 * N * 2);
    const char* Ab = (const char*)(A + kbeg);
    const char* Wb = (const char*)(W + (size_t)kbeg * N);
    constexpr bool CS = false;
    float csacc[8];
#pragma unroll
    for (int e = 0; e < 8; ++e) csacc[e] = 0.f;
    if (lw + (CMAX - 1) * NLW < NP)
      loader_loop<CMAX, NBUF, NPA, STAGE, CS>(Ab, Wb, voff, lw, nk, wstep, lds0, smem, csacc);
    else
      loader_loop<CMAX - 1, NBUF, NPA, STAGE, CS>(Ab, Wb, voff, lw, nk, wstep, lds0, smem, csacc);
    if (CS) {
#pragma unroll
      for (int e = 0; e < 8; ++e) {
        float t = csacc[e];
        const float t4 = dpp_add<0x124>(t);
        t = (lw == 3) ? t4 : t;
        t = dpp_add<0x128>(t);
        csacc[e] = xsum16_32(t);
      }
      const int nl = (lw < 3) ? 8 : 4;
      if (lane < nl) {
        const int col = ((lw < 3) ? cfull + lw * 64 : chalf) + lane * 8;
#pragma unroll
        for (int e = 0; e < 8; ++e) *(float*)(smem + SIDE + 1024 + (col + e) * 8) = csacc[e];
      }
    }
  } else {
    if (EPI == EPI_LN_GELU) {
      if (tid < 128) {
        const f32x4* sp = (const f32x4*)(aux0 + (size_t)(m0 + tid) * 32);
        f32x4 p[8];
#pragma unroll
        for (int i = 0; i < 8; ++i) p[i] = sp[i];
        float mean = 0.f;
#pragma unroll
        for (int i = 0; i < 8; ++i) mean += p[i][0] + p[i][2];
        mean *= (1.0f / 16.0f);
        float m2 = 0.f;
#pragma unroll
        for (int i = 0; i < 8; ++i) {
          const float d0 = p[i][0] - mean, d1 = p[i][2] - mean;
          m2 += p[i][1] + p[i][3] + 64.0f * (d0 * d0 + d1 * d1);
        }
        const float var = m2 * (1.0f / DM);
        const float r1 = 1.0f / sqrtf(var + LN_EPS);
        const float r2 = 1.0f / sqrtf(var * r1 * r1 + LN_EPS);
        *(f32x2*)(smem + SIDE + tid * 8) = f32x2{mean, r1 * r2};
      } else if (tid - 128 < BN / 2) {
        const int c = (tid - 128) * 2;
        f32x2 cs = {0.f, 0.f};
#pragma unroll
        for (int kc = 0; kc < 32; ++kc) cs += *(const f32x2*)(aux1 + (size_t)kc * FF + n0 + c);
        const unsigned bb = *(const unsigned*)(bias + n0 + c);
        *(f32x4*)(smem + SIDE + 1024 + c * 8) = f32x4{cs[0], bflo(bb), cs[1], bfhi(bb)};
      }
    }
    const int q = (lane >> 2) & 3, pp = lane & 3;
    const int wk = WIDE ? 0 : wx;
    int aoff[2];
#pragma unroll
    for (int ks = 0; ks < 2; ++ks) aoff[ks] = (wm * 64 + fr) * 128 + (((ks * 4 + g) ^ (fr & 7)) << 4);
    int tb[NT];
    const int f1 = (q & 2) | ((g & 1) << 2);
#pragma unroll
    for (int j = 0; j < NT; ++j) {
      int G, tp;
      if (!WIDE) { G = 0; tp = j; }
      else if (j < 4) { G = wx * (NG - 1); tp = j; }
      else { G = 1; tp = wx * 2 + (j - 4); }
      if (WIDE && HALF && j == NT - 1) {
        tb[j] = ABYTES + NG * 8192 + (8 * g + q) * 64 + (((2 * wx + (pp >> 1)) ^ ((g & 1) << 1)) << 4) + (pp & 1) * 8;
        tcol[j] = chalf + wx * 16;
      } else {
        tb[j] = ABYTES + G * 8192 + (8 * g + q) * 128 + (((2 * tp + (pp >> 1)) ^ f1) << 4) + (pp & 1) * 8;
        tcol[j] = cfull + G * 64 + tp * 16;
      }
    }
#pragma unroll
    for (int i = 0; i < 4; ++i)
#pragma unroll
      for (int j = 0; j < NT; ++j) acc[i][j] = f32x4{0.f, 0.f, 0.f, 0.f};

    bf16x8 xa[4], wa[NT], xb[4], wb[NT];
    auto rd = [&](bf16x8* xf, bf16x8* wf, int buf, int ks) {
      const char* base = smem + buf * STAGE;
#pragma unroll
      for (int j = 0; j < NT; ++j) {
        const int rs = (WIDE && HALF && j == NT - 1) ? 64 : 128;
        s16x4 lo = vtr((lds_cptr)(base + tb[j] + ks * 32 * rs));
        s16x4 hi = vtr((lds_cptr)(base + tb[j] + ks * 32 * rs + 4 * rs));
        wf[j] = __builtin_shufflevector(lo, hi, 0, 1, 2, 3, 4, 5, 6, 7);
      }
#pragma unroll
      for (int i = 0; i < 4; ++i) xf[i] = *(const bf16x8*)(base + aoff[ks] + i * 2048);
    };
    auto mm = [&](const bf16x8* xf, const bf16x8* wf) {
#pragma unroll
      for (int i = 0; i < 4; ++i)
#pragma unroll
        for (int j = 0; j < NT; ++j)
          acc[i][j] = __builtin_amdgcn_mfma_f32_16x16x32_bf16(wf[j], xf[i], acc[i][j], 0, 0, 0);
    };
#define INTERLEAVE() do { _Pragma("unroll") for (int z_ = 0; z_ < 2 * NT + 4; ++z_) { __builtin_amdgcn_sched_group_barrier(0x8, 1, 0); __builtin_amdgcn_sched_group_barrier(0x100, 1, 0); } __builtin_amdgcn_sched_group_barrier(0x8, 4 * NT - (2 * NT + 4), 0); } while (0)
    __builtin_amdgcn_s_waitcnt(0xC07F);
    __builtin_amdgcn_s_barrier();
    STAMP(EPI, 1);
    if (WIDE) {
      rd(xa, wa, 0, 0);
      int b0 = 0;
      for (int t = 0; t < nk; ++t) {
        rd(xb, wb, b0, 1);
        b0 = (b0 + 1 == NBUF) ? 0 : b0 + 1;
        mm(xa, wa);
        INTERLEAVE();
        __builtin_amdgcn_sched_barrier(0);
        __builtin_amdgcn_s_waitcnt(0xC07F);
        __builtin_amdgcn_s_barrier();
        rd(xa, wa, b0, 0);
        mm(xb, wb);
        INTERLEAVE();
        __builtin_amdgcn_sched_barrier(0);
      }
    } else {
      rd(xa, wa, 0, wk);
      int b1 = 1;
      for (int t = 0; t < nk; t += 2) {
        __builtin_amdgcn_s_waitcnt(0xC07F);
        __builtin_amdgcn_s_barrier();
        rd(xb, wb, b1, wk);
        b1 = (b1 + 1 == NBUF) ? 0 : b1 + 1;
        mm(xa, wa);
        INTERLEAVE();
        __builtin_amdgcn_sched_barrier(0);
        __builtin_amdgcn_s_waitcnt(0xC07F);
        __builtin_amdgcn_s_barrier();
        rd(xa, wa, b1, wk);
        b1 = (b1 + 1 == NBUF) ? 0 : b1 + 1;
        mm(xb, wb);
        INTERLEAVE();
        __builtin_amdgcn_sched_barrier(0);
      }
    }
    __builtin_amdgcn_s_waitcnt(0xC07F);
    STAMP(EPI, 2);
    if (SPLIT) {
      const int tile = bm * 8 + bn;
      const auto rsrc = __builtin_amdgcn_make_buffer_rsrc(aux0, 0, 128 * 2 * 2 * 16384, 0x00020000);
      if (wm != hk) {
        const int slot = ((tile * 2 + hk) * 2 + wx);
#pragma unroll
        for (int i = 0; i < 4; ++i)
#pragma unroll
          for (int j = 0; j < NT; ++j)
            __builtin_amdgcn_raw_buffer_store_b128(__builtin_bit_cast(u32x4, acc[i][j]), rsrc,
                                                   slot * 16384 + (i * NT + j) * 1024 + lane * 16, 0, 16);
        asm volatile("s_waitcnt vmcnt(0)" ::: "memory");
        if (lane == 0) __hip_atomic_store(flags + slot * 32, 1u, __ATOMIC_RELAXED, __HIP_MEMORY_SCOPE_AGENT);
      } else {
        const int slot = ((tile * 2 + (hk ^ 1)) * 2 + wx);
        for (int spin = 0; spin < (1 << 24); ++spin) {
          if (__hip_atomic_load(flags + slot * 32, __ATOMIC_RELAXED, __HIP_MEMORY_SCOPE_AGENT) != 0u) break;
          __builtin_amdgcn_s_sleep(2);
        }
        asm volatile("" ::: "memory");
#pragma unroll
        for (int i = 0; i < 4; ++i) {
          u32x4 t[NT];
#pragma unroll
          for (int j = 0; j < NT; ++j)
            t[j] = __builtin_amdgcn_raw_buffer_load_b128(rsrc, slot * 16384 + (i * NT + j) * 1024 + lane * 16, 0, 16);
#pragma unroll
          for (int j = 0; j < NT; ++j) acc[i][j] += __builtin_bit_cast(f32x4, t[j]);
        }
      }
    }
  }
  __syncthreads();
  STAMP(EPI, 3);

  constexpr int CPR = BN / 8;
  if (EPI == EPI_QKV) {
    constexpr int RS = BN * 2 + 16;
    if (wid < 4) {
#pragma unroll
      for (int j = 0; j < NT; ++j)
#pragma unroll
        for (int i = 0; i < 4; ++i) {
          u32x2 o;
          o[0] = pk2(acc[i][j][0], acc[i][j][1]);
          o[1] = pk2(acc[i][j][2], acc[i][j][3]);
          *(u32x2*)(smem + (wm * 64 + i * 16 + fr) * RS + (tcol[j] + g * 4) * 2) = o;
        }
    }
    __syncthreads();
#pragma unroll
    for (int c = tid; c < BM * CPR; c += NTHR) {
      const int hs = c >> 9, row = (c >> 2) & 127, c4 = c & 3;
      u32x4 v = *(const u32x4*)(smem + row * RS + (hs * 4 + c4) * 16);
      ST16(C + ((size_t)((n0 >> 5) + hs) * S_LEN + m0 + row) * 32 + c4 * 8, v);
    }
  } else if (EPI == EPI_LN_GELU) {
    constexpr int RS = BN * 4 + 16;
    if (wid < 4) {
#pragma unroll
      for (int j = 0; j < NT; ++j)
#pragma unroll
        for (int i = 0; i < 4; ++i)
          *(f32x4*)(smem + (wm * 64 + i * 16 + fr) * RS + (tcol[j] + g * 4) * 4) = acc[i][j];
    }
    __syncthreads();
#pragma unroll
    for (int c = tid; c < BM * CPR; c += NTHR) {
      const int row = c / CPR, ch = c % CPR;
      const f32x4 a0 = *(const f32x4*)(smem + row * RS + ch * 32);
      const f32x4 a1 = *(const f32x4*)(smem + row * RS + ch * 32 + 16);
      const f32x2 rs = *(const f32x2*)(smem + SIDE + row * 8);
      float v[8];
#pragma unroll
      for (int r = 0; r < 4; ++r) {
        const f32x4 cb = *(const f32x4*)(smem + SIDE + 1024 + ch * 64 + r * 16);
        const float x0 = (r < 2) ? a0[2 * r] : a1[2 * r - 4];
        const float x1 = (r < 2) ? a0[2 * r + 1] : a1[2 * r - 3];
        v[2 * r] = (x0 - rs[0] * cb[0]) * rs[1] + cb[1];
        v[2 * r + 1] = (x1 - rs[0] * cb[2]) * rs[1] + cb[3];
      }
      u32x4 o;
#pragma unroll
      for (int r = 0; r < 4; ++r) {
        const f32x2 x = {v[2 * r], v[2 * r + 1]};
        const f32x2 x2 = x * x;
        const f32x2 w = x2 * f32x2{-0.10294324f, -0.10294324f} + f32x2{-2.302208198f, -2.302208198f};
        const f32x2 z = x * w;
        f32x2 e = {__builtin_amdgcn_exp2f(z[0]), __builtin_amdgcn_exp2f(z[1])};
        e = e + f32x2{1.0f, 1.0f};
        const f32x2 y = x * f32x2{__builtin_amdgcn_rcpf(e[0]), __builtin_amdgcn_rcpf(e[1])};
        o[r] = pk2(y[0], y[1]);
      }
      ST16(C + (size_t)(m0 + row) * N + n0 + ch * 8, o);
    }
  } else if (EPI == EPI_RES_STATS) {
    constexpr int RS = BN * 4 + 16;
    constexpr int PART = BM * RS;
    if (wid < 4) {
#pragma unroll
      for (int i = 0; i < 4; ++i)
#pragma unroll
        for (int j = 0; j < NT; ++j)
          *(f32x4*)(smem + wx * PART + (wm * 64 + i * 16 + fr) * RS + (j * 16 + g * 4) * 4) = acc[i][j];
    }
    __syncthreads();
#pragma unroll
    for (int c = tid; c < BM * CPR; c += NTHR) {
      const int row = c / CPR, ch = c % CPR;
      const f32x4 a0 = *(const f32x4*)(smem + row * RS + ch * 32);
      const f32x4 a1 = *(const f32x4*)(smem + row * RS + ch * 32 + 16);
      const f32x4 b0 = *(const f32x4*)(smem + PART + row * RS + ch * 32);
      const f32x4 b1 = *(const f32x4*)(smem + PART + row * RS + ch * 32 + 16);
      const u32x4 rv = *(const u32x4*)(res + (size_t)(m0 + row) * N + n0 + ch * 8);
      u32x4 o;
      o[0] = pk2(a0[0] + b0[0] + bflo(rv[0]), a0[1] + b0[1] + bfhi(rv[0]));
      o[1] = pk2(a0[2] + b0[2] + bflo(rv[1]), a0[3] + b0[3] + bfhi(rv[1]));
      o[2] = pk2(a1[0] + b1[0] + bflo(rv[2]), a1[1] + b1[1] + bfhi(rv[2]));
      o[3] = pk2(a1[2] + b1[2] + bflo(rv[3]), a1[3] + b1[3] + bfhi(rv[3]));
      ST16(C + (size_t)(m0 + row) * N + n0 + ch * 8, o);
      float s = 0.f;
#pragma unroll
      for (int r = 0; r < 4; ++r) s += bflo(o[r]) + bfhi(o[r]);
      const float mean = sum8(s) * (1.0f / 64.0f);
      float m2 = 0.f;
#pragma unroll
      for (int r = 0; r < 4; ++r) {
        const float d0 = bflo(o[r]) - mean, d1 = bfhi(o[r]) - mean;
        m2 += d0 * d0 + d1 * d1;
      }
      m2 = sum8(m2);
      if (ch == 0) *(f32x2*)(aux0 + ((size_t)(m0 + row) * 16 + bn) * 2) = f32x2{mean, m2};
    }
  } else {
    constexpr int RS = BN * 4 + 16;
    if (wid < 4 && wm == hk) {
#pragma unroll
      for (int i = 0; i < 4; ++i)
#pragma unroll
        for (int j = 0; j < NT; ++j)
          *(f32x4*)(smem + (i * 16 + fr) * RS + (tcol[j] + g * 4) * 4) = acc[i][j];
    }
    __syncthreads();
#pragma unroll
    for (int c = tid; c < 64 * CPR; c += NTHR) {
      const int row = c / CPR, ch = c % CPR;
      const f32x4 a0 = *(const f32x4*)(smem + row * RS + ch * 32);
      const f32x4 a1 = *(const f32x4*)(smem + row * RS + ch * 32 + 16);
      const u32x4 bb = *(const u32x4*)(bias + n0 + ch * 8);
      const size_t go = (size_t)(m0 + hk * 64 + row) * N + n0 + ch * 8;
      const u32x4 rv = *(const u32x4*)(res + go);
      u32x4 o;
      o[0] = pk2(a0[0] + bflo(bb[0]) + bflo(rv[0]), a0[1] + bfhi(bb[0]) + bfhi(rv[0]));
      o[1] = pk2(a0[2] + bflo(bb[1]) + bflo(rv[1]), a0[3] + bfhi(bb[1]) + bfhi(rv[1]));
      o[2] = pk2(a1[0] + bflo(bb[2]) + bflo(rv[2]), a1[1] + bfhi(bb[2]) + bfhi(rv[2]));
      o[3] = pk2(a1[2] + bflo(bb[3]) + bflo(rv[3]), a1[3] + bfhi(bb[3]) + bfhi(rv[3]));
      ST16(C + go, o);
    }
  }
}

__device__ __forceinline__ float rowmax17(const f32x4* s) {
  float a = fmaxf(fmaxf(s[0][0], s[0][1]), fmaxf(s[0][2], s[0][3]));
#pragma unroll
  for (int t = 1; t < 17; ++t) {
    a = fmaxf(fmaxf(a, s[t][0]), s[t][1]);
    a = fmaxf(fmaxf(a, s[t][2]), s[t][3]);
  }
  return a;
}

template <bool EARLY>
__device__ __forceinline__ void attn_body(const char* ks, const char* vs, bf16x8 qf, int wid, int lane, int tmin,
                                          u16* __restrict__ op) {
  const int fr = lane & 15, g = lane >> 4;
  const float NEG = -1e30f;
  f32x4 s[17];
  const char* kp = ks + (wid * 16 + fr) * 64 + ((g ^ ((4 - ((fr >> 2) & 3)) & 3)) << 4);
#pragma unroll
  for (int t = 0; t < 17; ++t) {
    bf16x8 kf = *(const bf16x8*)(kp + t * 1024);
    s[t] = __builtin_amdgcn_mfma_f32_16x16x32_bf16(kf, qf, f32x4{0.f, 0.f, 0.f, 0.f}, 0, 0, 0);
  }
#pragma unroll
  for (int r = 0; r < 4; ++r) {
    s[0][r] = (4 * g + r > fr) ? s[0][r] : NEG;
    s[16][r] = (4 * g + r <= fr) ? s[16][r] : NEG;
  }
  if (EARLY) {
#pragma unroll
    for (int t = 0; t < 16; ++t)
      if (t < tmin) s[t] = f32x4{NEG, NEG, NEG, NEG};
  }
  const float mx = xmax16_32(rowmax17(s));
  const float c = 0.17677669529663687f * 1.4426950408889634f;
  const f32x2 c2 = {c, c};
  const f32x2 nmc2 = {-mx * c, -mx * c};
  f32x2 sum2 = {0.f, 0.f};
  unsigned pk[18][2];
#pragma unroll
  for (int t = 0; t < 17; ++t) {
#pragma unroll
    for (int hh = 0; hh < 2; ++hh) {
      const f32x2 z = f32x2{s[t][2 * hh], s[t][2 * hh + 1]} * c2 + nmc2;
      const f32x2 p = {__builtin_amdgcn_exp2f(z[0]), __builtin_amdgcn_exp2f(z[1])};
      sum2 += p;
      pk[t][hh] = pk2(p[0], p[1]);
    }
  }
  float sum = sum2[0] + sum2[1];
  pk[17][0] = 0u;
  pk[17][1] = 0u;
  sum = xsum16_32(sum);
  const int q4 = (lane >> 2) & 3, pp = lane & 3;
  const char* vp0 = vs + (wid * 16 + 4 * g + q4) * 64 + ((g & 1) << 5) + pp * 8;
  const char* vp1 = vs + (wid * 16 + 4 * g + q4) * 64 + (((g & 1) ^ 1) << 5) + pp * 8;
  f32x4 o0 = {0.f, 0.f, 0.f, 0.f}, o1 = {0.f, 0.f, 0.f, 0.f};
#pragma unroll
  for (int tp = 0; tp < 9; ++tp) {
    const int t0 = 2 * tp, t1 = (2 * tp + 1 < 17) ? 2 * tp + 1 : 16;
    u32x4 pb = {pk[2 * tp][0], pk[2 * tp][1], pk[2 * tp + 1][0], pk[2 * tp + 1][1]};
    bf16x8 pf = __builtin_bit_cast(bf16x8, pb);
    s16x4 a0 = vtr((lds_cptr)(vp0 + t0 * 1024)), a1 = vtr((lds_cptr)(vp0 + t1 * 1024));
    s16x4 b0 = vtr((lds_cptr)(vp1 + t0 * 1024)), b1 = vtr((lds_cptr)(vp1 + t1 * 1024));
    o0 = __builtin_amdgcn_mfma_f32_16x16x32_bf16(__builtin_shufflevector(a0, a1, 0, 1, 2, 3, 4, 5, 6, 7), pf, o0, 0, 0, 0);
    o1 = __builtin_amdgcn_mfma_f32_16x16x32_bf16(__builtin_shufflevector(b0, b1, 0, 1, 2, 3, 4, 5, 6, 7), pf, o1, 0, 0, 0);
  }
  const float inv = 1.0f / sum;
  u32x2 w0, w1;
  w0[0] = pk2(o0[0] * inv, o0[1] * inv);
  w0[1] = pk2(o0[2] * inv, o0[3] * inv);
  w1[0] = pk2(o1[0] * inv, o1[1] * inv);
  w1[1] = pk2(o1[2] * inv, o1[3] * inv);
  *(u32x2*)(op + 4 * g) = w0;
  *(u32x2*)(op + 16 + 4 * g) = w1;
}

__global__ __launch_bounds__(512) void attn_kernel(const u16* __restrict__ qkv, u16* __restrict__ o,
                                                   const char* __restrict__ wo, const u16* __restrict__ w1,
                                                   const char* __restrict__ w2, float* __restrict__ cspart) {
  constexpr int QB = 128, NKEY = WIN + QB;
  __shared__ __attribute__((aligned(16))) char smem[2 * NKEY * 64];
  const int tid = threadIdx.x, lane = tid & 63;
  const int wid = __builtin_amdgcn_readfirstlane(tid >> 6);
  const int bid = blockIdx.x;
  const int head = bid & 31, qb = bid >> 5;
  const int q0 = qb * QB, kbase = q0 - WIN;
  const u16* qh = qkv + (size_t)head * S_LEN * 32;
  const u16* kh = qkv + (size_t)(NH + head) * S_LEN * 32;
  const u16* vh = qkv + (size_t)(2 * NH + head) * S_LEN * 32;
  const unsigned lds0 = (unsigned)(uintptr_t)(__attribute__((address_space(3))) char*)smem;
#pragma unroll
  for (int i = 0; i < 3; ++i) {
    const int piece = wid * 3 + i;
    const int key = piece * 16 + (lane >> 2), slot = lane & 3;
    int kj = kbase + key;
    kj = kj < 0 ? 0 : kj;
    const int ck = slot ^ ((4 - ((key >> 2) & 3)) & 3);
    const int cv = slot ^ (((key >> 2) & 1) << 1);
    glds16(kh, (unsigned)((kj * 32 + ck * 8) * 2), lds0 + piece * 1024);
    glds16(vh, (unsigned)((kj * 32 + cv * 8) * 2), lds0 + NKEY * 64 + piece * 1024);
  }
  const int fr = lane & 15, g = lane >> 4;
  const int qw = q0 + wid * 16;
  bf16x8 qf = *(const bf16x8*)(qh + (size_t)(qw + fr) * 32 + g * 8);
  __builtin_amdgcn_s_waitcnt(0x0F70);
  asm volatile("" ::: "memory");
  unsigned touch = 0;
  if (wid < 3) {
    const int line = (bid * 3 + wid) * 64 + lane;
    if (line < 73728) {
      const char* p = line < 16384 ? wo + (size_t)line * 128 : w2 + (size_t)(line - 16384) * 128;
      asm volatile("global_load_dword %0, %1, off" : "+v"(touch) : "v"(p) : "memory");
    }
  }
  const int cb = bid < 448 ? bid : 447;
  const int cg = cb % 14, kc = cb / 14;
  const u16* cp = w1 + (size_t)(kc * 32 + (tid >> 5) * 2) * FF + cg * 256 + (tid & 31) * 8;
  const u32x4 cw0 = *(const u32x4*)cp;
  const u32x4 cw1 = *(const u32x4*)(cp + FF);
  __builtin_amdgcn_s_barrier();
  u16* op = o + (size_t)(qw + fr) * DM + head * DH;
  if (q0 < WIN) {
    const int tmin = (WIN - qw) >> 4;
    attn_body<true>(smem, smem + NKEY * 64, qf, wid, lane, tmin, op);
  } else {
    attn_body<false>(smem, smem + NKEY * 64, qf, wid, lane, 0, op);
  }
  if (bid < 448) {
    __syncthreads();
    float* red = (float*)smem;
    f32x4 lo, hi;
#pragma unroll
    for (int e = 0; e < 2; ++e) {
      lo[2 * e] = bflo(cw0[e]) + bflo(cw1[e]);
      lo[2 * e + 1] = bfhi(cw0[e]) + bfhi(cw1[e]);
      hi[2 * e] = bflo(cw0[2 + e]) + bflo(cw1[2 + e]);
      hi[2 * e + 1] = bfhi(cw0[2 + e]) + bfhi(cw1[2 + e]);
    }
    *(f32x4*)(red + (tid >> 5) * 256 + (tid & 31) * 8) = lo;
    *(f32x4*)(red + (tid >> 5) * 256 + (tid & 31) * 8 + 4) = hi;
    __syncthreads();
    if (tid < 256) {
      float t = 0.f;
#pragma unroll
      for (int i = 0; i < 16; ++i) t += red[i * 256 + tid];
      cspart[(size_t)kc * FF + cg * 256 + tid] = t;
    }
  }
  asm volatile("s_waitcnt vmcnt(0)" : "+v"(touch)::"memory");
}

__global__ __launch_bounds__(512, 1) void gemm_qkv_kernel(const u16* __restrict__ A, const u16* __restrict__ W,
                                                         u16* __restrict__ C) {
  gemm_body<192, 4, EPI_QKV, 8>(A, W, C, nullptr, nullptr, nullptr, nullptr, nullptr, 3 * DM, DM);
}
__global__ __launch_bounds__(512, 1) void gemm_wo_kernel(const u16* __restrict__ A, const u16* __restrict__ W,
                                                        u16* __restrict__ C, const u16* __restrict__ res,
                                                        float* __restrict__ stats) {
  gemm_body<64, 6, EPI_RES_STATS, 4>(A, W, C, nullptr, res, stats, nullptr, nullptr, DM, DM);
}
__global__ __launch_bounds__(512, 1) void gemm_w1_kernel(const u16* __restrict__ A, const u16* __restrict__ W,
                                                        u16* __restrict__ C, const u16* __restrict__ bias,
                                                        float* __restrict__ stats, float* __restrict__ cspart) {
  gemm_body<224, 3, EPI_LN_GELU, 8>(A, W, C, bias, nullptr, stats, cspart, nullptr, FF, DM);
}
__global__ __launch_bounds__(512, 1) void gemm_w2_kernel(const u16* __restrict__ A, const u16* __restrict__ W,
                                                        u16* __restrict__ C, const u16* __restrict__ bias,
                                                        const u16* __restrict__ res, float* __restrict__ xchg,
                                                        unsigned* __restrict__ flags) {
  gemm_body<128, 4, EPI_SPLIT_BIAS_RES, 4>(A, W, C, bias, res, xchg, nullptr, flags, DM, FF);
}
extern "C" __global__ void zz_text_end() {}

constexpr int lds_bytes(int BN, int NBUF, bool side) { return NBUF * (128 * 64 * 2 + 64 * BN * 2) + (side ? 1024 + BN * 8 : 0); }

extern "C" void kernel_launch(void* const* d_in, const int* in_sizes, int n_in, void* d_out, int out_size,
                              void* d_ws, size_t ws_size, hipStream_t stream) {
  const u16* x = (const u16*)d_in[0];
  const u16* wqkv = (const u16*)d_in[1];
  const u16* wo = (const u16*)d_in[2];
  const u16* w1 = (const u16*)d_in[4];
  const u16* b1 = (const u16*)d_in[5];
  const u16* w2 = (const u16*)d_in[6];
  const u16* b2 = (const u16*)d_in[7];
  u16* out = (u16*)d_out;
  char* ws = (char*)d_ws;
  u16* xn = (u16*)(ws);
  u16* qkv = (u16*)(ws + (4u << 20));
  u16* o = (u16*)(ws + (16u << 20));
  u16* h = (u16*)(ws + (20u << 20));
  u16* hh = (u16*)(ws + (28u << 20));
  float* stats = (float*)(ws + (44u << 20));
  float* cspart = (float*)(ws + (45u << 20));
  unsigned* flags = (unsigned*)(ws + (46u << 20));
  float* xchg = (float*)(ws + (48u << 20));

  (void)hipFuncSetAttribute((const void*)gemm_qkv_kernel, hipFuncAttributeMaxDynamicSharedMemorySize, lds_bytes(192, 4, false));
  (void)hipFuncSetAttribute((const void*)gemm_wo_kernel, hipFuncAttributeMaxDynamicSharedMemorySize, lds_bytes(64, 6, false));
  (void)hipFuncSetAttribute((const void*)gemm_w1_kernel, hipFuncAttributeMaxDynamicSharedMemorySize, lds_bytes(224, 3, true));
  (void)hipFuncSetAttribute((const void*)gemm_w2_kernel, hipFuncAttributeMaxDynamicSharedMemorySize, lds_bytes(128, 4, false));
  prep_kernel<<<S_LEN / 4, 256, 0, stream>>>(x, xn, (const char*)wqkv, (const char*)b1, (const char*)b2, flags);
  gemm_qkv_kernel<<<256, 512, lds_bytes(192, 4, false), stream>>>(xn, wqkv, qkv);
  attn_kernel<<<NH * (S_LEN / 128), 512, 0, stream>>>(qkv, o, (const char*)wo, w1, (const char*)w2, cspart);
  gemm_wo_kernel<<<256, 512, lds_bytes(64, 6, false), stream>>>(o, wo, h, x, stats);
  gemm_w1_kernel<<<256, 512, lds_bytes(224, 3, true), stream>>>(h, w1, hh, b1, stats, cspart);
  gemm_w2_kernel<<<256, 512, lds_bytes(128, 4, false), stream>>>(hh, w2, out, b2, h, xchg, flags);
}
```

```cpp
#include <hip/hip_runtime.h>
#include <hip/hip_bf16.h>
#include <stdint.h>

typedef unsigned short u16;
typedef __attribute__((ext_vector_type(8))) short bf16x8;
typedef __attribute__((ext_vector_type(4))) short s16x4;
typedef __attribute__((ext_vector_type(4))) float f32x4;
typedef __attribute__((ext_vector_type(2))) float f32x2;
typedef __attribute__((ext_vector_type(4))) unsigned u32x4;
typedef __attribute__((ext_vector_type(2))) unsigned u32x2;
typedef __attribute__((ext_vector_type(2))) __bf16 bf16x2_t;
typedef __attribute__((address_space(3))) const char* lds_cptr;

#define STAMP(kid, i)
__device__ __forceinline__ void st16_sc1(void* p, __attribute__((ext_vector_type(4))) unsigned v) { asm volatile("global_store_dwordx4 %0, %1, off sc1\n\ts_nop 1" :: "v"(p), "v"(v) : "memory"); }
#define ST16(p, v) st16_sc1((void*)(p), (v))
constexpr int S_LEN = 2048, DM = 1024, NH = 32, DH = 32, WIN = 256, FF = 3584;
constexpr float LN_EPS = 1e-5f;

__device__ __forceinline__ float bf2f(u16 v) { return __uint_as_float(((unsigned)v) << 16); }
__device__ __forceinline__ float bflo(unsigned v) { return __uint_as_float(v << 16); }
__device__ __forceinline__ float bfhi(unsigned v) { return __uint_as_float(v & 0xffff0000u); }
__device__ __forceinline__ unsigned pk2(float lo, float hi) {
  return __builtin_bit_cast(unsigned, __builtin_convertvector(f32x2{lo, hi}, bf16x2_t));
}
__device__ __forceinline__ void glds16(const void* sbase, unsigned voff, unsigned lds_addr) {
  unsigned sv;
  asm volatile("s_mov_b32 %0, m0\n\ts_mov_b32 m0, %3\n\ts_nop 0\n\tglobal_load_lds_dwordx4 %1, %2\n\ts_mov_b32 m0, %0"
               : "=&s"(sv) : "v"(voff), "s"(sbase), "s"(lds_addr) : "memory");
}
__device__ __forceinline__ s16x4 vtr(lds_cptr p) {
  return __builtin_bit_cast(s16x4, __builtin_amdgcn_ds_read_tr16_b64_v4i16((__attribute__((address_space(3))) s16x4*)p));
}
template <int CTRL>
__device__ __forceinline__ float dpp_add(float v) {
  return v + __int_as_float(__builtin_amdgcn_update_dpp(0, __float_as_int(v), CTRL, 0xf, 0xf, false));
}
__device__ __forceinline__ float sum8(float v) {
  v = dpp_add<0xB1>(v);
  v = dpp_add<0x4E>(v);
  return dpp_add<0x141>(v);
}
__device__ __forceinline__ float xsum16_32(float m) {
  auto a = __builtin_amdgcn_permlane32_swap(__float_as_uint(m), __float_as_uint(m), false, false);
  m = __uint_as_float(a[0]) + __uint_as_float(a[1]);
  auto b = __builtin_amdgcn_permlane16_swap(__float_as_uint(m), __float_as_uint(m), false, false);
  return __uint_as_float(b[0]) + __uint_as_float(b[1]);
}
__device__ __forceinline__ float xmax16_32(float m) {
  auto a = __builtin_amdgcn_permlane32_swap(__float_as_uint(m), __float_as_uint(m), false, false);
  m = fmaxf(__uint_as_float(a[0]), __uint_as_float(a[1]));
  auto b = __builtin_amdgcn_permlane16_swap(__float_as_uint(m), __float_as_uint(m), false, false);
  return fmaxf(__uint_as_float(b[0]), __uint_as_float(b[1]));
}
__device__ __forceinline__ float wave_sum(float v) {
  v = sum8(v);
  v = dpp_add<0x140>(v);
  return xsum16_32(v);
}

__global__ __launch_bounds__(256) void prep_kernel(const u16* __restrict__ in, u16* __restrict__ out,
                                                   const char* __restrict__ wqkv, const char* __restrict__ b1,
                                                   const char* __restrict__ b2, unsigned* __restrict__ flags) {
  const int lane = threadIdx.x & 63;
  const int row = blockIdx.x * 4 + (threadIdx.x >> 6);
  const u32x4* src = (const u32x4*)(in + (size_t)row * DM);
  u32x4 a = src[lane], b = src[lane + 64];
  const int gi = blockIdx.x * 256 + threadIdx.x;
  unsigned w0 = 0;
  if (gi < 49152) w0 = *(const unsigned*)(wqkv + (size_t)gi * 128);
  else if (gi < 49152 + 56) w0 = *(const unsigned*)(b1 + (size_t)(gi - 49152) * 128);
  else if (gi < 49152 + 72) w0 = *(const unsigned*)(b2 + (size_t)(gi - 49152 - 56) * 128);
  if (gi < 512) flags[gi * 32] = 0u;
  unsigned w1c = 0;
  if (threadIdx.x < 64 && blockIdx.x < 128) {
    unsigned long long pc, pe;
    asm volatile("s_getpc_b64 %0" : "=s"(pc));
    asm volatile("s_getpc_b64 s[40:41]\n\ts_add_u32 s40, s40, zz_text_end@rel32@lo+4\n\t"
                 "s_addc_u32 s41, s41, zz_text_end@rel32@hi+12\n\ts_mov_b64 %0, s[40:41]"
                 : "=s"(pe) : : "s40", "s41", "scc");
    const unsigned long long a = (pc & ~127ull) + (unsigned long long)((blockIdx.x >> 3) * 64 + lane) * 128;
    if (a + 4 <= pe) w1c = *(const unsigned*)a;
  }
  float v[16];
#pragma unroll
  for (int i = 0; i < 4; ++i) {
    v[2 * i] = bflo(a[i]);
    v[2 * i + 1] = bfhi(a[i]);
    v[8 + 2 * i] = bflo(b[i]);
    v[8 + 2 * i + 1] = bfhi(b[i]);
  }
  float s = 0.f;
#pragma unroll
  for (int i = 0; i < 16; ++i) s += v[i];
  const float mean = wave_sum(s) * (1.0f / DM);
  float q = 0.f;
#pragma unroll
  for (int i = 0; i < 16; ++i) { v[i] -= mean; q += v[i] * v[i]; }
  const float var = wave_sum(q) * (1.0f / DM);
  const float r = 1.0f / sqrtf(var + LN_EPS);
  u32x4 oa, ob;
#pragma unroll
  for (int i = 0; i < 4; ++i) {
    oa[i] = pk2(v[2 * i] * r, v[2 * i + 1] * r);
    ob[i] = pk2(v[8 + 2 * i] * r, v[8 + 2 * i + 1] * r);
  }
  u32x4* dst = (u32x4*)(out + (size_t)row * DM);
  dst[lane] = oa;
  dst[lane + 64] = ob;
  asm volatile("" ::"v"(w0), "v"(w1c));
}

enum { EPI_QKV = 0, EPI_RES_STATS = 1, EPI_LN_GELU = 2, EPI_SPLIT_BIAS_RES = 3 };
constexpr int NLW = 4;

template <int CNT, int NBUF, int NPA, int STAGE, bool CS>
__device__ __forceinline__ void loader_loop(const char* Ab, const char* Wb, const unsigned* voff, int lw, int nk,
                                            unsigned wstep, unsigned lds0, const char* smem, float* csacc) {
  const int lane = threadIdx.x & 63;
  int ca0 = 0, ca1 = 0, cstep = 0;
  if (CS) {
    if (lw < 3) {
      const int k = lane >> 3, c = lane & 7;
      ca0 = NPA * 1024 + lw * 8192 + k * 128 + ((c ^ (k & 2)) << 4);
      ca1 = NPA * 1024 + lw * 8192 + (k + 8) * 128 + ((c ^ ((k & 2) | 4)) << 4);
      cstep = 2048;
    } else {
      const int k = lane >> 2, c = lane & 3;
      ca0 = NPA * 1024 + 3 * 8192 + k * 64 + ((c ^ ((k >> 2) & 2)) << 4);
      ca1 = ca0 + 1024;
      cstep = 2048;
    }
  }
  auto colsum = [&](int buf) {
    const char* base = smem + buf * STAGE;
    const int n = (lw < 3) ? 4 : 2;
#pragma unroll
    for (int i = 0; i < 4; ++i) {
      if (i < n) {
        const u32x4 u = *(const u32x4*)(base + ca0 + i * cstep);
        const u32x4 v = *(const u32x4*)(base + ca1 + i * cstep);
#pragma unroll
        for (int e = 0; e < 4; ++e) {
          csacc[2 * e] += bflo(u[e]) + bflo(v[e]);
          csacc[2 * e + 1] += bfhi(u[e]) + bfhi(v[e]);
        }
      }
    }
  };
  const char* sp[CNT];
  unsigned st[CNT];
#pragma unroll
  for (int i = 0; i < CNT; ++i) {
    const bool isA = (lw + i * NLW) < NPA;
    sp[i] = isA ? Ab : Wb;
    st[i] = isA ? 128u : wstep;
  }
  auto issue = [&](int buf) {
#pragma unroll
    for (int i = 0; i < CNT; ++i) {
      glds16(sp[i], voff[i], lds0 + buf * STAGE + (lw + i * NLW) * 1024);
      sp[i] += st[i];
    }
  };
  issue(0);
  issue(1);
  asm volatile("s_waitcnt vmcnt(%0)" ::"i"(CNT) : "memory");
  __builtin_amdgcn_s_barrier();
#pragma unroll
  for (int s = 2; s < NBUF; ++s) issue(s);
  if (CS) colsum(0);
  int buf = 0;
  for (int t = 0; t < nk; ++t) {
    if (t + NBUF <= nk) {
      asm volatile("s_waitcnt vmcnt(%0)" ::"i"((NBUF - 2) * CNT) : "memory");
    } else {
      asm volatile("s_waitcnt vmcnt(0)" ::: "memory");
    }
    __builtin_amdgcn_s_barrier();
    if (t + NBUF < nk) issue(buf);
    buf = (buf + 1 == NBUF) ? 0 : buf + 1;
    if (CS && t + 1 < nk) colsum(buf);
  }
}

template <int BN, int NBUF, int EPI, int XA>
__device__ __forceinline__ void gemm_body(const u16* __restrict__ A, const u16* __restrict__ W,
                                          u16* __restrict__ C, const u16* __restrict__ bias,
                                          const u16* __restrict__ res, float* __restrict__ aux0,
                                          float* __restrict__ aux1, unsigned* __restrict__ flags,
                                          const int N, const int K) {
  constexpr int BM = 128;
  constexpr bool WIDE = BN > 64;
  constexpr bool SPLIT = (EPI == EPI_SPLIT_BIAS_RES);
  constexpr int NTHR = 512;
  constexpr int NG = BN / 64;
  constexpr bool HALF = (BN % 64) != 0;
  constexpr int NT = WIDE ? BN / 32 : 4;
  constexpr int ABYTES = BM * 128, WBYTES = 64 * BN * 2, STAGE = ABYTES + WBYTES;
  constexpr int NPA = ABYTES / 1024, NP = STAGE / 1024;
  constexpr int CMAX = (NP + NLW - 1) / NLW;
  constexpr int SIDE = NBUF * STAGE;
  extern __shared__ __attribute__((aligned(16))) char smem[];

  const int tid = threadIdx.x, lane = tid & 63;
  const int wid = __builtin_amdgcn_readfirstlane(tid >> 6);
  STAMP(EPI, 0);
  const int xc = blockIdx.x & 7, rr = blockIdx.x >> 3;
  int bm, bn, hk = 0;
  if (SPLIT) {
    hk = rr & 1;
    const int tt = rr >> 1;
    bm = (xc & 3) * 4 + (tt & 3);
    bn = (xc >> 2) * 4 + (tt >> 2);
  } else {
    constexpr int XB = 32 / XA, PM = 16 / XA;
    bm = (xc % PM) * XA + (rr % XA);
    bn = (xc / PM) * XB + (rr / XA);
  }
  const int m0 = bm * BM, n0 = bn * BN;
  const int nk = SPLIT ? K / 128 : K / 64;
  const int kbeg = hk * (K / 2);
  const int odd = HALF ? (bn & 1) : 0;
  const int cfull = odd ? 32 : 0, chalf = odd ? 0 : NG * 64;
  const unsigned lds0 = (unsigned)(uintptr_t)(__attribute__((address_space(3))) char*)smem;
  const int fr = lane & 15, g = lane >> 4;

  f32x4 acc[4][NT];
  const int wm = wid & 1, wx = (wid >> 1) & 1;
  int tcol[NT];

  if (wid >= 4) {
    const int lw = wid - 4;
    unsigned voff[CMAX];
#pragma unroll
    for (int i = 0; i < CMAX; ++i) {
      const int idx = lw + i * NLW;
      if (idx < NPA) {
        const int row = idx * 8 + (lane >> 3), ch = (lane & 7) ^ (row & 7);
        voff[i] = (unsigned)(((m0 + row) * K + ch * 8) * 2);
      } else if (idx < NPA + NG * 8) {
        const int w = idx - NPA, G = w >> 3, k = (w & 7) * 8 + (lane >> 3);
        const int ch = (lane & 7) ^ ((k & 2) | ((k >> 1) & 4));
        voff[i] = (unsigned)((k * N + n0 + cfull + G * 64 + ch * 8) * 2);
      } else {
        const int w = idx - NPA - NG * 8, k = w * 16 + (lane >> 2);
        const int ch = (lane & 3) ^ ((k >> 2) & 2);
        voff[i] = (unsigned)((k * N + n0 + chalf + ch * 8) * 2);
      }
    }
    const unsigned wstep = (unsigned)(64 * N * 2);
    const char* Ab = (const char*)(A + kbeg);
    const char* Wb = (const char*)(W + (size_t)kbeg * N);
    constexpr bool CS = false;
    float csacc[8];
#pragma unroll
    for (int e = 0; e < 8; ++e) csacc[e] = 0.f;
    if (lw + (CMAX - 1) * NLW < NP)
      loader_loop<CMAX, NBUF, NPA, STAGE, CS>(Ab, Wb, voff, lw, nk, wstep, lds0, smem, csacc);
    else
      loader_loop<CMAX - 1, NBUF, NPA, STAGE, CS>(Ab, Wb, voff, lw, nk, wstep, lds0, smem, csacc);
    if (CS) {
#pragma unroll
      for (int e = 0; e < 8; ++e) {
        float t = csacc[e];
        const float t4 = dpp_add<0x124>(t);
        t = (lw == 3) ? t4 : t;
        t = dpp_add<0x128>(t);
        csacc[e] = xsum16_32(t);
      }
      const int nl = (lw < 3) ? 8 : 4;
      if (lane < nl) {
        const int col = ((lw < 3) ? cfull + lw * 64 : chalf) + lane * 8;
#pragma unroll
        for (int e = 0; e < 8; ++e) *(float*)(smem + SIDE + 1024 + (col + e) * 8) = csacc[e];
      }
    }
  } else {
    if (EPI == EPI_LN_GELU) {
      if (tid < 128) {
        const f32x4* sp = (const f32x4*)(aux0 + (size_t)(m0 + tid) * 32);
        f32x4 p[8];
#pragma unroll
        for (int i = 0; i < 8; ++i) p[i] = sp[i];
        float mean = 0.f;
#pragma unroll
        for (int i = 0; i < 8; ++i) mean += p[i][0] + p[i][2];
        mean *= (1.0f / 16.0f);
        float m2 = 0.f;
#pragma unroll
        for (int i = 0; i < 8; ++i) {
          const float d0 = p[i][0] - mean, d1 = p[i][2] - mean;
          m2 += p[i][1] + p[i][3] + 64.0f * (d0 * d0 + d1 * d1);
        }
        const float var = m2 * (1.0f / DM);
        const float r1 = 1.0f / sqrtf(var + LN_EPS);
        const float r2 = 1.0f / sqrtf(var * r1 * r1 + LN_EPS);
        *(f32x2*)(smem + SIDE + tid * 8) = f32x2{mean, r1 * r2};
      } else if (tid - 128 < BN / 2) {
        const int c = (tid - 128) * 2;
        f32x2 cs = {0.f, 0.f};
#pragma unroll
        for (int kc = 0; kc < 32; ++kc) cs += *(const f32x2*)(aux1 + (size_t)kc * FF + n0 + c);
        const unsigned bb = *(const unsigned*)(bias + n0 + c);
        *(f32x4*)(smem + SIDE + 1024 + c * 8) = f32x4{cs[0], bflo(bb), cs[1], bfhi(bb)};
      }
    }
    const int q = (lane >> 2) & 3, pp = lane & 3;
    const int wk = WIDE ? 0 : wx;
    int aoff[2];
#pragma unroll
    for (int ks = 0; ks < 2; ++ks) aoff[ks] = (wm * 64 + fr) * 128 + (((ks * 4 + g) ^ (fr & 7)) << 4);
    int tb[NT];
    const int f1 = (q & 2) | ((g & 1) << 2);
#pragma unroll
    for (int j = 0; j < NT; ++j) {
      int G, tp;
      if (!WIDE) { G = 0; tp = j; }
      else if (j < 4) { G = wx * (NG - 1); tp = j; }
      else { G = 1; tp = wx * 2 + (j - 4); }
      if (WIDE && HALF && j == NT - 1) {
        tb[j] = ABYTES + NG * 8192 + (8 * g + q) * 64 + (((2 * wx + (pp >> 1)) ^ ((g & 1) << 1)) << 4) + (pp & 1) * 8;
        tcol[j] = chalf + wx * 16;
      } else {
        tb[j] = ABYTES + G * 8192 + (8 * g + q) * 128 + (((2 * tp + (pp >> 1)) ^ f1) << 4) + (pp & 1) * 8;
        tcol[j] = cfull + G * 64 + tp * 16;
      }
    }
#pragma unroll
    for (int i = 0; i < 4; ++i)
#pragma unroll
      for (int j = 0; j < NT; ++j) acc[i][j] = f32x4{0.f, 0.f, 0.f, 0.f};

    bf16x8 xa[4], wa[NT], xb[4], wb[NT];
    auto rd = [&](bf16x8* xf, bf16x8* wf, int buf, int ks) {
      const char* base = smem + buf * STAGE;
#pragma unroll
      for (int j = 0; j < NT; ++j) {
        const int rs = (WIDE && HALF && j == NT - 1) ? 64 : 128;
        s16x4 lo = vtr((lds_cptr)(base + tb[j] + ks * 32 * rs));
        s16x4 hi = vtr((lds_cptr)(base + tb[j] + ks * 32 * rs + 4 * rs));
        wf[j] = __builtin_shufflevector(lo, hi, 0, 1, 2, 3, 4, 5, 6, 7);
      }
#pragma unroll
      for (int i = 0; i < 4; ++i) xf[i] = *(const bf16x8*)(base + aoff[ks] + i * 2048);
    };
    auto mm = [&](const bf16x8* xf, const bf16x8* wf) {
#pragma unroll
      for (int i = 0; i < 4; ++i)
#pragma unroll
        for (int j = 0; j < NT; ++j)
          acc[i][j] = __builtin_amdgcn_mfma_f32_16x16x32_bf16(wf[j], xf[i], acc[i][j], 0, 0, 0);
    };
#define INTERLEAVE() do { _Pragma("unroll") for (int z_ = 0; z_ < 2 * NT + 4; ++z_) { __builtin_amdgcn_sched_group_barrier(0x8, 1, 0); __builtin_amdgcn_sched_group_barrier(0x100, 1, 0); } __builtin_amdgcn_sched_group_barrier(0x8, 4 * NT - (2 * NT + 4), 0); } while (0)
    __builtin_amdgcn_s_waitcnt(0xC07F);
    __builtin_amdgcn_s_barrier();
    STAMP(EPI, 1);
    if (WIDE) {
      rd(xa, wa, 0, 0);
      int b0 = 0;
      for (int t = 0; t < nk; ++t) {
        rd(xb, wb, b0, 1);
        b0 = (b0 + 1 == NBUF) ? 0 : b0 + 1;
        mm(xa, wa);
        INTERLEAVE();
        __builtin_amdgcn_sched_barrier(0);
        __builtin_amdgcn_s_waitcnt(0xC07F);
        __builtin_amdgcn_s_barrier();
        rd(xa, wa, b0, 0);
        mm(xb, wb);
        INTERLEAVE();
        __builtin_amdgcn_sched_barrier(0);
      }
    } else {
      rd(xa, wa, 0, wk);
      int b1 = 1;
      for (int t = 0; t < nk; t += 2) {
        __builtin_amdgcn_s_waitcnt(0xC07F);
        __builtin_amdgcn_s_barrier();
        rd(xb, wb, b1, wk);
        b1 = (b1 + 1 == NBUF) ? 0 : b1 + 1;
        mm(xa, wa);
        INTERLEAVE();
        __builtin_amdgcn_sched_barrier(0);
        __builtin_amdgcn_s_waitcnt(0xC07F);
        __builtin_amdgcn_s_barrier();
        rd(xa, wa, b1, wk);
        b1 = (b1 + 1 == NBUF) ? 0 : b1 + 1;
        mm(xb, wb);
        INTERLEAVE();
        __builtin_amdgcn_sched_barrier(0);
      }
    }
    __builtin_amdgcn_s_waitcnt(0xC07F);
    STAMP(EPI, 2);
    if (SPLIT) {
      const int tile = bm * 8 + bn;
      const auto rsrc = __builtin_amdgcn_make_buffer_rsrc(aux0, 0, 128 * 2 * 2 * 16384, 0x00020000);
      if (wm != hk) {
        const int slot = ((tile * 2 + hk) * 2 + wx);
#pragma unroll
        for (int i = 0; i < 4; ++i)
#pragma unroll
          for (int j = 0; j < NT; ++j)
            __builtin_amdgcn_raw_buffer_store_b128(__builtin_bit_cast(u32x4, acc[i][j]), rsrc,
                                                   slot * 16384 + (i * NT + j) * 1024 + lane * 16, 0, 16);
        asm volatile("s_waitcnt vmcnt(0)" ::: "memory");
        if (lane == 0) __hip_atomic_store(flags + slot * 32, 1u, __ATOMIC_RELAXED, __HIP_MEMORY_SCOPE_AGENT);
      } else {
        const int slot = ((tile * 2 + (hk ^ 1)) * 2 + wx);
        for (int spin = 0; spin < (1 << 24); ++spin) {
          if (__hip_atomic_load(flags + slot * 32, __ATOMIC_RELAXED, __HIP_MEMORY_SCOPE_AGENT) != 0u) break;
          __builtin_amdgcn_s_sleep(2);
        }
        asm volatile("" ::: "memory");
#pragma unroll
        for (int i = 0; i < 4; ++i) {
          u32x4 t[NT];
#pragma unroll
          for (int j = 0; j < NT; ++j)
            t[j] = __builtin_amdgcn_raw_buffer_load_b128(rsrc, slot * 16384 + (i * NT + j) * 1024 + lane * 16, 0, 16);
#pragma unroll
          for (int j = 0; j < NT; ++j) acc[i][j] += __builtin_bit_cast(f32x4, t[j]);
        }
      }
    }
  }
  __syncthreads();
  STAMP(EPI, 3);

  constexpr int CPR = BN / 8;
  if (EPI == EPI_QKV) {
    constexpr int RS = BN * 2 + 16;
    if (wid < 4) {
#pragma unroll
      for (int j = 0; j < NT; ++j)
#pragma unroll
        for (int i = 0; i < 4; ++i) {
          u32x2 o;
          o[0] = pk2(acc[i][j][0], acc[i][j][1]);
          o[1] = pk2(acc[i][j][2], acc[i][j][3]);
          *(u32x2*)(smem + (wm * 64 + i * 16 + fr) * RS + (tcol[j] + g * 4) * 2) = o;
        }
    }
    __syncthreads();
#pragma unroll
    for (int c = tid; c < BM * CPR; c += NTHR) {
      const int hs = c >> 9, row = (c >> 2) & 127, c4 = c & 3;
      u32x4 v = *(const u32x4*)(smem + row * RS + (hs * 4 + c4) * 16);
      ST16(C + ((size_t)((n0 >> 5) + hs) * S_LEN + m0 + row) * 32 + c4 * 8, v);
    }
  } else if (EPI == EPI_LN_GELU) {
    constexpr int RS = BN * 4 + 16;
    if (wid < 4) {
#pragma unroll
      for (int j = 0; j < NT; ++j)
#pragma unroll
        for (int i = 0; i < 4; ++i)
          *(f32x4*)(smem + (wm * 64 + i * 16 + fr) * RS + (tcol[j] + g * 4) * 4) = acc[i][j];
    }
    __syncthreads();
#pragma unroll
    for (int c = tid; c < BM * CPR; c += NTHR) {
      const int row = c / CPR, ch = c % CPR;
      const f32x4 a0 = *(const f32x4*)(smem + row * RS + ch * 32);
      const f32x4 a1 = *(const f32x4*)(smem + row * RS + ch * 32 + 16);
      const f32x2 rs = *(const f32x2*)(smem + SIDE + row * 8);
      float v[8];
#pragma unroll
      for (int r = 0; r < 4; ++r) {
        const f32x4 cb = *(const f32x4*)(smem + SIDE + 1024 + ch * 64 + r * 16);
        const float x0 = (r < 2) ? a0[2 * r] : a1[2 * r - 4];
        const float x1 = (r < 2) ? a0[2 * r + 1] : a1[2 * r - 3];
        v[2 * r] = (x0 - rs[0] * cb[0]) * rs[1] + cb[1];
        v[2 * r + 1] = (x1 - rs[0] * cb[2]) * rs[1] + cb[3];
      }
      u32x4 o;
#pragma unroll
      for (int r = 0; r < 4; ++r) {
        const f32x2 x = {v[2 * r], v[2 * r + 1]};
        const f32x2 x2 = x * x;
        const f32x2 w = x2 * f32x2{-0.10294324f, -0.10294324f} + f32x2{-2.302208198f, -2.302208198f};
        const f32x2 z = x * w;
        f32x2 e = {__builtin_amdgcn_exp2f(z[0]), __builtin_amdgcn_exp2f(z[1])};
        e = e + f32x2{1.0f, 1.0f};
        const f32x2 y = x * f32x2{__builtin_amdgcn_rcpf(e[0]), __builtin_amdgcn_rcpf(e[1])};
        o[r] = pk2(y[0], y[1]);
      }
      ST16(C + (size_t)(m0 + row) * N + n0 + ch * 8, o);
    }
  } else if (EPI == EPI_RES_STATS) {
    constexpr int RS = BN * 4 + 16;
    constexpr int PART = BM * RS;
    if (wid < 4) {
#pragma unroll
      for (int i = 0; i < 4; ++i)
#pragma unroll
        for (int j = 0; j < NT; ++j)
          *(f32x4*)(smem + wx * PART + (wm * 64 + i * 16 + fr) * RS + (j * 16 + g * 4) * 4) = acc[i][j];
    }
    __syncthreads();
#pragma unroll
    for (int c = tid; c < BM * CPR; c += NTHR) {
      const int row = c / CPR, ch = c % CPR;
      const f32x4 a0 = *(const f32x4*)(smem + row * RS + ch * 32);
      const f32x4 a1 = *(const f32x4*)(smem + row * RS + ch * 32 + 16);
      const f32x4 b0 = *(const f32x4*)(smem + PART + row * RS + ch * 32);
      const f32x4 b1 = *(const f32x4*)(smem + PART + row * RS + ch * 32 + 16);
      const u32x4 rv = *(const u32x4*)(res + (size_t)(m0 + row) * N + n0 + ch * 8);
      u32x4 o;
      o[0] = pk2(a0[0] + b0[0] + bflo(rv[0]), a0[1] + b0[1] + bfhi(rv[0]));
      o[1] = pk2(a0[2] + b0[2] + bflo(rv[1]), a0[3] + b0[3] + bfhi(rv[1]));
      o[2] = pk2(a1[0] + b1[0] + bflo(rv[2]), a1[1] + b1[1] + bfhi(rv[2]));
      o[3] = pk2(a1[2] + b1[2] + bflo(rv[3]), a1[3] + b1[3] + bfhi(rv[3]));
      ST16(C + (size_t)(m0 + row) * N + n0 + ch * 8, o);
      float s = 0.f;
#pragma unroll
      for (int r = 0; r < 4; ++r) s += bflo(o[r]) + bfhi(o[r]);
      const float mean = sum8(s) * (1.0f / 64.0f);
      float m2 = 0.f;
#pragma unroll
      for (int r = 0; r < 4; ++r) {
        const float d0 = bflo(o[r]) - mean, d1 = bfhi(o[r]) - mean;
        m2 += d0 * d0 + d1 * d1;
      }
      m2 = sum8(m2);
      if (ch == 0) *(f32x2*)(aux0 + ((size_t)(m0 + row) * 16 + bn) * 2) = f32x2{mean, m2};
    }
  } else {
    constexpr int RS = BN * 4 + 16;
    if (wid < 4 && wm == hk) {
#pragma unroll
      for (int i = 0; i < 4; ++i)
#pragma unroll
        for (int j = 0; j < NT; ++j)
          *(f32x4*)(smem + (i * 16 + fr) * RS + (tcol[j] + g * 4) * 4) = acc[i][j];
    }
    __syncthreads();
#pragma unroll
    for (int c = tid; c < 64 * CPR; c += NTHR) {
      const int row = c / CPR, ch = c % CPR;
      const f32x4 a0 = *(const f32x4*)(smem + row * RS + ch * 32);
      const f32x4 a1 = *(const f32x4*)(smem + row * RS + ch * 32 + 16);
      const u32x4 bb = *(const u32x4*)(bias + n0 + ch * 8);
      const size_t go = (size_t)(m0 + hk * 64 + row) * N + n0 + ch * 8;
      const u32x4 rv = *(const u32x4*)(res + go);
      u32x4 o;
      o[0] = pk2(a0[0] + bflo(bb[0]) + bflo(rv[0]), a0[1] + bfhi(bb[0]) + bfhi(rv[0]));
      o[1] = pk2(a0[2] + bflo(bb[1]) + bflo(rv[1]), a0[3] + bfhi(bb[1]) + bfhi(rv[1]));
      o[2] = pk2(a1[0] + bflo(bb[2]) + bflo(rv[2]), a1[1] + bfhi(bb[2]) + bfhi(rv[2]));
      o[3] = pk2(a1[2] + bflo(bb[3]) + bflo(rv[3]), a1[3] + bfhi(bb[3]) + bfhi(rv[3]));
      ST16(C + go, o);
    }
  }
}

__device__ __forceinline__ float rowmax17(const f32x4* s) {
  float a = fmaxf(fmaxf(s[0][0], s[0][1]), fmaxf(s[0][2], s[0][3]));
#pragma unroll
  for (int t = 1; t < 17; ++t) {
    a = fmaxf(fmaxf(a, s[t][0]), s[t][1]);
    a = fmaxf(fmaxf(a, s[t][2]), s[t][3]);
  }
  return a;
}

template <bool EARLY>
__device__ __forceinline__ void attn_body(const char* ks, const char* vs, bf16x8 qf, int wid, int lane, int tmin,
                                          u16* __restrict__ op) {
  const int fr = lane & 15, g = lane >> 4;
  const float NEG = -1e30f;
  f32x4 s[17];
  const char* kp = ks + (wid * 16 + fr) * 64 + ((g ^ ((4 - ((fr >> 2) & 3)) & 3)) << 4);
#pragma unroll
  for (int t = 0; t < 17; ++t) {
    bf16x8 kf = *(const bf16x8*)(kp + t * 1024);
    s[t] = __builtin_amdgcn_mfma_f32_16x16x32_bf16(kf, qf, f32x4{0.f, 0.f, 0.f, 0.f}, 0, 0, 0);
  }
#pragma unroll
  for (int r = 0; r < 4; ++r) {
    s[0][r] = (4 * g + r > fr) ? s[0][r] : NEG;
    s[16][r] = (4 * g + r <= fr) ? s[16][r] : NEG;
  }
  if (EARLY) {
#pragma unroll
    for (int t = 0; t < 16; ++t)
      if (t < tmin) s[t] = f32x4{NEG, NEG, NEG, NEG};
  }
  const float mx = xmax16_32(rowmax17(s));
  const float c = 0.17677669529663687f * 1.4426950408889634f;
  const f32x2 c2 = {c, c};
  const f32x2 nmc2 = {-mx * c, -mx * c};
  f32x2 sum2 = {0.f, 0.f};
  unsigned pk[18][2];
#pragma unroll
  for (int t = 0; t < 17; ++t) {
#pragma unroll
    for (int hh = 0; hh < 2; ++hh) {
      const f32x2 z = f32x2{s[t][2 * hh], s[t][2 * hh + 1]} * c2 + nmc2;
      const f32x2 p = {__builtin_amdgcn_exp2f(z[0]), __builtin_amdgcn_exp2f(z[1])};
      sum2 += p;
      pk[t][hh] = pk2(p[0], p[1]);
    }
  }
  float sum = sum2[0] + sum2[1];
  pk[17][0] = 0u;
  pk[17][1] = 0u;
  sum = xsum16_32(sum);
  asm volatile("s_waitcnt vmcnt(3)\n\ts_barrier" : "+v"(sum)::"memory");
  const int q4 = (lane >> 2) & 3, pp = lane & 3;
  const char* vp0 = vs + (wid * 16 + 4 * g + q4) * 64 + ((g & 1) << 5) + pp * 8;
  const char* vp1 = vs + (wid * 16 + 4 * g + q4) * 64 + (((g & 1) ^ 1) << 5) + pp * 8;
  f32x4 o0 = {0.f, 0.f, 0.f, 0.f}, o1 = {0.f, 0.f, 0.f, 0.f};
#pragma unroll
  for (int tp = 0; tp < 9; ++tp) {
    const int t0 = 2 * tp, t1 = (2 * tp + 1 < 17) ? 2 * tp + 1 : 16;
    u32x4 pb = {pk[2 * tp][0], pk[2 * tp][1], pk[2 * tp + 1][0], pk[2 * tp + 1][1]};
    bf16x8 pf = __builtin_bit_cast(bf16x8, pb);
    s16x4 a0 = vtr((lds_cptr)(vp0 + t0 * 1024)), a1 = vtr((lds_cptr)(vp0 + t1 * 1024));
    s16x4 b0 = vtr((lds_cptr)(vp1 + t0 * 1024)), b1 = vtr((lds_cptr)(vp1 + t1 * 1024));
    o0 = __builtin_amdgcn_mfma_f32_16x16x32_bf16(__builtin_shufflevector(a0, a1, 0, 1, 2, 3, 4, 5, 6, 7), pf, o0, 0, 0, 0);
    o1 = __builtin_amdgcn_mfma_f32_16x16x32_bf16(__builtin_shufflevector(b0, b1, 0, 1, 2, 3, 4, 5, 6, 7), pf, o1, 0, 0, 0);
  }
  const float inv = 1.0f / sum;
  u32x2 w0, w1;
  w0[0] = pk2(o0[0] * inv, o0[1] * inv);
  w0[1] = pk2(o0[2] * inv, o0[3] * inv);
  w1[0] = pk2(o1[0] * inv, o1[1] * inv);
  w1[1] = pk2(o1[2] * inv, o1[3] * inv);
  *(u32x2*)(op + 4 * g) = w0;
  *(u32x2*)(op + 16 + 4 * g) = w1;
}

__global__ __launch_bounds__(512) void attn_kernel(const u16* __restrict__ qkv, u16* __restrict__ o,
                                                   const char* __restrict__ wo, const u16* __restrict__ w1,
                                                   const char* __restrict__ w2, float* __restrict__ cspart) {
  constexpr int QB = 128, NKEY = WIN + QB;
  __shared__ __attribute__((aligned(16))) char smem[2 * NKEY * 64];
  const int tid = threadIdx.x, lane = tid & 63;
  const int wid = __builtin_amdgcn_readfirstlane(tid >> 6);
  const int bid = blockIdx.x;
  STAMP(4, 0);
  const int head = bid & 31, qb = bid >> 5;
  const int q0 = qb * QB, kbase = q0 - WIN;
  const u16* qh = qkv + (size_t)head * S_LEN * 32;
  const u16* kh = qkv + (size_t)(NH + head) * S_LEN * 32;
  const u16* vh = qkv + (size_t)(2 * NH + head) * S_LEN * 32;
  const unsigned lds0 = (unsigned)(uintptr_t)(__attribute__((address_space(3))) char*)smem;
  unsigned vo[3];
#pragma unroll
  for (int i = 0; i < 3; ++i) {
    const int piece = wid * 3 + i;
    const int key = piece * 16 + (lane >> 2), slot = lane & 3;
    int kj = kbase + key;
    kj = kj < 0 ? 0 : kj;
    const int ck = slot ^ ((4 - ((key >> 2) & 3)) & 3);
    const int cv = slot ^ (((key >> 2) & 1) << 1);
    glds16(kh, (unsigned)((kj * 32 + ck * 8) * 2), lds0 + piece * 1024);
    vo[i] = (unsigned)((kj * 32 + cv * 8) * 2);
  }
  const int fr = lane & 15, g = lane >> 4;
  const int qw = q0 + wid * 16;
  const u16* qp = qh + (size_t)(qw + fr) * 32 + g * 8;
  bf16x8 qf;
  asm volatile("global_load_dwordx4 %0, %1, off" : "=v"(qf) : "v"(qp) : "memory");
#pragma unroll
  for (int i = 0; i < 3; ++i) glds16(vh, vo[i], lds0 + NKEY * 64 + (wid * 3 + i) * 1024);
  asm volatile("s_waitcnt vmcnt(3)" : "+v"(qf)::"memory");
  unsigned touch = 0;
  {
    const int line = (bid * 8 + wid) * 64 + lane;
    const char* p = line < 16384 ? wo + (size_t)line * 128
                  : line < 73728 ? w2 + (size_t)(line - 16384) * 128 : (const char*)qp;
    asm volatile("global_load_dword %0, %1, off" : "+v"(touch) : "v"(p) : "memory");
  }
  const int cb = bid < 448 ? bid : 447;
  const int cg = cb % 14, kc = cb / 14;
  const u16* cp = w1 + (size_t)(kc * 32 + (tid >> 5) * 2) * FF + cg * 256 + (tid & 31) * 8;
  const u32x4 cw0 = *(const u32x4*)cp;
  const u32x4 cw1 = *(const u32x4*)(cp + FF);
  __builtin_amdgcn_s_barrier();
  STAMP(4, 1);
  u16* op = o + (size_t)(qw + fr) * DM + head * DH;
  if (q0 < WIN) {
    const int tmin = (WIN - qw) >> 4;
    attn_body<true>(smem, smem + NKEY * 64, qf, wid, lane, tmin, op);
  } else {
    attn_body<false>(smem, smem + NKEY * 64, qf, wid, lane, 0, op);
  }
  STAMP(4, 2);
  if (bid < 448) {
    __syncthreads();
    float* red = (float*)smem;
    f32x4 lo, hi;
#pragma unroll
    for (int e = 0; e < 2; ++e) {
      lo[2 * e] = bflo(cw0[e]) + bflo(cw1[e]);
      lo[2 * e + 1] = bfhi(cw0[e]) + bfhi(cw1[e]);
      hi[2 * e] = bflo(cw0[2 + e]) + bflo(cw1[2 + e]);
      hi[2 * e + 1] = bfhi(cw0[2 + e]) + bfhi(cw1[2 + e]);
    }
    *(f32x4*)(red + (tid >> 5) * 256 + (tid & 31) * 8) = lo;
    *(f32x4*)(red + (tid >> 5) * 256 + (tid & 31) * 8 + 4) = hi;
    __syncthreads();
    if (tid < 256) {
      float t = 0.f;
#pragma unroll
      for (int i = 0; i < 16; ++i) t += red[i * 256 + tid];
      cspart[(size_t)kc * FF + cg * 256 + tid] = t;
    }
  }
  asm volatile("s_waitcnt vmcnt(0)" : "+v"(touch)::"memory");
  STAMP(4, 3);
}

__global__ __launch_bounds__(512, 1) void gemm_qkv_kernel(const u16* __restrict__ A, const u16* __restrict__ W,
                                                         u16* __restrict__ C) {
  gemm_body<192, 4, EPI_QKV, 8>(A, W, C, nullptr, nullptr, nullptr, nullptr, nullptr, 3 * DM, DM);
}
__global__ __launch_bounds__(512, 1) void gemm_wo_kernel(const u16* __restrict__ A, const u16* __restrict__ W,
                                                        u16* __restrict__ C, const u16* __restrict__ res,
                                                        float* __restrict__ stats) {
  gemm_body<64, 6, EPI_RES_STATS, 4>(A, W, C, nullptr, res, stats, nullptr, nullptr, DM, DM);
}
__global__ __launch_bounds__(512, 1) void gemm_w1_kernel(const u16* __restrict__ A, const u16* __restrict__ W,
                                                        u16* __restrict__ C, const u16* __restrict__ bias,
                                                        float* __restrict__ stats, float* __restrict__ cspart) {
  gemm_body<224, 3, EPI_LN_GELU, 8>(A, W, C, bias, nullptr, stats, cspart, nullptr, FF, DM);
}
__global__ __launch_bounds__(512, 1) void gemm_w2_kernel(const u16* __restrict__ A, const u16* __restrict__ W,
                                                        u16* __restrict__ C, const u16* __restrict__ bias,
                                                        const u16* __restrict__ res, float* __restrict__ xchg,
                                                        unsigned* __restrict__ flags) {
  gemm_body<128, 4, EPI_SPLIT_BIAS_RES, 4>(A, W, C, bias, res, xchg, nullptr, flags, DM, FF);
}
extern "C" __global__ void zz_text_end() {}

constexpr int lds_bytes(int BN, int NBUF, bool side) { return NBUF * (128 * 64 * 2 + 64 * BN * 2) + (side ? 1024 + BN * 8 : 0); }

extern "C" void kernel_launch(void* const* d_in, const int* in_sizes, int n_in, void* d_out, int out_size,
                              void* d_ws, size_t ws_size, hipStream_t stream) {
  const u16* x = (const u16*)d_in[0];
  const u16* wqkv = (const u16*)d_in[1];
  const u16* wo = (const u16*)d_in[2];
  const u16* w1 = (const u16*)d_in[4];
  const u16* b1 = (const u16*)d_in[5];
  const u16* w2 = (const u16*)d_in[6];
  const u16* b2 = (const u16*)d_in[7];
  u16* out = (u16*)d_out;
  char* ws = (char*)d_ws;
  u16* xn = (u16*)(ws);
  u16* qkv = (u16*)(ws + (4u << 20));
  u16* o = (u16*)(ws + (16u << 20));
  u16* h = (u16*)(ws + (20u << 20));
  u16* hh = (u16*)(ws + (28u << 20));
  float* stats = (float*)(ws + (44u << 20));
  float* cspart = (float*)(ws + (45u << 20));
  unsigned* flags = (unsigned*)(ws + (46u << 20));
  float* xchg = (float*)(ws + (48u << 20));

  (void)hipFuncSetAttribute((const void*)gemm_qkv_kernel, hipFuncAttributeMaxDynamicSharedMemorySize, lds_bytes(192, 4, false));
  (void)hipFuncSetAttribute((const void*)gemm_wo_kernel, hipFuncAttributeMaxDynamicSharedMemorySize, lds_bytes(64, 6, false));
  (void)hipFuncSetAttribute((const void*)gemm_w1_kernel, hipFuncAttributeMaxDynamicSharedMemorySize, lds_bytes(224, 3, true));
  (void)hipFuncSetAttribute((const void*)gemm_w2_kernel, hipFuncAttributeMaxDynamicSharedMemorySize, lds_bytes(128, 4, false));
  prep_kernel<<<S_LEN / 4, 256, 0, stream>>>(x, xn, (const char*)wqkv, (const char*)b1, (const char*)b2, flags);
  gemm_qkv_kernel<<<256, 512, lds_bytes(192, 4, false), stream>>>(xn, wqkv, qkv);
  attn_kernel<<<NH * (S_LEN / 128), 512, 0, stream>>>(qkv, o, (const char*)wo, w1, (const char*)w2, cspart);
  gemm_wo_kernel<<<256, 512, lds_bytes(64, 6, false), stream>>>(o, wo, h, x, stats);
  gemm_w1_kernel<<<256, 512, lds_bytes(224, 3, true), stream>>>(h, w1, hh, b1, stats, cspart);
  gemm_w2_kernel<<<256, 512, lds_bytes(128, 4, false), stream>>>(hh, w2, out, b2, h, xchg, flags);
}
```

```cpp
#include <hip/hip_runtime.h>
#include <hip/hip_bf16.h>
#include <stdint.h>

typedef unsigned short u16;
typedef __attribute__((ext_vector_type(8))) short bf16x8;
typedef __attribute__((ext_vector_type(4))) short s16x4;
typedef __attribute__((ext_vector_type(4))) float f32x4;
typedef __attribute__((ext_vector_type(2))) float f32x2;
typedef __attribute__((ext_vector_type(4))) unsigned u32x4;
typedef __attribute__((ext_vector_type(2))) unsigned u32x2;
typedef __attribute__((ext_vector_type(2))) __bf16 bf16x2_t;
typedef __attribute__((address_space(3))) const char* lds_cptr;

#define STAMP(kid, i)
__device__ __forceinline__ void st16_sc1(void* p, __attribute__((ext_vector_type(4))) unsigned v) { asm volatile("global_store_dwordx4 %0, %1, off sc1\n\ts_nop 1" :: "v"(p), "v"(v) : "memory"); }
#define ST16(p, v) st16_sc1((void*)(p), (v))
constexpr int S_LEN = 2048, DM = 1024, NH = 32, DH = 32, WIN = 256, FF = 3584;
constexpr float LN_EPS = 1e-5f;

__device__ __forceinline__ float bf2f(u16 v) { return __uint_as_float(((unsigned)v) << 16); }
__device__ __forceinline__ float bflo(unsigned v) { return __uint_as_float(v << 16); }
__device__ __forceinline__ float bfhi(unsigned v) { return __uint_as_float(v & 0xffff0000u); }
__device__ __forceinline__ unsigned pk2(float lo, float hi) {
  return __builtin_bit_cast(unsigned, __builtin_convertvector(f32x2{lo, hi}, bf16x2_t));
}
__device__ __forceinline__ void glds16(const void* sbase, unsigned voff, unsigned lds_addr) {
  unsigned sv;
  asm volatile("s_mov_b32 %0, m0\n\ts_mov_b32 m0, %3\n\ts_nop 0\n\tglobal_load_lds_dwordx4 %1, %2\n\ts_mov_b32 m0, %0"
               : "=&s"(sv) : "v"(voff), "s"(sbase), "s"(lds_addr) : "memory");
}
__device__ __forceinline__ s16x4 vtr(lds_cptr p) {
  return __builtin_bit_cast(s16x4, __builtin_amdgcn_ds_read_tr16_b64_v4i16((__attribute__((address_space(3))) s16x4*)p));
}
template <int CTRL>
__device__ __forceinline__ float dpp_add(float v) {
  return v + __int_as_float(__builtin_amdgcn_update_dpp(0, __float_as_int(v), CTRL, 0xf, 0xf, false));
}
__device__ __forceinline__ float sum8(float v) {
  v = dpp_add<0xB1>(v);
  v = dpp_add<0x4E>(v);
  return dpp_add<0x141>(v);
}
__device__ __forceinline__ float xsum16_32(float m) {
  auto a = __builtin_amdgcn_permlane32_swap(__float_as_uint(m), __float_as_uint(m), false, false);
  m = __uint_as_float(a[0]) + __uint_as_float(a[1]);
  auto b = __builtin_amdgcn_permlane16_swap(__float_as_uint(m), __float_as_uint(m), false, false);
  return __uint_as_float(b[0]) + __uint_as_float(b[1]);
}
__device__ __forceinline__ float xmax16_32(float m) {
  auto a = __builtin_amdgcn_permlane32_swap(__float_as_uint(m), __float_as_uint(m), false, false);
  m = fmaxf(__uint_as_float(a[0]), __uint_as_float(a[1]));
  auto b = __builtin_amdgcn_permlane16_swap(__float_as_uint(m), __float_as_uint(m), false, false);
  return fmaxf(__uint_as_float(b[0]), __uint_as_float(b[1]));
}
__device__ __forceinline__ float wave_sum(float v) {
  v = sum8(v);
  v = dpp_add<0x140>(v);
  return xsum16_32(v);
}

__global__ __launch_bounds__(256) void prep_kernel(const u16* __restrict__ in, u16* __restrict__ out,
                                                   const char* __restrict__ wqkv, const char* __restrict__ b1,
                                                   const char* __restrict__ b2, unsigned* __restrict__ flags) {
  const int lane = threadIdx.x & 63;
  const int row = blockIdx.x * 4 + (threadIdx.x >> 6);
  const u32x4* src = (const u32x4*)(in + (size_t)row * DM);
  u32x4 a = src[lane], b = src[lane + 64];
  const int gi = blockIdx.x * 256 + threadIdx.x;
  unsigned w0 = 0;
  if (gi < 49152) w0 = *(const unsigned*)(wqkv + (size_t)gi * 128);
  else if (gi < 49152 + 56) w0 = *(const unsigned*)(b1 + (size_t)(gi - 49152) * 128);
  else if (gi < 49152 + 72) w0 = *(const unsigned*)(b2 + (size_t)(gi - 49152 - 56) * 128);
  if (gi < 512) flags[gi * 32] = 0u;
  unsigned w1c = 0;
  if (threadIdx.x < 64 && blockIdx.x < 128) {
    unsigned long long pc, pe;
    asm volatile("s_getpc_b64 %0" : "=s"(pc));
    asm volatile("s_getpc_b64 s[40:41]\n\ts_add_u32 s40, s40, zz_text_end@rel32@lo+4\n\t"
                 "s_addc_u32 s41, s41, zz_text_end@rel32@hi+12\n\ts_mov_b64 %0, s[40:41]"
                 : "=s"(pe) : : "s40", "s41", "scc");
    const unsigned long long a = (pc & ~127ull) + (unsigned long long)((blockIdx.x >> 3) * 64 + lane) * 128;
    if (a + 4 <= pe) w1c = *(const unsigned*)a;
  }
  float v[16];
#pragma unroll
  for (int i = 0; i < 4; ++i) {
    v[2 * i] = bflo(a[i]);
    v[2 * i + 1] = bfhi(a[i]);
    v[8 + 2 * i] = bflo(b[i]);
    v[8 + 2 * i + 1] = bfhi(b[i]);
  }
  float s = 0.f;
#pragma unroll
  for (int i = 0; i < 16; ++i) s += v[i];
  const float mean = wave_sum(s) * (1.0f / DM);
  float q = 0.f;
#pragma unroll
  for (int i = 0; i < 16; ++i) { v[i] -= mean; q += v[i] * v[i]; }
  const float var = wave_sum(q) * (1.0f / DM);
  const float r = 1.0f / sqrtf(var + LN_EPS);
  u32x4 oa, ob;
#pragma unroll
  for (int i = 0; i < 4; ++i) {
    oa[i] = pk2(v[2 * i] * r, v[2 * i + 1] * r);
    ob[i] = pk2(v[8 + 2 * i] * r, v[8 + 2 * i + 1] * r);
  }
  u32x4* dst = (u32x4*)(out + (size_t)row * DM);
  dst[lane] = oa;
  dst[lane + 64] = ob;
  asm volatile("" ::"v"(w0), "v"(w1c));
}

enum { EPI_QKV = 0, EPI_RES_STATS = 1, EPI_LN_GELU = 2, EPI_SPLIT_BIAS_RES = 3 };
constexpr int NLW = 4;

template <int CNT, int NBUF, int NPA, int STAGE, bool CS>
__device__ __forceinline__ void loader_loop(const char* Ab, const char* Wb, const unsigned* voff, int lw, int nk,
                                            unsigned wstep, unsigned lds0, const char* smem, float* csacc) {
  const int lane = threadIdx.x & 63;
  int ca0 = 0, ca1 = 0, cstep = 0;
  if (CS) {
    if (lw < 3) {
      const int k = lane >> 3, c = lane & 7;
      ca0 = NPA * 1024 + lw * 8192 + k * 128 + ((c ^ (k & 2)) << 4);
      ca1 = NPA * 1024 + lw * 8192 + (k + 8) * 128 + ((c ^ ((k & 2) | 4)) << 4);
      cstep = 2048;
    } else {
      const int k = lane >> 2, c = lane & 3;
      ca0 = NPA * 1024 + 3 * 8192 + k * 64 + ((c ^ ((k >> 2) & 2)) << 4);
      ca1 = ca0 + 1024;
      cstep = 2048;
    }
  }
  auto colsum = [&](int buf) {
    const char* base = smem + buf * STAGE;
    const int n = (lw < 3) ? 4 : 2;
#pragma unroll
    for (int i = 0; i < 4; ++i) {
      if (i < n) {
        const u32x4 u = *(const u32x4*)(base + ca0 + i * cstep);
        const u32x4 v = *(const u32x4*)(base + ca1 + i * cstep);
#pragma unroll
        for (int e = 0; e < 4; ++e) {
          csacc[2 * e] += bflo(u[e]) + bflo(v[e]);
          csacc[2 * e + 1] += bfhi(u[e]) + bfhi(v[e]);
        }
      }
    }
  };
  const char* sp[CNT];
  unsigned st[CNT];
#pragma unroll
  for (int i = 0; i < CNT; ++i) {
    const bool isA = (lw + i * NLW) < NPA;
    sp[i] = isA ? Ab : Wb;
    st[i] = isA ? 128u : wstep;
  }
  auto issue = [&](int buf) {
#pragma unroll
    for (int i = 0; i < CNT; ++i) {
      glds16(sp[i], voff[i], lds0 + buf * STAGE + (lw + i * NLW) * 1024);
      sp[i] += st[i];
    }
  };
  issue(0);
  issue(1);
  asm volatile("s_waitcnt vmcnt(%0)" ::"i"(CNT) : "memory");
  __builtin_amdgcn_s_barrier();
#pragma unroll
  for (int s = 2; s < NBUF; ++s) issue(s);
  if (CS) colsum(0);
  int buf = 0;
  for (int t = 0; t < nk; ++t) {
    if (t + NBUF <= nk) {
      asm volatile("s_waitcnt vmcnt(%0)" ::"i"((NBUF - 2) * CNT) : "memory");
    } else {
      asm volatile("s_waitcnt vmcnt(0)" ::: "memory");
    }
    __builtin_amdgcn_s_barrier();
    if (t + NBUF < nk) issue(buf);
    buf = (buf + 1 == NBUF) ? 0 : buf + 1;
    if (CS && t + 1 < nk) colsum(buf);
  }
}

template <int BN, int NBUF, int EPI, int XA, int TL0 = 0, int TL1 = 0>
__device__ __forceinline__ void gemm_body(const char* __restrict__ tp0, const char* __restrict__ tp1, const u16* __restrict__ A, const u16* __restrict__ W,
                                          u16* __restrict__ C, const u16* __restrict__ bias,
                                          const u16* __restrict__ res, float* __restrict__ aux0,
                                          float* __restrict__ aux1, unsigned* __restrict__ flags,
                                          const int N, const int K) {
  constexpr int BM = 128;
  constexpr bool WIDE = BN > 64;
  constexpr bool SPLIT = (EPI == EPI_SPLIT_BIAS_RES);
  constexpr int NTHR = 512;
  constexpr int NG = BN / 64;
  constexpr bool HALF = (BN % 64) != 0;
  constexpr int NT = WIDE ? BN / 32 : 4;
  constexpr int ABYTES = BM * 128, WBYTES = 64 * BN * 2, STAGE = ABYTES + WBYTES;
  constexpr int NPA = ABYTES / 1024, NP = STAGE / 1024;
  constexpr int CMAX = (NP + NLW - 1) / NLW;
  constexpr int TSCR = NBUF * STAGE + 4096;
  constexpr int SIDE = NBUF * STAGE;
  extern __shared__ __attribute__((aligned(16))) char smem[];

  const int tid = threadIdx.x, lane = tid & 63;
  const int wid = __builtin_amdgcn_readfirstlane(tid >> 6);
  STAMP(EPI, 0);
  const int xc = blockIdx.x & 7, rr = blockIdx.x >> 3;
  int bm, bn, hk = 0;
  if (SPLIT) {
    hk = rr & 1;
    const int tt = rr >> 1;
    bm = (xc & 3) * 4 + (tt & 3);
    bn = (xc >> 2) * 4 + (tt >> 2);
  } else {
    constexpr int XB = 32 / XA, PM = 16 / XA;
    bm = (xc % PM) * XA + (rr % XA);
    bn = (xc / PM) * XB + (rr / XA);
  }
  const int m0 = bm * BM, n0 = bn * BN;
  const int nk = SPLIT ? K / 128 : K / 64;
  const int kbeg = hk * (K / 2);
  const int odd = HALF ? (bn & 1) : 0;
  const int cfull = odd ? 32 : 0, chalf = odd ? 0 : NG * 64;
  const unsigned lds0 = (unsigned)(uintptr_t)(__attribute__((address_space(3))) char*)smem;
  const int fr = lane & 15, g = lane >> 4;

  f32x4 acc[4][NT];
  const int wm = wid & 1, wx = (wid >> 1) & 1;
  int tcol[NT];

  if (wid >= 4) {
    const int lw = wid - 4;
    unsigned voff[CMAX];
#pragma unroll
    for (int i = 0; i < CMAX; ++i) {
      const int idx = lw + i * NLW;
      if (idx < NPA) {
        const int row = idx * 8 + (lane >> 3), ch = (lane & 7) ^ (row & 7);
        voff[i] = (unsigned)(((m0 + row) * K + ch * 8) * 2);
      } else if (idx < NPA + NG * 8) {
        const int w = idx - NPA, G = w >> 3, k = (w & 7) * 8 + (lane >> 3);
        const int ch = (lane & 7) ^ ((k & 2) | ((k >> 1) & 4));
        voff[i] = (unsigned)((k * N + n0 + cfull + G * 64 + ch * 8) * 2);
      } else {
        const int w = idx - NPA - NG * 8, k = w * 16 + (lane >> 2);
        const int ch = (lane & 3) ^ ((k >> 2) & 2);
        voff[i] = (unsigned)((k * N + n0 + chalf + ch * 8) * 2);
      }
    }
    const unsigned wstep = (unsigned)(64 * N * 2);
    const char* Ab = (const char*)(A + kbeg);
    const char* Wb = (const char*)(W + (size_t)kbeg * N);
    constexpr bool CS = false;
    float csacc[8];
#pragma unroll
    for (int e = 0; e < 8; ++e) csacc[e] = 0.f;
    if (lw + (CMAX - 1) * NLW < NP)
      loader_loop<CMAX, NBUF, NPA, STAGE, CS>(Ab, Wb, voff, lw, nk, wstep, lds0, smem, csacc);
    else
      loader_loop<CMAX - 1, NBUF, NPA, STAGE, CS>(Ab, Wb, voff, lw, nk, wstep, lds0, smem, csacc);
    if (CS) {
#pragma unroll
      for (int e = 0; e < 8; ++e) {
        float t = csacc[e];
        const float t4 = dpp_add<0x124>(t);
        t = (lw == 3) ? t4 : t;
        t = dpp_add<0x128>(t);
        csacc[e] = xsum16_32(t);
      }
      const int nl = (lw < 3) ? 8 : 4;
      if (lane < nl) {
        const int col = ((lw < 3) ? cfull + lw * 64 : chalf) + lane * 8;
#pragma unroll
        for (int e = 0; e < 8; ++e) *(float*)(smem + SIDE + 1024 + (col + e) * 8) = csacc[e];
      }
    }
  } else {
    if (EPI == EPI_LN_GELU) {
      if (tid < 128) {
        const f32x4* sp = (const f32x4*)(aux0 + (size_t)(m0 + tid) * 32);
        f32x4 p[8];
#pragma unroll
        for (int i = 0; i < 8; ++i) p[i] = sp[i];
        float mean = 0.f;
#pragma unroll
        for (int i = 0; i < 8; ++i) mean += p[i][0] + p[i][2];
        mean *= (1.0f / 16.0f);
        float m2 = 0.f;
#pragma unroll
        for (int i = 0; i < 8; ++i) {
          const float d0 = p[i][0] - mean, d1 = p[i][2] - mean;
          m2 += p[i][1] + p[i][3] + 64.0f * (d0 * d0 + d1 * d1);
        }
        const float var = m2 * (1.0f / DM);
        const float r1 = 1.0f / sqrtf(var + LN_EPS);
        const float r2 = 1.0f / sqrtf(var * r1 * r1 + LN_EPS);
        *(f32x2*)(smem + SIDE + tid * 8) = f32x2{mean, r1 * r2};
      } else if (tid - 128 < BN / 2) {
        const int c = (tid - 128) * 2;
        f32x2 cs = {0.f, 0.f};
#pragma unroll
        for (int kc = 0; kc < 32; ++kc) cs += *(const f32x2*)(aux1 + (size_t)kc * FF + n0 + c);
        const unsigned bb = *(const unsigned*)(bias + n0 + c);
        *(f32x4*)(smem + SIDE + 1024 + c * 8) = f32x4{cs[0], bflo(bb), cs[1], bfhi(bb)};
      }
    }
    const int q = (lane >> 2) & 3, pp = lane & 3;
    const int wk = WIDE ? 0 : wx;
    int aoff[2];
#pragma unroll
    for (int ks = 0; ks < 2; ++ks) aoff[ks] = (wm * 64 + fr) * 128 + (((ks * 4 + g) ^ (fr & 7)) << 4);
    int tb[NT];
    const int f1 = (q & 2) | ((g & 1) << 2);
#pragma unroll
    for (int j = 0; j < NT; ++j) {
      int G, tp;
      if (!WIDE) { G = 0; tp = j; }
      else if (j < 4) { G = wx * (NG - 1); tp = j; }
      else { G = 1; tp = wx * 2 + (j - 4); }
      if (WIDE && HALF && j == NT - 1) {
        tb[j] = ABYTES + NG * 8192 + (8 * g + q) * 64 + (((2 * wx + (pp >> 1)) ^ ((g & 1) << 1)) << 4) + (pp & 1) * 8;
        tcol[j] = chalf + wx * 16;
      } else {
        tb[j] = ABYTES + G * 8192 + (8 * g + q) * 128 + (((2 * tp + (pp >> 1)) ^ f1) << 4) + (pp & 1) * 8;
        tcol[j] = cfull + G * 64 + tp * 16;
      }
    }
#pragma unroll
    for (int i = 0; i < 4; ++i)
#pragma unroll
      for (int j = 0; j < NT; ++j) acc[i][j] = f32x4{0.f, 0.f, 0.f, 0.f};

    bf16x8 xa[4], wa[NT], xb[4], wb[NT];
    auto rd = [&](bf16x8* xf, bf16x8* wf, int buf, int ks) {
      const char* base = smem + buf * STAGE;
#pragma unroll
      for (int j = 0; j < NT; ++j) {
        const int rs = (WIDE && HALF && j == NT - 1) ? 64 : 128;
        s16x4 lo = vtr((lds_cptr)(base + tb[j] + ks * 32 * rs));
        s16x4 hi = vtr((lds_cptr)(base + tb[j] + ks * 32 * rs + 4 * rs));
        wf[j] = __builtin_shufflevector(lo, hi, 0, 1, 2, 3, 4, 5, 6, 7);
      }
#pragma unroll
      for (int i = 0; i < 4; ++i) xf[i] = *(const bf16x8*)(base + aoff[ks] + i * 2048);
    };
    auto mm = [&](const bf16x8* xf, const bf16x8* wf) {
#pragma unroll
      for (int i = 0; i < 4; ++i)
#pragma unroll
        for (int j = 0; j < NT; ++j)
          acc[i][j] = __builtin_amdgcn_mfma_f32_16x16x32_bf16(wf[j], xf[i], acc[i][j], 0, 0, 0);
    };
#define INTERLEAVE() do { _Pragma("unroll") for (int z_ = 0; z_ < 2 * NT + 4; ++z_) { __builtin_amdgcn_sched_group_barrier(0x8, 1, 0); __builtin_amdgcn_sched_group_barrier(0x100, 1, 0); } __builtin_amdgcn_sched_group_barrier(0x8, 4 * NT - (2 * NT + 4), 0); } while (0)
    __builtin_amdgcn_s_waitcnt(0xC07F);
    __builtin_amdgcn_s_barrier();
    STAMP(EPI, 1);
    auto do_touch = [&](int i) {
      const int line = ((blockIdx.x * 4 + wid) * 2 + i) * 64 + lane;
      if (__builtin_amdgcn_readfirstlane(line) < TL0 + TL1) {
        const int lc = line < TL0 + TL1 ? line : TL0 + TL1 - 1;
        const char* p = lc < TL0 ? tp0 + (size_t)lc * 128 : tp1 + (size_t)(lc - TL0) * 128;
        unsigned sv;
        asm volatile("s_mov_b32 %0, m0\n\ts_mov_b32 m0, %2\n\ts_nop 0\n\tglobal_load_lds_dword %1, off\n\ts_mov_b32 m0, %0"
                     : "=&s"(sv) : "v"(p), "s"(lds0 + TSCR + wid * 256) : "memory");
      }
    };
    if (WIDE) {
      rd(xa, wa, 0, 0);
      int b0 = 0;
      for (int t = 0; t < nk; ++t) {
        if (TL0 + TL1 > 0) { if (t == 2) do_touch(0); if (t == 7) do_touch(1); }
        rd(xb, wb, b0, 1);
        b0 = (b0 + 1 == NBUF) ? 0 : b0 + 1;
        mm(xa, wa);
        INTERLEAVE();
        __builtin_amdgcn_sched_barrier(0);
        __builtin_amdgcn_s_waitcnt(0xC07F);
        __builtin_amdgcn_s_barrier();
        rd(xa, wa, b0, 0);
        mm(xb, wb);
        INTERLEAVE();
        __builtin_amdgcn_sched_barrier(0);
      }
    } else {
      rd(xa, wa, 0, wk);
      int b1 = 1;
      for (int t = 0; t < nk; t += 2) {
        __builtin_amdgcn_s_waitcnt(0xC07F);
        __builtin_amdgcn_s_barrier();
        rd(xb, wb, b1, wk);
        b1 = (b1 + 1 == NBUF) ? 0 : b1 + 1;
        mm(xa, wa);
        INTERLEAVE();
        __builtin_amdgcn_sched_barrier(0);
        __builtin_amdgcn_s_waitcnt(0xC07F);
        __builtin_amdgcn_s_barrier();
        rd(xa, wa, b1, wk);
        b1 = (b1 + 1 == NBUF) ? 0 : b1 + 1;
        mm(xb, wb);
        INTERLEAVE();
        __builtin_amdgcn_sched_barrier(0);
      }
    }
    __builtin_amdgcn_s_waitcnt(0xC07F);
    if (TL0 + TL1 > 0) asm volatile("s_waitcnt vmcnt(0)" ::: "memory");
    STAMP(EPI, 2);
    if (SPLIT) {
      const int tile = bm * 8 + bn;
      const auto rsrc = __builtin_amdgcn_make_buffer_rsrc(aux0, 0, 128 * 2 * 2 * 16384, 0x00020000);
      if (wm != hk) {
        const int slot = ((tile * 2 + hk) * 2 + wx);
#pragma unroll
        for (int i = 0; i < 4; ++i)
#pragma unroll
          for (int j = 0; j < NT; ++j)
            __builtin_amdgcn_raw_buffer_store_b128(__builtin_bit_cast(u32x4, acc[i][j]), rsrc,
                                                   slot * 16384 + (i * NT + j) * 1024 + lane * 16, 0, 16);
        asm volatile("s_waitcnt vmcnt(0)" ::: "memory");
        if (lane == 0) __hip_atomic_store(flags + slot * 32, 1u, __ATOMIC_RELAXED, __HIP_MEMORY_SCOPE_AGENT);
      } else {
        const int slot = ((tile * 2 + (hk ^ 1)) * 2 + wx);
        for (int spin = 0; spin < (1 << 24); ++spin) {
          if (__hip_atomic_load(flags + slot * 32, __ATOMIC_RELAXED, __HIP_MEMORY_SCOPE_AGENT) != 0u) break;
          __builtin_amdgcn_s_sleep(2);
        }
        asm volatile("" ::: "memory");
#pragma unroll
        for (int i = 0; i < 4; ++i) {
          u32x4 t[NT];
#pragma unroll
          for (int j = 0; j < NT; ++j)
            t[j] = __builtin_amdgcn_raw_buffer_load_b128(rsrc, slot * 16384 + (i * NT + j) * 1024 + lane * 16, 0, 16);
#pragma unroll
          for (int j = 0; j < NT; ++j) acc[i][j] += __builtin_bit_cast(f32x4, t[j]);
        }
      }
    }
  }
  __syncthreads();
  STAMP(EPI, 3);

  constexpr int CPR = BN / 8;
  if (EPI == EPI_QKV) {
    constexpr int RS = BN * 2 + 16;
    if (wid < 4) {
#pragma unroll
      for (int j = 0; j < NT; ++j)
#pragma unroll
        for (int i = 0; i < 4; ++i) {
          u32x2 o;
          o[0] = pk2(acc[i][j][0], acc[i][j][1]);
          o[1] = pk2(acc[i][j][2], acc[i][j][3]);
          *(u32x2*)(smem + (wm * 64 + i * 16 + fr) * RS + (tcol[j] + g * 4) * 2) = o;
        }
    }
    __syncthreads();
#pragma unroll
    for (int c = tid; c < BM * CPR; c += NTHR) {
      const int hs = c >> 9, row = (c >> 2) & 127, c4 = c & 3;
      u32x4 v = *(const u32x4*)(smem + row * RS + (hs * 4 + c4) * 16);
      ST16(C + ((size_t)((n0 >> 5) + hs) * S_LEN + m0 + row) * 32 + c4 * 8, v);
    }
  } else if (EPI == EPI_LN_GELU) {
    constexpr int RS = BN * 4 + 16;
    if (wid < 4) {
#pragma unroll
      for (int j = 0; j < NT; ++j)
#pragma unroll
        for (int i = 0; i < 4; ++i)
          *(f32x4*)(smem + (wm * 64 + i * 16 + fr) * RS + (tcol[j] + g * 4) * 4) = acc[i][j];
    }
    __syncthreads();
#pragma unroll
    for (int c = tid; c < BM * CPR; c += NTHR) {
      const int row = c / CPR, ch = c % CPR;
      const f32x4 a0 = *(const f32x4*)(smem + row * RS + ch * 32);
      const f32x4 a1 = *(const f32x4*)(smem + row * RS + ch * 32 + 16);
      const f32x2 rs = *(const f32x2*)(smem + SIDE + row * 8);
      float v[8];
#pragma unroll
      for (int r = 0; r < 4; ++r) {
        const f32x4 cb = *(const f32x4*)(smem + SIDE + 1024 + ch * 64 + r * 16);
        const float x0 = (r < 2) ? a0[2 * r] : a1[2 * r - 4];
        const float x1 = (r < 2) ? a0[2 * r + 1] : a1[2 * r - 3];
        v[2 * r] = (x0 - rs[0] * cb[0]) * rs[1] + cb[1];
        v[2 * r + 1] = (x1 - rs[0] * cb[2]) * rs[1] + cb[3];
      }
      u32x4 o;
#pragma unroll
      for (int r = 0; r < 4; ++r) {
        const f32x2 x = {v[2 * r], v[2 * r + 1]};
        const f32x2 x2 = x * x;
        const f32x2 w = x2 * f32x2{-0.10294324f, -0.10294324f} + f32x2{-2.302208198f, -2.302208198f};
        const f32x2 z = x * w;
        f32x2 e = {__builtin_amdgcn_exp2f(z[0]), __builtin_amdgcn_exp2f(z[1])};
        e = e + f32x2{1.0f, 1.0f};
        const f32x2 y = x * f32x2{__builtin_amdgcn_rcpf(e[0]), __builtin_amdgcn_rcpf(e[1])};
        o[r] = pk2(y[0], y[1]);
      }
      ST16(C + (size_t)(m0 + row) * N + n0 + ch * 8, o);
    }
  } else if (EPI == EPI_RES_STATS) {
    constexpr int RS = BN * 4 + 16;
    constexpr int PART = BM * RS;
    if (wid < 4) {
#pragma unroll
      for (int i = 0; i < 4; ++i)
#pragma unroll
        for (int j = 0; j < NT; ++j)
          *(f32x4*)(smem + wx * PART + (wm * 64 + i * 16 + fr) * RS + (j * 16 + g * 4) * 4) = acc[i][j];
    }
    __syncthreads();
#pragma unroll
    for (int c = tid; c < BM * CPR; c += NTHR) {
      const int row = c / CPR, ch = c % CPR;
      const f32x4 a0 = *(const f32x4*)(smem + row * RS + ch * 32);
      const f32x4 a1 = *(const f32x4*)(smem + row * RS + ch * 32 + 16);
      const f32x4 b0 = *(const f32x4*)(smem + PART + row * RS + ch * 32);
      const f32x4 b1 = *(const f32x4*)(smem + PART + row * RS + ch * 32 + 16);
      const u32x4 rv = *(const u32x4*)(res + (size_t)(m0 + row) * N + n0 + ch * 8);
      u32x4 o;
      o[0] = pk2(a0[0] + b0[0] + bflo(rv[0]), a0[1] + b0[1] + bfhi(rv[0]));
      o[1] = pk2(a0[2] + b0[2] + bflo(rv[1]), a0[3] + b0[3] + bfhi(rv[1]));
      o[2] = pk2(a1[0] + b1[0] + bflo(rv[2]), a1[1] + b1[1] + bfhi(rv[2]));
      o[3] = pk2(a1[2] + b1[2] + bflo(rv[3]), a1[3] + b1[3] + bfhi(rv[3]));
      ST16(C + (size_t)(m0 + row) * N + n0 + ch * 8, o);
      float s = 0.f;
#pragma unroll
      for (int r = 0; r < 4; ++r) s += bflo(o[r]) + bfhi(o[r]);
      const float mean = sum8(s) * (1.0f / 64.0f);
      float m2 = 0.f;
#pragma unroll
      for (int r = 0; r < 4; ++r) {
        const float d0 = bflo(o[r]) - mean, d1 = bfhi(o[r]) - mean;
        m2 += d0 * d0 + d1 * d1;
      }
      m2 = sum8(m2);
      if (ch == 0) *(f32x2*)(aux0 + ((size_t)(m0 + row) * 16 + bn) * 2) = f32x2{mean, m2};
    }
  } else {
    constexpr int RS = BN * 4 + 16;
    if (wid < 4 && wm == hk) {
#pragma unroll
      for (int i = 0; i < 4; ++i)
#pragma unroll
        for (int j = 0; j < NT; ++j)
          *(f32x4*)(smem + (i * 16 + fr) * RS + (tcol[j] + g * 4) * 4) = acc[i][j];
    }
    __syncthreads();
#pragma unroll
    for (int c = tid; c < 64 * CPR; c += NTHR) {
      const int row = c / CPR, ch = c % CPR;
      const f32x4 a0 = *(const f32x4*)(smem + row * RS + ch * 32);
      const f32x4 a1 = *(const f32x4*)(smem + row * RS + ch * 32 + 16);
      const u32x4 bb = *(const u32x4*)(bias + n0 + ch * 8);
      const size_t go = (size_t)(m0 + hk * 64 + row) * N + n0 + ch * 8;
      const u32x4 rv = *(const u32x4*)(res + go);
      u32x4 o;
      o[0] = pk2(a0[0] + bflo(bb[0]) + bflo(rv[0]), a0[1] + bfhi(bb[0]) + bfhi(rv[0]));
      o[1] = pk2(a0[2] + bflo(bb[1]) + bflo(rv[1]), a0[3] + bfhi(bb[1]) + bfhi(rv[1]));
      o[2] = pk2(a1[0] + bflo(bb[2]) + bflo(rv[2]), a1[1] + bfhi(bb[2]) + bfhi(rv[2]));
      o[3] = pk2(a1[2] + bflo(bb[3]) + bflo(rv[3]), a1[3] + bfhi(bb[3]) + bfhi(rv[3]));
      ST16(C + go, o);
    }
  }
}

__device__ __forceinline__ float rowmax17(const f32x4* s) {
  float a = fmaxf(fmaxf(s[0][0], s[0][1]), fmaxf(s[0][2], s[0][3]));
#pragma unroll
  for (int t = 1; t < 17; ++t) {
    a = fmaxf(fmaxf(a, s[t][0]), s[t][1]);
    a = fmaxf(fmaxf(a, s[t][2]), s[t][3]);
  }
  return a;
}

template <bool EARLY>
__device__ __forceinline__ void attn_body(const char* ks, const char* vs, bf16x8 qf, int wid, int lane, int tmin,
                                          u16* __restrict__ op) {
  const int fr = lane & 15, g = lane >> 4;
  const float NEG = -1e30f;
  f32x4 s[17];
  const char* kp = ks + (wid * 16 + fr) * 64 + ((g ^ ((4 - ((fr >> 2) & 3)) & 3)) << 4);
#pragma unroll
  for (int t = 0; t < 17; ++t) {
    bf16x8 kf = *(const bf16x8*)(kp + t * 1024);
    s[t] = __builtin_amdgcn_mfma_f32_16x16x32_bf16(kf, qf, f32x4{0.f, 0.f, 0.f, 0.f}, 0, 0, 0);
  }
#pragma unroll
  for (int r = 0; r < 4; ++r) {
    s[0][r] = (4 * g + r > fr) ? s[0][r] : NEG;
    s[16][r] = (4 * g + r <= fr) ? s[16][r] : NEG;
  }
  if (EARLY) {
#pragma unroll
    for (int t = 0; t < 16; ++t)
      if (t < tmin) s[t] = f32x4{NEG, NEG, NEG, NEG};
  }
  const float mx = xmax16_32(rowmax17(s));
  const float c = 0.17677669529663687f * 1.4426950408889634f;
  const f32x2 c2 = {c, c};
  const f32x2 nmc2 = {-mx * c, -mx * c};
  f32x2 sum2 = {0.f, 0.f};
  unsigned pk[18][2];
#pragma unroll
  for (int t = 0; t < 17; ++t) {
#pragma unroll
    for (int hh = 0; hh < 2; ++hh) {
      const f32x2 z = f32x2{s[t][2 * hh], s[t][2 * hh + 1]} * c2 + nmc2;
      const f32x2 p = {__builtin_amdgcn_exp2f(z[0]), __builtin_amdgcn_exp2f(z[1])};
      sum2 += p;
      pk[t][hh] = pk2(p[0], p[1]);
    }
  }
  float sum = sum2[0] + sum2[1];
  pk[17][0] = 0u;
  pk[17][1] = 0u;
  sum = xsum16_32(sum);
  asm volatile("s_waitcnt vmcnt(3)\n\ts_barrier" : "+v"(sum)::"memory");
  const int q4 = (lane >> 2) & 3, pp = lane & 3;
  const char* vp0 = vs + (wid * 16 + 4 * g + q4) * 64 + ((g & 1) << 5) + pp * 8;
  const char* vp1 = vs + (wid * 16 + 4 * g + q4) * 64 + (((g & 1) ^ 1) << 5) + pp * 8;
  f32x4 o0 = {0.f, 0.f, 0.f, 0.f}, o1 = {0.f, 0.f, 0.f, 0.f};
#pragma unroll
  for (int tp = 0; tp < 9; ++tp) {
    const int t0 = 2 * tp, t1 = (2 * tp + 1 < 17) ? 2 * tp + 1 : 16;
    u32x4 pb = {pk[2 * tp][0], pk[2 * tp][1], pk[2 * tp + 1][0], pk[2 * tp + 1][1]};
    bf16x8 pf = __builtin_bit_cast(bf16x8, pb);
    s16x4 a0 = vtr((lds_cptr)(vp0 + t0 * 1024)), a1 = vtr((lds_cptr)(vp0 + t1 * 1024));
    s16x4 b0 = vtr((lds_cptr)(vp1 + t0 * 1024)), b1 = vtr((lds_cptr)(vp1 + t1 * 1024));
    o0 = __builtin_amdgcn_mfma_f32_16x16x32_bf16(__builtin_shufflevector(a0, a1, 0, 1, 2, 3, 4, 5, 6, 7), pf, o0, 0, 0, 0);
    o1 = __builtin_amdgcn_mfma_f32_16x16x32_bf16(__builtin_shufflevector(b0, b1, 0, 1, 2, 3, 4, 5, 6, 7), pf, o1, 0, 0, 0);
  }
  const float inv = 1.0f / sum;
  u32x2 w0, w1;
  w0[0] = pk2(o0[0] * inv, o0[1] * inv);
  w0[1] = pk2(o0[2] * inv, o0[3] * inv);
  w1[0] = pk2(o1[0] * inv, o1[1] * inv);
  w1[1] = pk2(o1[2] * inv, o1[3] * inv);
  *(u32x2*)(op + 4 * g) = w0;
  *(u32x2*)(op + 16 + 4 * g) = w1;
}

__global__ __launch_bounds__(512) void attn_kernel(const u16* __restrict__ qkv, u16* __restrict__ o,
                                                   const char* __restrict__ wo, const u16* __restrict__ w1,
                                                   const char* __restrict__ w2, float* __restrict__ cspart) {
  constexpr int QB = 128, NKEY = WIN + QB;
  __shared__ __attribute__((aligned(16))) char smem[2 * NKEY * 64];
  const int tid = threadIdx.x, lane = tid & 63;
  const int wid = __builtin_amdgcn_readfirstlane(tid >> 6);
  const int bid = blockIdx.x;
  STAMP(4, 0);
  const int head = bid & 31, qb = bid >> 5;
  const int q0 = qb * QB, kbase = q0 - WIN;
  const u16* qh = qkv + (size_t)head * S_LEN * 32;
  const u16* kh = qkv + (size_t)(NH + head) * S_LEN * 32;
  const u16* vh = qkv + (size_t)(2 * NH + head) * S_LEN * 32;
  const unsigned lds0 = (unsigned)(uintptr_t)(__attribute__((address_space(3))) char*)smem;
  unsigned vo[3];
#pragma unroll
  for (int i = 0; i < 3; ++i) {
    const int piece = wid * 3 + i;
    const int key = piece * 16 + (lane >> 2), slot = lane & 3;
    int kj = kbase + key;
    kj = kj < 0 ? 0 : kj;
    const int ck = slot ^ ((4 - ((key >> 2) & 3)) & 3);
    const int cv = slot ^ (((key >> 2) & 1) << 1);
    glds16(kh, (unsigned)((kj * 32 + ck * 8) * 2), lds0 + piece * 1024);
    vo[i] = (unsigned)((kj * 32 + cv * 8) * 2);
  }
  const int fr = lane & 15, g = lane >> 4;
  const int qw = q0 + wid * 16;
  const u16* qp = qh + (size_t)(qw + fr) * 32 + g * 8;
  bf16x8 qf;
  asm volatile("global_load_dwordx4 %0, %1, off" : "=v"(qf) : "v"(qp) : "memory");
#pragma unroll
  for (int i = 0; i < 3; ++i) glds16(vh, vo[i], lds0 + NKEY * 64 + (wid * 3 + i) * 1024);
  asm volatile("s_waitcnt vmcnt(3)" : "+v"(qf)::"memory");
  unsigned touch = 0;
  {
    const char* p = (const char*)qp;
    asm volatile("global_load_dword %0, %1, off" : "+v"(touch) : "v"(p) : "memory");
  }
  const int cb = bid < 448 ? bid : 447;
  const int cg = cb % 14, kc = cb / 14;
  const u16* cp = w1 + (size_t)(kc * 32 + (tid >> 5) * 2) * FF + cg * 256 + (tid & 31) * 8;
  const u32x4 cw0 = *(const u32x4*)cp;
  const u32x4 cw1 = *(const u32x4*)(cp + FF);
  __builtin_amdgcn_s_barrier();
  STAMP(4, 1);
  u16* op = o + (size_t)(qw + fr) * DM + head * DH;
  if (q0 < WIN) {
    const int tmin = (WIN - qw) >> 4;
    attn_body<true>(smem, smem + NKEY * 64, qf, wid, lane, tmin, op);
  } else {
    attn_body<false>(smem, smem + NKEY * 64, qf, wid, lane, 0, op);
  }
  STAMP(4, 2);
  if (bid < 448) {
    __syncthreads();
    float* red = (float*)smem;
    f32x4 lo, hi;
#pragma unroll
    for (int e = 0; e < 2; ++e) {
      lo[2 * e] = bflo(cw0[e]) + bflo(cw1[e]);
      lo[2 * e + 1] = bfhi(cw0[e]) + bfhi(cw1[e]);
      hi[2 * e] = bflo(cw0[2 + e]) + bflo(cw1[2 + e]);
      hi[2 * e + 1] = bfhi(cw0[2 + e]) + bfhi(cw1[2 + e]);
    }
    *(f32x4*)(red + (tid >> 5) * 256 + (tid & 31) * 8) = lo;
    *(f32x4*)(red + (tid >> 5) * 256 + (tid & 31) * 8 + 4) = hi;
    __syncthreads();
    if (tid < 256) {
      float t = 0.f;
#pragma unroll
      for (int i = 0; i < 16; ++i) t += red[i * 256 + tid];
      cspart[(size_t)kc * FF + cg * 256 + tid] = t;
    }
  }
  asm volatile("s_waitcnt vmcnt(0)" : "+v"(touch)::"memory");
  STAMP(4, 3);
}

__global__ __launch_bounds__(512, 1) void gemm_qkv_kernel(const u16* __restrict__ A, const u16* __restrict__ W,
                                                         u16* __restrict__ C, const char* __restrict__ t0,
                                                         const char* __restrict__ t1) {
  gemm_body<192, 3, EPI_QKV, 8, 16384, 57344>(t0, t1, A, W, C, nullptr, nullptr, nullptr, nullptr, nullptr, 3 * DM, DM);
}
__global__ __launch_bounds__(512, 1) void gemm_wo_kernel(const u16* __restrict__ A, const u16* __restrict__ W,
                                                        u16* __restrict__ C, const u16* __restrict__ res,
                                                        float* __restrict__ stats) {
  gemm_body<64, 6, EPI_RES_STATS, 4>(nullptr, nullptr, A, W, C, nullptr, res, stats, nullptr, nullptr, DM, DM);
}
__global__ __launch_bounds__(512, 1) void gemm_w1_kernel(const u16* __restrict__ A, const u16* __restrict__ W,
                                                        u16* __restrict__ C, const u16* __restrict__ bias,
                                                        float* __restrict__ stats, float* __restrict__ cspart,
                                                        const char* __restrict__ t0) {
  gemm_body<224, 3, EPI_LN_GELU, 8, 57344, 0>(t0, t0, A, W, C, bias, nullptr, stats, cspart, nullptr, FF, DM);
}
__global__ __launch_bounds__(512, 1) void gemm_w2_kernel(const u16* __restrict__ A, const u16* __restrict__ W,
                                                        u16* __restrict__ C, const u16* __restrict__ bias,
                                                        const u16* __restrict__ res, float* __restrict__ xchg,
                                                        unsigned* __restrict__ flags) {
  gemm_body<128, 4, EPI_SPLIT_BIAS_RES, 4>(nullptr, nullptr, A, W, C, bias, res, xchg, nullptr, flags, DM, FF);
}
extern "C" __global__ void zz_text_end() {}

constexpr int lds_bytes(int BN, int NBUF, bool side) { return NBUF * (128 * 64 * 2 + 64 * BN * 2) + (side ? 5120 : 0); }

extern "C" void kernel_launch(void* const* d_in, const int* in_sizes, int n_in, void* d_out, int out_size,
                              void* d_ws, size_t ws_size, hipStream_t stream) {
  const u16* x = (const u16*)d_in[0];
  const u16* wqkv = (const u16*)d_in[1];
  const u16* wo = (const u16*)d_in[2];
  const u16* w1 = (const u16*)d_in[4];
  const u16* b1 = (const u16*)d_in[5];
  const u16* w2 = (const u16*)d_in[6];
  const u16* b2 = (const u16*)d_in[7];
  u16* out = (u16*)d_out;
  char* ws = (char*)d_ws;
  u16* xn = (u16*)(ws);
  u16* qkv = (u16*)(ws + (4u << 20));
  u16* o = (u16*)(ws + (16u << 20));
  u16* h = (u16*)(ws + (20u << 20));
  u16* hh = (u16*)(ws + (28u << 20));
  float* stats = (float*)(ws + (44u << 20));
  float* cspart = (float*)(ws + (45u << 20));
  unsigned* flags = (unsigned*)(ws + (46u << 20));
  float* xchg = (float*)(ws + (48u << 20));

  (void)hipFuncSetAttribute((const void*)gemm_qkv_kernel, hipFuncAttributeMaxDynamicSharedMemorySize, lds_bytes(192, 3, true));
  (void)hipFuncSetAttribute((const void*)gemm_wo_kernel, hipFuncAttributeMaxDynamicSharedMemorySize, lds_bytes(64, 6, false));
  (void)hipFuncSetAttribute((const void*)gemm_w1_kernel, hipFuncAttributeMaxDynamicSharedMemorySize, lds_bytes(224, 3, true));
  (void)hipFuncSetAttribute((const void*)gemm_w2_kernel, hipFuncAttributeMaxDynamicSharedMemorySize, lds_bytes(128, 4, false));
  prep_kernel<<<S_LEN / 4, 256, 0, stream>>>(x, xn, (const char*)wqkv, (const char*)b1, (const char*)b2, flags);
  gemm_qkv_kernel<<<256, 512, lds_bytes(192, 3, true), stream>>>(xn, wqkv, qkv, (const char*)wo, (const char*)w1);
  attn_kernel<<<NH * (S_LEN / 128), 512, 0, stream>>>(qkv, o, (const char*)wo, w1, (const char*)w2, cspart);
  gemm_wo_kernel<<<256, 512, lds_bytes(64, 6, false), stream>>>(o, wo, h, x, stats);
  gemm_w1_kernel<<<256, 512, lds_bytes(224, 3, true), stream>>>(h, w1, hh, b1, stats, cspart, (const char*)w2);
  gemm_w2_kernel<<<256, 512, lds_bytes(128, 4, false), stream>>>(hh, w2, out, b2, h, xchg, flags);
}
```

```cpp
#include <hip/hip_runtime.h>
#include <hip/hip_bf16.h>
#include <stdint.h>

typedef unsigned short u16;
typedef __attribute__((ext_vector_type(8))) short bf16x8;
typedef __attribute__((ext_vector_type(4))) short s16x4;
typedef __attribute__((ext_vector_type(4))) float f32x4;
typedef __attribute__((ext_vector_type(2))) float f32x2;
typedef __attribute__((ext_vector_type(4))) unsigned u32x4;
typedef __attribute__((ext_vector_type(2))) unsigned u32x2;
typedef __attribute__((ext_vector_type(2))) __bf16 bf16x2_t;
typedef __attribute__((address_space(3))) const char* lds_cptr;

#define STAMP(kid, i)
__device__ __forceinline__ void st16_sc1(void* p, __attribute__((ext_vector_type(4))) unsigned v) { asm volatile("global_store_dwordx4 %0, %1, off sc1\n\ts_nop 1" :: "v"(p), "v"(v) : "memory"); }
#define ST16(p, v) st16_sc1((void*)(p), (v))
constexpr int S_LEN = 2048, DM = 1024, NH = 32, DH = 32, WIN = 256, FF = 3584;
constexpr float LN_EPS = 1e-5f;

__device__ __forceinline__ float bf2f(u16 v) { return __uint_as_float(((unsigned)v) << 16); }
__device__ __forceinline__ float bflo(unsigned v) { return __uint_as_float(v << 16); }
__device__ __forceinline__ float bfhi(unsigned v) { return __uint_as_float(v & 0xffff0000u); }
__device__ __forceinline__ unsigned pk2(float lo, float hi) {
  return __builtin_bit_cast(unsigned, __builtin_convertvector(f32x2{lo, hi}, bf16x2_t));
}
__device__ __forceinline__ void glds16(const void* sbase, unsigned voff, unsigned lds_addr) {
  unsigned sv;
  asm volatile("s_mov_b32 %0, m0\n\ts_mov_b32 m0, %3\n\ts_nop 0\n\tglobal_load_lds_dwordx4 %1, %2\n\ts_mov_b32 m0, %0"
               : "=&s"(sv) : "v"(voff), "s"(sbase), "s"(lds_addr) : "memory");
}
__device__ __forceinline__ void glds16_sc1(const void* sbase, unsigned voff, unsigned lds_addr) {
  unsigned sv;
  asm volatile("s_mov_b32 %0, m0\n\ts_mov_b32 m0, %3\n\ts_nop 0\n\tglobal_load_lds_dwordx4 %1, %2 sc1\n\ts_mov_b32 m0, %0"
               : "=&s"(sv) : "v"(voff), "s"(sbase), "s"(lds_addr) : "memory");
}
__device__ __forceinline__ s16x4 vtr(lds_cptr p) {
  return __builtin_bit_cast(s16x4, __builtin_amdgcn_ds_read_tr16_b64_v4i16((__attribute__((address_space(3))) s16x4*)p));
}
template <int CTRL>
__device__ __forceinline__ float dpp_add(float v) {
  return v + __int_as_float(__builtin_amdgcn_update_dpp(0, __float_as_int(v), CTRL, 0xf, 0xf, false));
}
__device__ __forceinline__ float sum8(float v) {
  v = dpp_add<0xB1>(v);
  v = dpp_add<0x4E>(v);
  return dpp_add<0x141>(v);
}
__device__ __forceinline__ float xsum16_32(float m) {
  auto a = __builtin_amdgcn_permlane32_swap(__float_as_uint(m), __float_as_uint(m), false, false);
  m = __uint_as_float(a[0]) + __uint_as_float(a[1]);
  auto b = __builtin_amdgcn_permlane16_swap(__float_as_uint(m), __float_as_uint(m), false, false);
  return __uint_as_float(b[0]) + __uint_as_float(b[1]);
}
__device__ __forceinline__ float xmax16_32(float m) {
  auto a = __builtin_amdgcn_permlane32_swap(__float_as_uint(m), __float_as_uint(m), false, false);
  m = fmaxf(__uint_as_float(a[0]), __uint_as_float(a[1]));
  auto b = __builtin_amdgcn_permlane16_swap(__float_as_uint(m), __float_as_uint(m), false, false);
  return fmaxf(__uint_as_float(b[0]), __uint_as_float(b[1]));
}
__device__ __forceinline__ float wave_sum(float v) {
  v = sum8(v);
  v = dpp_add<0x140>(v);
  return xsum16_32(v);
}

__global__ __launch_bounds__(256) void prep_kernel(const u16* __restrict__ in, u16* __restrict__ out,
                                                   const char* __restrict__ wqkv, const char* __restrict__ b1,
                                                   const char* __restrict__ b2, unsigned* __restrict__ flags) {
  const int lane = threadIdx.x & 63;
  const int row = blockIdx.x * 4 + (threadIdx.x >> 6);
  const u32x4* src = (const u32x4*)(in + (size_t)row * DM);
  u32x4 a = src[lane], b = src[lane + 64];
  const int gi = blockIdx.x * 256 + threadIdx.x;
  unsigned w0 = 0;
  if (gi < 49152) w0 = *(const unsigned*)(wqkv + (size_t)gi * 128);
  else if (gi < 49152 + 56) w0 = *(const unsigned*)(b1 + (size_t)(gi - 49152) * 128);
  else if (gi < 49152 + 72) w0 = *(const unsigned*)(b2 + (size_t)(gi - 49152 - 56) * 128);
  if (gi < 768) flags[gi * 32] = 0u;
  unsigned w1c = 0;
  if (threadIdx.x < 64 && blockIdx.x < 128) {
    unsigned long long pc, pe;
    asm volatile("s_getpc_b64 %0" : "=s"(pc));
    asm volatile("s_getpc_b64 s[40:41]\n\ts_add_u32 s40, s40, zz_text_end@rel32@lo+4\n\t"
                 "s_addc_u32 s41, s41, zz_text_end@rel32@hi+12\n\ts_mov_b64 %0, s[40:41]"
                 : "=s"(pe) : : "s40", "s41", "scc");
    const unsigned long long a = (pc & ~127ull) + (unsigned long long)((blockIdx.x >> 3) * 64 + lane) * 128;
    if (a + 4 <= pe) w1c = *(const unsigned*)a;
  }
  float v[16];
#pragma unroll
  for (int i = 0; i < 4; ++i) {
    v[2 * i] = bflo(a[i]);
    v[2 * i + 1] = bfhi(a[i]);
    v[8 + 2 * i] = bflo(b[i]);
    v[8 + 2 * i + 1] = bfhi(b[i]);
  }
  float s = 0.f;
#pragma unroll
  for (int i = 0; i < 16; ++i) s += v[i];
  const float mean = wave_sum(s) * (1.0f / DM);
  float q = 0.f;
#pragma unroll
  for (int i = 0; i < 16; ++i) { v[i] -= mean; q += v[i] * v[i]; }
  const float var = wave_sum(q) * (1.0f / DM);
  const float r = 1.0f / sqrtf(var + LN_EPS);
  u32x4 oa, ob;
#pragma unroll
  for (int i = 0; i < 4; ++i) {
    oa[i] = pk2(v[2 * i] * r, v[2 * i + 1] * r);
    ob[i] = pk2(v[8 + 2 * i] * r, v[8 + 2 * i + 1] * r);
  }
  u32x4* dst = (u32x4*)(out + (size_t)row * DM);
  dst[lane] = oa;
  dst[lane + 64] = ob;
  asm volatile("" ::"v"(w0), "v"(w1c));
}

__device__ __forceinline__ float rowmax17(const f32x4* s) {
  float a = fmaxf(fmaxf(s[0][0], s[0][1]), fmaxf(s[0][2], s[0][3]));
#pragma unroll
  for (int t = 1; t < 17; ++t) {
    a = fmaxf(fmaxf(a, s[t][0]), s[t][1]);
    a = fmaxf(fmaxf(a, s[t][2]), s[t][3]);
  }
  return a;
}

template <bool EARLY>
__device__ __forceinline__ void attn_body(const char* ks, const char* vs, bf16x8 qf, int wid, int lane, int tmin,
                                          char* op) {
  const int fr = lane & 15, g = lane >> 4;
  const float NEG = -1e30f;
  f32x4 s[17];
  const char* kp = ks + (wid * 16 + fr) * 64 + ((g ^ ((4 - ((fr >> 2) & 3)) & 3)) << 4);
#pragma unroll
  for (int t = 0; t < 17; ++t) {
    bf16x8 kf = *(const bf16x8*)(kp + t * 1024);
    s[t] = __builtin_amdgcn_mfma_f32_16x16x32_bf16(kf, qf, f32x4{0.f, 0.f, 0.f, 0.f}, 0, 0, 0);
  }
#pragma unroll
  for (int r = 0; r < 4; ++r) {
    s[0][r] = (4 * g + r > fr) ? s[0][r] : NEG;
    s[16][r] = (4 * g + r <= fr) ? s[16][r] : NEG;
  }
  if (EARLY) {
#pragma unroll
    for (int t = 0; t < 16; ++t)
      if (t < tmin) s[t] = f32x4{NEG, NEG, NEG, NEG};
  }
  const float mx = xmax16_32(rowmax17(s));
  const float c = 0.17677669529663687f * 1.4426950408889634f;
  const f32x2 c2 = {c, c};
  const f32x2 nmc2 = {-mx * c, -mx * c};
  f32x2 sum2 = {0.f, 0.f};
  unsigned pk[18][2];
#pragma unroll
  for (int t = 0; t < 17; ++t) {
#pragma unroll
    for (int hh = 0; hh < 2; ++hh) {
      const f32x2 z = f32x2{s[t][2 * hh], s[t][2 * hh + 1]} * c2 + nmc2;
      const f32x2 p = {__builtin_amdgcn_exp2f(z[0]), __builtin_amdgcn_exp2f(z[1])};
      sum2 += p;
      pk[t][hh] = pk2(p[0], p[1]);
    }
  }
  float sum = sum2[0] + sum2[1];
  pk[17][0] = 0u;
  pk[17][1] = 0u;
  sum = xsum16_32(sum);
  const int q4 = (lane >> 2) & 3, pp = lane & 3;
  const char* vp0 = vs + (wid * 16 + 4 * g + q4) * 64 + ((g & 1) << 5) + pp * 8;
  const char* vp1 = vs + (wid * 16 + 4 * g + q4) * 64 + (((g & 1) ^ 1) << 5) + pp * 8;
  f32x4 o0 = {0.f, 0.f, 0.f, 0.f}, o1 = {0.f, 0.f, 0.f, 0.f};
#pragma unroll
  for (int tp = 0; tp < 9; ++tp) {
    const int t0 = 2 * tp, t1 = (2 * tp + 1 < 17) ? 2 * tp + 1 : 16;
    u32x4 pb = {pk[2 * tp][0], pk[2 * tp][1], pk[2 * tp + 1][0], pk[2 * tp + 1][1]};
    bf16x8 pf = __builtin_bit_cast(bf16x8, pb);
    s16x4 a0 = vtr((lds_cptr)(vp0 + t0 * 1024)), a1 = vtr((lds_cptr)(vp0 + t1 * 1024));
    s16x4 b0 = vtr((lds_cptr)(vp1 + t0 * 1024)), b1 = vtr((lds_cptr)(vp1 + t1 * 1024));
    o0 = __builtin_amdgcn_mfma_f32_16x16x32_bf16(__builtin_shufflevector(a0, a1, 0, 1, 2, 3, 4, 5, 6, 7), pf, o0, 0, 0, 0);
    o1 = __builtin_amdgcn_mfma_f32_16x16x32_bf16(__builtin_shufflevector(b0, b1, 0, 1, 2, 3, 4, 5, 6, 7), pf, o1, 0, 0, 0);
  }
  const float inv = 1.0f / sum;
  u32x2 w0, w1;
  w0[0] = pk2(o0[0] * inv, o0[1] * inv);
  w0[1] = pk2(o0[2] * inv, o0[3] * inv);
  w1[0] = pk2(o1[0] * inv, o1[1] * inv);
  w1[1] = pk2(o1[2] * inv, o1[3] * inv);
  *(u32x2*)(op + 8 * g) = w0;
  *(u32x2*)(op + 32 + 8 * g) = w1;
}

enum { EPI_QKV = 0, EPI_RES_STATS = 1, EPI_LN_GELU = 2, EPI_SPLIT_BIAS_RES = 3 };
constexpr int NLW = 4;

template <int CNT, int NBUF, int NPA, int STAGE>
__device__ __forceinline__ void loader_loop(const char* Ab, const char* Wb, const unsigned* voff, int lw, int nk,
                                            unsigned wstep, unsigned lds0) {
  const char* sp[CNT];
  unsigned st[CNT];
#pragma unroll
  for (int i = 0; i < CNT; ++i) {
    const bool isA = (lw + i * NLW) < NPA;
    sp[i] = isA ? Ab : Wb;
    st[i] = isA ? 128u : wstep;
  }
  auto issue = [&](int buf) {
#pragma unroll
    for (int i = 0; i < CNT; ++i) {
      glds16(sp[i], voff[i], lds0 + buf * STAGE + (lw + i * NLW) * 1024);
      sp[i] += st[i];
    }
  };
  issue(0);
  issue(1);
  asm volatile("s_waitcnt vmcnt(%0)" ::"i"(CNT) : "memory");
  __builtin_amdgcn_s_barrier();
#pragma unroll
  for (int s = 2; s < NBUF; ++s) issue(s);
  int buf = 0;
  for (int t = 0; t < nk; ++t) {
    if (t + NBUF <= nk) {
      asm volatile("s_waitcnt vmcnt(%0)" ::"i"((NBUF - 2) * CNT) : "memory");
    } else {
      asm volatile("s_waitcnt vmcnt(0)" ::: "memory");
    }
    __builtin_amdgcn_s_barrier();
    if (t + NBUF < nk) issue(buf);
    buf = (buf + 1 == NBUF) ? 0 : buf + 1;
  }
}

template <int BN, int NBUF, int EPI, int XA, int TL0 = 0, int TL1 = 0>
__device__ __forceinline__ void gemm_body(const char* __restrict__ tp0, const char* __restrict__ tp1, const u16* __restrict__ A, const u16* __restrict__ W,
                                          u16* __restrict__ C, const u16* __restrict__ bias,
                                          const u16* __restrict__ res, float* __restrict__ aux0,
                                          float* __restrict__ aux1, unsigned* __restrict__ flags,
                                          const int N, const int K) {
  constexpr int BM = 128;
  constexpr bool WIDE = BN > 64;
  constexpr bool SPLIT = (EPI == EPI_SPLIT_BIAS_RES);
  constexpr int NTHR = 512;
  constexpr int NG = BN / 64;
  constexpr bool HALF = (BN % 64) != 0;
  constexpr int NT = WIDE ? BN / 32 : 4;
  constexpr int ABYTES = BM * 128, WBYTES = 64 * BN * 2, STAGE = ABYTES + WBYTES;
  constexpr int NPA = ABYTES / 1024, NP = STAGE / 1024;
  constexpr int CMAX = (NP + NLW - 1) / NLW;
  constexpr int TSCR = NBUF * STAGE + 4096;
  constexpr int SIDE = NBUF * STAGE;
  extern __shared__ __attribute__((aligned(16))) char smem[];

  const int tid = threadIdx.x, lane = tid & 63;
  const int wid = __builtin_amdgcn_readfirstlane(tid >> 6);
  STAMP(EPI, 0);
  const int xc = blockIdx.x & 7, rr = blockIdx.x >> 3;
  int bm, bn, hk = 0;
  if (SPLIT) {
    hk = rr & 1;
    const int tt = rr >> 1;
    bm = (xc & 3) * 4 + (tt & 3);
    bn = (xc >> 2) * 4 + (tt >> 2);
  } else {
    constexpr int XB = 32 / XA, PM = 16 / XA;
    bm = (xc % PM) * XA + (rr % XA);
    bn = (xc / PM) * XB + (rr / XA);
  }
  const int m0 = bm * BM, n0 = bn * BN;
  const int nk = SPLIT ? K / 128 : K / 64;
  const int kbeg = hk * (K / 2);
  const int odd = HALF ? (bn & 1) : 0;
  const int cfull = odd ? 32 : 0, chalf = odd ? 0 : NG * 64;
  const unsigned lds0 = (unsigned)(uintptr_t)(__attribute__((address_space(3))) char*)smem;
  const int fr = lane & 15, g = lane >> 4;

  f32x4 acc[4][NT];
  const int wm = wid & 1, wx = (wid >> 1) & 1;
  int tcol[NT];

  if (wid >= 4) {
    const int lw = wid - 4;
    unsigned voff[CMAX];
#pragma unroll
    for (int i = 0; i < CMAX; ++i) {
      const int idx = lw + i * NLW;
      if (idx < NPA) {
        const int row = idx * 8 + (lane >> 3), ch = (lane & 7) ^ (row & 7);
        voff[i] = (unsigned)(((m0 + row) * K + ch * 8) * 2);
      } else if (idx < NPA + NG * 8) {
        const int w = idx - NPA, G = w >> 3, k = (w & 7) * 8 + (lane >> 3);
        const int ch = (lane & 7) ^ ((k & 2) | ((k >> 1) & 4));
        const int col = (EPI == EPI_QKV) ? G * DM + bn * 64 : n0 + cfull + G * 64;
        voff[i] = (unsigned)((k * N + col + ch * 8) * 2);
      } else {
        const int w = idx - NPA - NG * 8, k = w * 16 + (lane >> 2);
        const int ch = (lane & 3) ^ ((k >> 2) & 2);
        voff[i] = (unsigned)((k * N + n0 + chalf + ch * 8) * 2);
      }
    }
    const unsigned wstep = (unsigned)(64 * N * 2);
    const char* Ab = (const char*)(A + kbeg);
    const char* Wb = (const char*)(W + (size_t)kbeg * N);
    if (lw + (CMAX - 1) * NLW < NP)
      loader_loop<CMAX, NBUF, NPA, STAGE>(Ab, Wb, voff, lw, nk, wstep, lds0);
    else
      loader_loop<CMAX - 1, NBUF, NPA, STAGE>(Ab, Wb, voff, lw, nk, wstep, lds0);
  } else {
    if (EPI == EPI_LN_GELU) {
      if (tid < 128) {
        const f32x4* sp = (const f32x4*)(aux0 + (size_t)(m0 + tid) * 32);
        f32x4 p[8];
#pragma unroll
        for (int i = 0; i < 8; ++i) p[i] = sp[i];
        float mean = 0.f;
#pragma unroll
        for (int i = 0; i < 8; ++i) mean += p[i][0] + p[i][2];
        mean *= (1.0f / 16.0f);
        float m2 = 0.f;
#pragma unroll
        for (int i = 0; i < 8; ++i) {
          const float d0 = p[i][0] - mean, d1 = p[i][2] - mean;
          m2 += p[i][1] + p[i][3] + 64.0f * (d0 * d0 + d1 * d1);
        }
        const float var = m2 * (1.0f / DM);
        const float r1 = 1.0f / sqrtf(var + LN_EPS);
        const float r2 = 1.0f / sqrtf(var * r1 * r1 + LN_EPS);
        *(f32x2*)(smem + SIDE + tid * 8) = f32x2{mean, r1 * r2};
      } else if (tid - 128 < BN / 2) {
        const int c = (tid - 128) * 2;
        f32x2 cs = {0.f, 0.f};
#pragma unroll
        for (int kc = 0; kc < 16; ++kc) cs += *(const f32x2*)(aux1 + (size_t)kc * FF + n0 + c);
        const unsigned bb = *(const unsigned*)(bias + n0 + c);
        *(f32x4*)(smem + SIDE + 1024 + c * 8) = f32x4{cs[0], bflo(bb), cs[1], bfhi(bb)};
      }
    }
    const int q = (lane >> 2) & 3, pp = lane & 3;
    const int wk = WIDE ? 0 : wx;
    int aoff[2];
#pragma unroll
    for (int ks = 0; ks < 2; ++ks) aoff[ks] = (wm * 64 + fr) * 128 + (((ks * 4 + g) ^ (fr & 7)) << 4);
    int tb[NT];
    const int f1 = (q & 2) | ((g & 1) << 2);
#pragma unroll
    for (int j = 0; j < NT; ++j) {
      int G, tp;
      if (!WIDE) { G = 0; tp = j; }
      else if (EPI == EPI_QKV) { G = (j >> 1) ^ 2; G = G == 3 ? 1 : G; tp = wx * 2 + (j & 1); }
      else if (j < 4) { G = wx * (NG - 1); tp = j; }
      else { G = 1; tp = wx * 2 + (j - 4); }
      if (WIDE && HALF && j == NT - 1) {
        tb[j] = ABYTES + NG * 8192 + (8 * g + q) * 64 + (((2 * wx + (pp >> 1)) ^ ((g & 1) << 1)) << 4) + (pp & 1) * 8;
        tcol[j] = chalf + wx * 16;
      } else {
        tb[j] = ABYTES + G * 8192 + (8 * g + q) * 128 + (((2 * tp + (pp >> 1)) ^ f1) << 4) + (pp & 1) * 8;
        tcol[j] = cfull + G * 64 + tp * 16;
      }
    }
#pragma unroll
    for (int i = 0; i < 4; ++i)
#pragma unroll
      for (int j = 0; j < NT; ++j) acc[i][j] = f32x4{0.f, 0.f, 0.f, 0.f};

    bf16x8 xa[4], wa[NT], xb[4], wb[NT];
    auto rd = [&](bf16x8* xf, bf16x8* wf, int buf, int ks) {
      const char* base = smem + buf * STAGE;
#pragma unroll
      for (int j = 0; j < NT; ++j) {
        const int rs = (WIDE && HALF && j == NT - 1) ? 64 : 128;
        s16x4 lo = vtr((lds_cptr)(base + tb[j] + ks * 32 * rs));
        s16x4 hi = vtr((lds_cptr)(base + tb[j] + ks * 32 * rs + 4 * rs));
        wf[j] = __builtin_shufflevector(lo, hi, 0, 1, 2, 3, 4, 5, 6, 7);
      }
#pragma unroll
      for (int i = 0; i < 4; ++i) xf[i] = *(const bf16x8*)(base + aoff[ks] + i * 2048);
    };
    auto mm = [&](const bf16x8* xf, const bf16x8* wf) {
#pragma unroll
      for (int i = 0; i < 4; ++i)
#pragma unroll
        for (int j = 0; j < NT; ++j)
          acc[i][j] = __builtin_amdgcn_mfma_f32_16x16x32_bf16(wf[j], xf[i], acc[i][j], 0, 0, 0);
    };
#define INTERLEAVE() do { _Pragma("unroll") for (int z_ = 0; z_ < 2 * NT + 4; ++z_) { __builtin_amdgcn_sched_group_barrier(0x8, 1, 0); __builtin_amdgcn_sched_group_barrier(0x100, 1, 0); } __builtin_amdgcn_sched_group_barrier(0x8, 4 * NT - (2 * NT + 4), 0); } while (0)
    __builtin_amdgcn_s_waitcnt(0xC07F);
    __builtin_amdgcn_s_barrier();
    STAMP(EPI, 1);
    auto do_touch = [&](int i) {
      const int line = ((blockIdx.x * 4 + wid) * 2 + i) * 64 + lane;
      if (__builtin_amdgcn_readfirstlane(line) < TL0 + TL1) {
        const int lc = line < TL0 + TL1 ? line : TL0 + TL1 - 1;
        const char* p = lc < TL0 ? tp0 + (size_t)lc * 128 : tp1 + (size_t)(lc - TL0) * 128;
        unsigned sv;
        asm volatile("s_mov_b32 %0, m0\n\ts_mov_b32 m0, %2\n\ts_nop 0\n\tglobal_load_lds_dword %1, off\n\ts_mov_b32 m0, %0"
                     : "=&s"(sv) : "v"(p), "s"(lds0 + TSCR + wid * 256) : "memory");
      }
    };
    if (WIDE) {
      rd(xa, wa, 0, 0);
      int b0 = 0;
      for (int t = 0; t < nk; ++t) {
        if (TL0 + TL1 > 0) { if (t == 2) do_touch(0); if (t == 7) do_touch(1); }
        rd(xb, wb, b0, 1);
        b0 = (b0 + 1 == NBUF) ? 0 : b0 + 1;
        mm(xa, wa);
        INTERLEAVE();
        __builtin_amdgcn_sched_barrier(0);
        __builtin_amdgcn_s_waitcnt(0xC07F);
        __builtin_amdgcn_s_barrier();
        rd(xa, wa, b0, 0);
        mm(xb, wb);
        INTERLEAVE();
        __builtin_amdgcn_sched_barrier(0);
      }
    } else {
      rd(xa, wa, 0, wk);
      int b1 = 1;
      for (int t = 0; t < nk; t += 2) {
        __builtin_amdgcn_s_waitcnt(0xC07F);
        __builtin_amdgcn_s_barrier();
        rd(xb, wb, b1, wk);
        b1 = (b1 + 1 == NBUF) ? 0 : b1 + 1;
        mm(xa, wa);
        INTERLEAVE();
        __builtin_amdgcn_sched_barrier(0);
        __builtin_amdgcn_s_waitcnt(0xC07F);
        __builtin_amdgcn_s_barrier();
        rd(xa, wa, b1, wk);
        b1 = (b1 + 1 == NBUF) ? 0 : b1 + 1;
        mm(xb, wb);
        INTERLEAVE();
        __builtin_amdgcn_sched_barrier(0);
      }
    }
    __builtin_amdgcn_s_waitcnt(0xC07F);
    if (TL0 + TL1 > 0) asm volatile("s_waitcnt vmcnt(0)" ::: "memory");
    STAMP(EPI, 2);
    if (SPLIT) {
      const int tile = bm * 8 + bn;
      const auto rsrc = __builtin_amdgcn_make_buffer_rsrc(aux0, 0, 128 * 2 * 2 * 16384, 0x00020000);
      if (wm != hk) {
        const int slot = ((tile * 2 + hk) * 2 + wx);
#pragma unroll
        for (int i = 0; i < 4; ++i)
#pragma unroll
          for (int j = 0; j < NT; ++j)
            __builtin_amdgcn_raw_buffer_store_b128(__builtin_bit_cast(u32x4, acc[i][j]), rsrc,
                                                   slot * 16384 + (i * NT + j) * 1024 + lane * 16, 0, 16);
        asm volatile("s_waitcnt vmcnt(0)" ::: "memory");
        if (lane == 0) __hip_atomic_store(flags + slot * 32, 1u, __ATOMIC_RELAXED, __HIP_MEMORY_SCOPE_AGENT);
      } else {
        const int slot = ((tile * 2 + (hk ^ 1)) * 2 + wx);
        for (int spin = 0; spin < (1 << 24); ++spin) {
          if (__hip_atomic_load(flags + slot * 32, __ATOMIC_RELAXED, __HIP_MEMORY_SCOPE_AGENT) != 0u) break;
          __builtin_amdgcn_s_sleep(2);
        }
        asm volatile("" ::: "memory");
#pragma unroll
        for (int i = 0; i < 4; ++i) {
          u32x4 t[NT];
#pragma unroll
          for (int j = 0; j < NT; ++j)
            t[j] = __builtin_amdgcn_raw_buffer_load_b128(rsrc, slot * 16384 + (i * NT + j) * 1024 + lane * 16, 0, 16);
#pragma unroll
          for (int j = 0; j < NT; ++j) acc[i][j] += __builtin_bit_cast(f32x4, t[j]);
        }
      }
    }
  }
  __syncthreads();
  STAMP(EPI, 3);

  constexpr int CPR = BN / 8;
  if (EPI == EPI_QKV) {
    constexpr int KIMG = 0, VIMG = 49152, QIMG = 98304, RED = 114688, HIMG = 24576;
    u16* kvg = (u16*)aux0;
    u32x4 cw[8];
    const int t2 = tid - 256;
    const int cb = blockIdx.x < 224 ? blockIdx.x : 223;
    const int cg = cb % 14, kc = cb / 14;
    if (wid < 4) {
      auto put = [&](int j) {
        const int grp = tcol[j] >> 6, hd = (tcol[j] >> 5) & 1;
        const int dd = (tcol[j] & 31) + g * 4, chn = dd >> 3, hf = (dd >> 2) & 1;
        const int sw = (grp == 2) ? (((fr >> 2) & 1) << 1) : ((4 - ((fr >> 2) & 3)) & 3);
        const int base = (grp == 0) ? QIMG + hd * 8192 : ((grp == 1) ? KIMG : VIMG) + hd * HIMG + 256 * 64;
#pragma unroll
        for (int i = 0; i < 4; ++i) {
          u32x2 o;
          o[0] = pk2(acc[i][j][0], acc[i][j][1]);
          o[1] = pk2(acc[i][j][2], acc[i][j][3]);
          *(u32x2*)(smem + base + (wm * 64 + i * 16 + fr) * 64 + ((chn ^ sw) << 4) + hf * 8) = o;
        }
      };
#pragma unroll
      for (int j = 0; j < 4; ++j) put(j);
      if (bm < 15) {
#pragma unroll
        for (int kv = 0; kv < 2; ++kv)
#pragma unroll
          for (int pc = 0; pc < 4; ++pc) {
            const int row = wm * 64 + pc * 16 + (lane >> 2), slot = lane & 3;
            const int sw = kv ? (((row >> 2) & 1) << 1) : ((4 - ((row >> 2) & 3)) & 3);
            const u32x4 v = *(const u32x4*)(smem + (kv ? VIMG : KIMG) + wx * HIMG + (256 + row) * 64 + slot * 16);
            ST16(kvg + ((size_t)(kv * NH + bn * 2 + wx) * S_LEN + m0 + row) * 32 + (slot ^ sw) * 8, v);
          }
      }
      put(4);
      put(5);
      if (bm < 15) {
        asm volatile("s_waitcnt vmcnt(0)" ::: "memory");
        if (lane == 0)
          __hip_atomic_fetch_add(flags + (bm * 16 + bn) * 32, 1u, __ATOMIC_RELAXED, __HIP_MEMORY_SCOPE_AGENT);
      }
    } else {
      const u16* cp = res + (size_t)(kc * 64 + (t2 >> 5) * 8) * FF + cg * 256 + (t2 & 31) * 8;
#pragma unroll
      for (int i = 0; i < 8; ++i) cw[i] = *(const u32x4*)(cp + (size_t)i * FF);
      const int lw = wid - 4, pr = lw >> 1, kv = lw & 1;
      const int pb = bm - 2 + pr;
      const unsigned dst = lds0 + (kv ? VIMG : KIMG) + pr * 128 * 64;
      if (pb >= 0) {
        const unsigned* fp = flags + (pb * 16 + bn) * 32;
        for (int spin = 0; spin < (1 << 24); ++spin) {
          if (__hip_atomic_load(fp, __ATOMIC_RELAXED, __HIP_MEMORY_SCOPE_AGENT) >= 4u) break;
          __builtin_amdgcn_s_sleep(2);
        }
        asm volatile("" ::: "memory");
        const int kl = lane >> 2, slot = lane & 3;
        const int sw = kv ? (((kl >> 2) & 1) << 1) : ((4 - ((kl >> 2) & 3)) & 3);
        const unsigned vo = (unsigned)((kl * 32 + (slot ^ sw) * 8) * 2);
#pragma unroll
        for (int hd = 0; hd < 2; ++hd) {
          const u16* src = kvg + ((size_t)(kv * NH + bn * 2 + hd) * S_LEN + pb * 128) * 32;
#pragma unroll
          for (int pc = 0; pc < 8; ++pc) glds16_sc1(src + pc * 512, vo, dst + hd * HIMG + pc * 1024);
        }
      } else {
#pragma unroll
        for (int hd = 0; hd < 2; ++hd)
#pragma unroll
          for (int pc = 0; pc < 8; ++pc)
            *(u32x4*)(smem + (kv ? VIMG : KIMG) + pr * 128 * 64 + hd * HIMG + pc * 1024 + lane * 16) = u32x4{0u, 0u, 0u, 0u};
      }
      {
        float* red = (float*)(smem + RED);
        float sacc[8];
#pragma unroll
        for (int e = 0; e < 8; ++e) sacc[e] = 0.f;
#pragma unroll
        for (int i = 0; i < 8; ++i)
#pragma unroll
          for (int e = 0; e < 4; ++e) { sacc[2 * e] += bflo(cw[i][e]); sacc[2 * e + 1] += bfhi(cw[i][e]); }
        *(f32x4*)(red + (t2 >> 5) * 256 + (t2 & 31) * 8) = f32x4{sacc[0], sacc[1], sacc[2], sacc[3]};
        *(f32x4*)(red + (t2 >> 5) * 256 + (t2 & 31) * 8 + 4) = f32x4{sacc[4], sacc[5], sacc[6], sacc[7]};
      }
      asm volatile("s_waitcnt vmcnt(0)" ::: "memory");
    }
    STAMP(EPI, 5);
    __syncthreads();
    STAMP(EPI, 6);
    if (blockIdx.x < 224 && tid < 256) {
      const float* red = (const float*)(smem + RED);
      float t = 0.f;
#pragma unroll
      for (int i = 0; i < 8; ++i) t += red[i * 256 + tid];
      aux1[(size_t)kc * FF + cg * 256 + tid] = t;
    }
    const int qw = m0 + wid * 16;
    const int tmin = (qw < WIN) ? (WIN - qw) >> 4 : 0;
    const int qo = (wid * 16 + fr) * 64 + ((g ^ ((4 - ((fr >> 2) & 3)) & 3)) << 4);
    const bf16x8 qf0 = *(const bf16x8*)(smem + QIMG + qo), qf1 = *(const bf16x8*)(smem + QIMG + 8192 + qo);
    char* ost = smem + QIMG + wid * 1024;
#pragma unroll 1
    for (int hd = 0; hd < 2; ++hd) {
      const bf16x8 qf = hd ? qf1 : qf0;
      char* op = ost + hd * 8192 + fr * 64;
      if (bm < 2) attn_body<true>(smem + KIMG + hd * HIMG, smem + VIMG + hd * HIMG, qf, wid, lane, tmin, op);
      else attn_body<false>(smem + KIMG + hd * HIMG, smem + VIMG + hd * HIMG, qf, wid, lane, 0, op);
    }
#pragma unroll
    for (int i = 0; i < 2; ++i) {
      const int row = i * 8 + (lane >> 3), ch = lane & 7;
      const u32x4 v = *(const u32x4*)(ost + (ch >> 2) * 8192 + row * 64 + (ch & 3) * 16);
      ST16(C + (size_t)(qw + row) * DM + bn * 64 + ch * 8, v);
    }
    STAMP(EPI, 7);
  } else if (EPI == EPI_LN_GELU) {
    constexpr int RS = BN * 4 + 16;
    if (wid < 4) {
#pragma unroll
      for (int j = 0; j < NT; ++j)
#pragma unroll
        for (int i = 0; i < 4; ++i)
          *(f32x4*)(smem + (wm * 64 + i * 16 + fr) * RS + (tcol[j] + g * 4) * 4) = acc[i][j];
    }
    __syncthreads();
#pragma unroll
    for (int c = tid; c < BM * CPR; c += NTHR) {
      const int row = c / CPR, ch = c % CPR;
      const f32x4 a0 = *(const f32x4*)(smem + row * RS + ch * 32);
      const f32x4 a1 = *(const f32x4*)(smem + row * RS + ch * 32 + 16);
      const f32x2 rs = *(const f32x2*)(smem + SIDE + row * 8);
      float v[8];
#pragma unroll
      for (int r = 0; r < 4; ++r) {
        const f32x4 cb = *(const f32x4*)(smem + SIDE + 1024 + ch * 64 + r * 16);
        const float x0 = (r < 2) ? a0[2 * r] : a1[2 * r - 4];
        const float x1 = (r < 2) ? a0[2 * r + 1] : a1[2 * r - 3];
        v[2 * r] = (x0 - rs[0] * cb[0]) * rs[1] + cb[1];
        v[2 * r + 1] = (x1 - rs[0] * cb[2]) * rs[1] + cb[3];
      }
      u32x4 o;
#pragma unroll
      for (int r = 0; r < 4; ++r) {
        const f32x2 x = {v[2 * r], v[2 * r + 1]};
        const f32x2 x2 = x * x;
        const f32x2 w = x2 * f32x2{-0.10294324f, -0.10294324f} + f32x2{-2.302208198f, -2.302208198f};
        const f32x2 z = x * w;
        f32x2 e = {__builtin_amdgcn_exp2f(z[0]), __builtin_amdgcn_exp2f(z[1])};
        e = e + f32x2{1.0f, 1.0f};
        const f32x2 y = x * f32x2{__builtin_amdgcn_rcpf(e[0]), __builtin_amdgcn_rcpf(e[1])};
        o[r] = pk2(y[0], y[1]);
      }
      ST16(C + (size_t)(m0 + row) * N + n0 + ch * 8, o);
    }
  } else if (EPI == EPI_RES_STATS) {
    constexpr int RS = BN * 4 + 16;
    constexpr int PART = BM * RS;
    if (wid < 4) {
#pragma unroll
      for (int i = 0; i < 4; ++i)
#pragma unroll
        for (int j = 0; j < NT; ++j)
          *(f32x4*)(smem + wx * PART + (wm * 64 + i * 16 + fr) * RS + (j * 16 + g * 4) * 4) = acc[i][j];
    }
    __syncthreads();
#pragma unroll
    for (int c = tid; c < BM * CPR; c += NTHR) {
      const int row = c / CPR, ch = c % CPR;
      const f32x4 a0 = *(const f32x4*)(smem + row * RS + ch * 32);
      const f32x4 a1 = *(const f32x4*)(smem + row * RS + ch * 32 + 16);
      const f32x4 b0 = *(const f32x4*)(smem + PART + row * RS + ch * 32);
      const f32x4 b1 = *(const f32x4*)(smem + PART + row * RS + ch * 32 + 16);
      const u32x4 rv = *(const u32x4*)(res + (size_t)(m0 + row) * N + n0 + ch * 8);
      u32x4 o;
      o[0] = pk2(a0[0] + b0[0] + bflo(rv[0]), a0[1] + b0[1] + bfhi(rv[0]));
      o[1] = pk2(a0[2] + b0[2] + bflo(rv[1]), a0[3] + b0[3] + bfhi(rv[1]));
      o[2] = pk2(a1[0] + b1[0] + bflo(rv[2]), a1[1] + b1[1] + bfhi(rv[2]));
      o[3] = pk2(a1[2] + b1[2] + bflo(rv[3]), a1[3] + b1[3] + bfhi(rv[3]));
      ST16(C + (size_t)(m0 + row) * N + n0 + ch * 8, o);
      float s = 0.f;
#pragma unroll
      for (int r = 0; r < 4; ++r) s += bflo(o[r]) + bfhi(o[r]);
      const float mean = sum8(s) * (1.0f / 64.0f);
      float m2 = 0.f;
#pragma unroll
      for (int r = 0; r < 4; ++r) {
        const float d0 = bflo(o[r]) - mean, d1 = bfhi(o[r]) - mean;
        m2 += d0 * d0 + d1 * d1;
      }
      m2 = sum8(m2);
      if (ch == 0) *(f32x2*)(aux0 + ((size_t)(m0 + row) * 16 + bn) * 2) = f32x2{mean, m2};
    }
  } else {
    constexpr int RS = BN * 4 + 16;
    if (wid < 4 && wm == hk) {
#pragma unroll
      for (int i = 0; i < 4; ++i)
#pragma unroll
        for (int j = 0; j < NT; ++j)
          *(f32x4*)(smem + (i * 16 + fr) * RS + (tcol[j] + g * 4) * 4) = acc[i][j];
    }
    __syncthreads();
#pragma unroll
    for (int c = tid; c < 64 * CPR; c += NTHR) {
      const int row = c / CPR, ch = c % CPR;
      const f32x4 a0 = *(const f32x4*)(smem + row * RS + ch * 32);
      const f32x4 a1 = *(const f32x4*)(smem + row * RS + ch * 32 + 16);
      const u32x4 bb = *(const u32x4*)(bias + n0 + ch * 8);
      const size_t go = (size_t)(m0 + hk * 64 + row) * N + n0 + ch * 8;
      const u32x4 rv = *(const u32x4*)(res + go);
      u32x4 o;
      o[0] = pk2(a0[0] + bflo(bb[0]) + bflo(rv[0]), a0[1] + bfhi(bb[0]) + bfhi(rv[0]));
      o[1] = pk2(a0[2] + bflo(bb[1]) + bflo(rv[1]), a0[3] + bfhi(bb[1]) + bfhi(rv[1]));
      o[2] = pk2(a1[0] + bflo(bb[2]) + bflo(rv[2]), a1[1] + bfhi(bb[2]) + bfhi(rv[2]));
      o[3] = pk2(a1[2] + bflo(bb[3]) + bflo(rv[3]), a1[3] + bfhi(bb[3]) + bfhi(rv[3]));
      ST16(C + go, o);
    }
  }
}

__global__ __launch_bounds__(512, 1) void qkv_attn_kernel(const u16* __restrict__ A, const u16* __restrict__ W,
                                                         u16* __restrict__ o, const char* __restrict__ wo,
                                                         const u16* __restrict__ w1, float* __restrict__ kvg,
                                                         float* __restrict__ cspart, unsigned* __restrict__ flags) {
  gemm_body<192, 3, EPI_QKV, 8, 16384, 57344>(wo, (const char*)w1, A, W, o, nullptr, w1, kvg, cspart, flags, 3 * DM, DM);
}
__global__ __launch_bounds__(512, 1) void gemm_wo_kernel(const u16* __restrict__ A, const u16* __restrict__ W,
                                                        u16* __restrict__ C, const u16* __restrict__ res,
                                                        float* __restrict__ stats) {
  gemm_body<64, 6, EPI_RES_STATS, 4>(nullptr, nullptr, A, W, C, nullptr, res, stats, nullptr, nullptr, DM, DM);
}
__global__ __launch_bounds__(512, 1) void gemm_w1_kernel(const u16* __restrict__ A, const u16* __restrict__ W,
                                                        u16* __restrict__ C, const u16* __restrict__ bias,
                                                        float* __restrict__ stats, float* __restrict__ cspart,
                                                        const char* __restrict__ t0) {
  gemm_body<224, 3, EPI_LN_GELU, 8, 57344, 0>(t0, t0, A, W, C, bias, nullptr, stats, cspart, nullptr, FF, DM);
}
__global__ __launch_bounds__(512, 1) void gemm_w2_kernel(const u16* __restrict__ A, const u16* __restrict__ W,
                                                        u16* __restrict__ C, const u16* __restrict__ bias,
                                                        const u16* __restrict__ res, float* __restrict__ xchg,
                                                        unsigned* __restrict__ flags) {
  gemm_body<128, 4, EPI_SPLIT_BIAS_RES, 4>(nullptr, nullptr, A, W, C, bias, res, xchg, nullptr, flags, DM, FF);
}
extern "C" __global__ void zz_text_end() {}

constexpr int lds_bytes(int BN, int NBUF, bool side) { return NBUF * (128 * 64 * 2 + 64 * BN * 2) + (side ? 5120 : 0); }

extern "C" void kernel_launch(void* const* d_in, const int* in_sizes, int n_in, void* d_out, int out_size,
                              void* d_ws, size_t ws_size, hipStream_t stream) {
  const u16* x = (const u16*)d_in[0];
  const u16* wqkv = (const u16*)d_in[1];
  const u16* wo = (const u16*)d_in[2];
  const u16* w1 = (const u16*)d_in[4];
  const u16* b1 = (const u16*)d_in[5];
  const u16* w2 = (const u16*)d_in[6];
  const u16* b2 = (const u16*)d_in[7];
  u16* out = (u16*)d_out;
  char* ws = (char*)d_ws;
  u16* xn = (u16*)(ws);
  u16* qkv = (u16*)(ws + (4u << 20));
  u16* o = (u16*)(ws + (16u << 20));
  u16* h = (u16*)(ws + (20u << 20));
  u16* hh = (u16*)(ws + (28u << 20));
  float* stats = (float*)(ws + (44u << 20));
  float* cspart = (float*)(ws + (45u << 20));
  unsigned* flags = (unsigned*)(ws + (46u << 20));
  float* xchg = (float*)(ws + (48u << 20));

  (void)hipFuncSetAttribute((const void*)qkv_attn_kernel, hipFuncAttributeMaxDynamicSharedMemorySize, lds_bytes(192, 3, true));
  (void)hipFuncSetAttribute((const void*)gemm_wo_kernel, hipFuncAttributeMaxDynamicSharedMemorySize, lds_bytes(64, 6, false));
  (void)hipFuncSetAttribute((const void*)gemm_w1_kernel, hipFuncAttributeMaxDynamicSharedMemorySize, lds_bytes(224, 3, true));
  (void)hipFuncSetAttribute((const void*)gemm_w2_kernel, hipFuncAttributeMaxDynamicSharedMemorySize, lds_bytes(128, 4, false));
  prep_kernel<<<S_LEN / 4, 256, 0, stream>>>(x, xn, (const char*)wqkv, (const char*)b1, (const char*)b2, flags);
  qkv_attn_kernel<<<256, 512, lds_bytes(192, 3, true), stream>>>(xn, wqkv, o, (const char*)wo, w1, (float*)qkv, cspart, flags + 512 * 32);
  gemm_wo_kernel<<<256, 512, lds_bytes(64, 6, false), stream>>>(o, wo, h, x, stats);
  gemm_w1_kernel<<<256, 512, lds_bytes(224, 3, true), stream>>>(h, w1, hh, b1, stats, cspart, (const char*)w2);
  gemm_w2_kernel<<<256, 512, lds_bytes(128, 4, false), stream>>>(hh, w2, out, b2, h, xchg, flags);
}
```

```cpp
#include <hip/hip_runtime.h>
#include <hip/hip_bf16.h>
#include <stdint.h>

typedef unsigned short u16;
typedef __attribute__((ext_vector_type(8))) short bf16x8;
typedef __attribute__((ext_vector_type(4))) short s16x4;
typedef __attribute__((ext_vector_type(4))) float f32x4;
typedef __attribute__((ext_vector_type(2))) float f32x2;
typedef __attribute__((ext_vector_type(4))) unsigned u32x4;
typedef __attribute__((ext_vector_type(2))) unsigned u32x2;
typedef __attribute__((ext_vector_type(2))) __bf16 bf16x2_t;
typedef __attribute__((address_space(3))) const char* lds_cptr;

#define STAMP(kid, i)
__device__ __forceinline__ void st16_sc1(void* p, __attribute__((ext_vector_type(4))) unsigned v) { asm volatile("global_store_dwordx4 %0, %1, off sc1\n\ts_nop 1" :: "v"(p), "v"(v) : "memory"); }
#define ST16(p, v) st16_sc1((void*)(p), (v))
constexpr int S_LEN = 2048, DM = 1024, NH = 32, DH = 32, WIN = 256, FF = 3584;
constexpr float LN_EPS = 1e-5f;

__device__ __forceinline__ float bf2f(u16 v) { return __uint_as_float(((unsigned)v) << 16); }
__device__ __forceinline__ float bflo(unsigned v) { return __uint_as_float(v << 16); }
__device__ __forceinline__ float bfhi(unsigned v) { return __uint_as_float(v & 0xffff0000u); }
__device__ __forceinline__ unsigned pk2(float lo, float hi) {
  return __builtin_bit_cast(unsigned, __builtin_convertvector(f32x2{lo, hi}, bf16x2_t));
}
__device__ __forceinline__ void glds16(const void* sbase, unsigned voff, unsigned lds_addr) {
  unsigned sv;
  asm volatile("s_mov_b32 %0, m0\n\ts_mov_b32 m0, %3\n\ts_nop 0\n\tglobal_load_lds_dwordx4 %1, %2\n\ts_mov_b32 m0, %0"
               : "=&s"(sv) : "v"(voff), "s"(sbase), "s"(lds_addr) : "memory");
}
__device__ __forceinline__ void glds16_sc1(const void* sbase, unsigned voff, unsigned lds_addr) {
  unsigned sv;
  asm volatile("s_mov_b32 %0, m0\n\ts_mov_b32 m0, %3\n\ts_nop 0\n\tglobal_load_lds_dwordx4 %1, %2 sc1\n\ts_mov_b32 m0, %0"
               : "=&s"(sv) : "v"(voff), "s"(sbase), "s"(lds_addr) : "memory");
}
__device__ __forceinline__ s16x4 vtr(lds_cptr p) {
  return __builtin_bit_cast(s16x4, __builtin_amdgcn_ds_read_tr16_b64_v4i16((__attribute__((address_space(3))) s16x4*)p));
}
template <int CTRL>
__device__ __forceinline__ float dpp_add(float v) {
  return v + __int_as_float(__builtin_amdgcn_update_dpp(0, __float_as_int(v), CTRL, 0xf, 0xf, false));
}
__device__ __forceinline__ float sum8(float v) {
  v = dpp_add<0xB1>(v);
  v = dpp_add<0x4E>(v);
  return dpp_add<0x141>(v);
}
__device__ __forceinline__ float xsum16_32(float m) {
  auto a = __builtin_amdgcn_permlane32_swap(__float_as_uint(m), __float_as_uint(m), false, false);
  m = __uint_as_float(a[0]) + __uint_as_float(a[1]);
  auto b = __builtin_amdgcn_permlane16_swap(__float_as_uint(m), __float_as_uint(m), false, false);
  return __uint_as_float(b[0]) + __uint_as_float(b[1]);
}
__device__ __forceinline__ float xmax16_32(float m) {
  auto a = __builtin_amdgcn_permlane32_swap(__float_as_uint(m), __float_as_uint(m), false, false);
  m = fmaxf(__uint_as_float(a[0]), __uint_as_float(a[1]));
  auto b = __builtin_amdgcn_permlane16_swap(__float_as_uint(m), __float_as_uint(m), false, false);
  return fmaxf(__uint_as_float(b[0]), __uint_as_float(b[1]));
}
__device__ __forceinline__ float wave_sum(float v) {
  v = sum8(v);
  v = dpp_add<0x140>(v);
  return xsum16_32(v);
}

__global__ __launch_bounds__(256) void prep_kernel(const u16* __restrict__ in, u16* __restrict__ out,
                                                   const char* __restrict__ wqkv, const char* __restrict__ b1,
                                                   const char* __restrict__ b2, unsigned* __restrict__ flags) {
  const int lane = threadIdx.x & 63;
  const int row = blockIdx.x * 4 + (threadIdx.x >> 6);
  const u32x4* src = (const u32x4*)(in + (size_t)row * DM);
  u32x4 a = src[lane], b = src[lane + 64];
  const int gi = blockIdx.x * 256 + threadIdx.x;
  unsigned w0 = 0;
  if (gi < 49152) w0 = *(const unsigned*)(wqkv + (size_t)gi * 128);
  else if (gi < 49152 + 56) w0 = *(const unsigned*)(b1 + (size_t)(gi - 49152) * 128);
  else if (gi < 49152 + 72) w0 = *(const unsigned*)(b2 + (size_t)(gi - 49152 - 56) * 128);
  if (gi < 768) flags[gi * 32] = 0u;
  unsigned w1c = 0;
  if (threadIdx.x < 64 && blockIdx.x < 128) {
    unsigned long long pc, pe;
    asm volatile("s_getpc_b64 %0" : "=s"(pc));
    asm volatile("s_getpc_b64 s[40:41]\n\ts_add_u32 s40, s40, zz_text_end@rel32@lo+4\n\t"
                 "s_addc_u32 s41, s41, zz_text_end@rel32@hi+12\n\ts_mov_b64 %0, s[40:41]"
                 : "=s"(pe) : : "s40", "s41", "scc");
    const unsigned long long a = (pc & ~127ull) + (unsigned long long)((blockIdx.x >> 3) * 64 + lane) * 128;
    if (a + 4 <= pe) w1c = *(const unsigned*)a;
  }
  float v[16];
#pragma unroll
  for (int i = 0; i < 4; ++i) {
    v[2 * i] = bflo(a[i]);
    v[2 * i + 1] = bfhi(a[i]);
    v[8 + 2 * i] = bflo(b[i]);
    v[8 + 2 * i + 1] = bfhi(b[i]);
  }
  float s = 0.f;
#pragma unroll
  for (int i = 0; i < 16; ++i) s += v[i];
  const float mean = wave_sum(s) * (1.0f / DM);
  float q = 0.f;
#pragma unroll
  for (int i = 0; i < 16; ++i) { v[i] -= mean; q += v[i] * v[i]; }
  const float var = wave_sum(q) * (1.0f / DM);
  const float r = 1.0f / sqrtf(var + LN_EPS);
  u32x4 oa, ob;
#pragma unroll
  for (int i = 0; i < 4; ++i) {
    oa[i] = pk2(v[2 * i] * r, v[2 * i + 1] * r);
    ob[i] = pk2(v[8 + 2 * i] * r, v[8 + 2 * i + 1] * r);
  }
  u32x4* dst = (u32x4*)(out + (size_t)row * DM);
  dst[lane] = oa;
  dst[lane + 64] = ob;
  asm volatile("" ::"v"(w0), "v"(w1c));
}

__device__ __forceinline__ float rowmax17(const f32x4* s) {
  float a = fmaxf(fmaxf(s[0][0], s[0][1]), fmaxf(s[0][2], s[0][3]));
#pragma unroll
  for (int t = 1; t < 17; ++t) {
    a = fmaxf(fmaxf(a, s[t][0]), s[t][1]);
    a = fmaxf(fmaxf(a, s[t][2]), s[t][3]);
  }
  return a;
}

template <bool EARLY>
__device__ __forceinline__ void attn_body(const char* ks, const char* vs, bf16x8 qf, int wid, int lane, int tmin,
                                          char* op) {
  const int fr = lane & 15, g = lane >> 4;
  const float NEG = -1e30f;
  f32x4 s[17];
  const char* kp = ks + (wid * 16 + fr) * 64 + ((g ^ ((4 - ((fr >> 2) & 3)) & 3)) << 4);
#pragma unroll
  for (int t = 0; t < 17; ++t) {
    bf16x8 kf = *(const bf16x8*)(kp + t * 1024);
    s[t] = __builtin_amdgcn_mfma_f32_16x16x32_bf16(kf, qf, f32x4{0.f, 0.f, 0.f, 0.f}, 0, 0, 0);
  }
#pragma unroll
  for (int r = 0; r < 4; ++r) {
    s[0][r] = (4 * g + r > fr) ? s[0][r] : NEG;
    s[16][r] = (4 * g + r <= fr) ? s[16][r] : NEG;
  }
  if (EARLY) {
#pragma unroll
    for (int t = 0; t < 16; ++t)
      if (t < tmin) s[t] = f32x4{NEG, NEG, NEG, NEG};
  }
  const float mx = xmax16_32(rowmax17(s));
  const float c = 0.17677669529663687f * 1.4426950408889634f;
  const f32x2 c2 = {c, c};
  const f32x2 nmc2 = {-mx * c, -mx * c};
  f32x2 sum2 = {0.f, 0.f};
  unsigned pk[18][2];
#pragma unroll
  for (int t = 0; t < 17; ++t) {
#pragma unroll
    for (int hh = 0; hh < 2; ++hh) {
      const f32x2 z = f32x2{s[t][2 * hh], s[t][2 * hh + 1]} * c2 + nmc2;
      const f32x2 p = {__builtin_amdgcn_exp2f(z[0]), __builtin_amdgcn_exp2f(z[1])};
      sum2 += p;
      pk[t][hh] = pk2(p[0], p[1]);
    }
  }
  float sum = sum2[0] + sum2[1];
  pk[17][0] = 0u;
  pk[17][1] = 0u;
  sum = xsum16_32(sum);
  const int q4 = (lane >> 2) & 3, pp = lane & 3;
  const char* vp0 = vs + (wid * 16 + 4 * g + q4) * 64 + ((g & 1) << 5) + pp * 8;
  const char* vp1 = vs + (wid * 16 + 4 * g + q4) * 64 + (((g & 1) ^ 1) << 5) + pp * 8;
  f32x4 o0 = {0.f, 0.f, 0.f, 0.f}, o1 = {0.f, 0.f, 0.f, 0.f};
#pragma unroll
  for (int tp = 0; tp < 9; ++tp) {
    const int t0 = 2 * tp, t1 = (2 * tp + 1 < 17) ? 2 * tp + 1 : 16;
    u32x4 pb = {pk[2 * tp][0], pk[2 * tp][1], pk[2 * tp + 1][0], pk[2 * tp + 1][1]};
    bf16x8 pf = __builtin_bit_cast(bf16x8, pb);
    s16x4 a0 = vtr((lds_cptr)(vp0 + t0 * 1024)), a1 = vtr((lds_cptr)(vp0 + t1 * 1024));
    s16x4 b0 = vtr((lds_cptr)(vp1 + t0 * 1024)), b1 = vtr((lds_cptr)(vp1 + t1 * 1024));
    o0 = __builtin_amdgcn_mfma_f32_16x16x32_bf16(__builtin_shufflevector(a0, a1, 0, 1, 2, 3, 4, 5, 6, 7), pf, o0, 0, 0, 0);
    o1 = __builtin_amdgcn_mfma_f32_16x16x32_bf16(__builtin_shufflevector(b0, b1, 0, 1, 2, 3, 4, 5, 6, 7), pf, o1, 0, 0, 0);
  }
  const float inv = 1.0f / sum;
  u32x2 w0, w1;
  w0[0] = pk2(o0[0] * inv, o0[1] * inv);
  w0[1] = pk2(o0[2] * inv, o0[3] * inv);
  w1[0] = pk2(o1[0] * inv, o1[1] * inv);
  w1[1] = pk2(o1[2] * inv, o1[3] * inv);
  *(u32x2*)(op + 8 * g) = w0;
  *(u32x2*)(op + 32 + 8 * g) = w1;
}

enum { EPI_QKV = 0, EPI_RES_STATS = 1, EPI_LN_GELU = 2, EPI_SPLIT_BIAS_RES = 3 };
constexpr int NLW = 4;

template <int CNT, int NBUF, int NPA, int STAGE>
__device__ __forceinline__ void loader_loop(const char* Ab, const char* Wb, const unsigned* voff, int lw, int nk,
                                            unsigned wstep, unsigned lds0) {
  const char* sp[CNT];
  unsigned st[CNT];
#pragma unroll
  for (int i = 0; i < CNT; ++i) {
    const bool isA = (lw + i * NLW) < NPA;
    sp[i] = isA ? Ab : Wb;
    st[i] = isA ? 128u : wstep;
  }
  auto issue = [&](int buf) {
#pragma unroll
    for (int i = 0; i < CNT; ++i) {
      glds16(sp[i], voff[i], lds0 + buf * STAGE + (lw + i * NLW) * 1024);
      sp[i] += st[i];
    }
  };
  issue(0);
  issue(1);
  asm volatile("s_waitcnt vmcnt(%0)" ::"i"(CNT) : "memory");
  __builtin_amdgcn_s_barrier();
#pragma unroll
  for (int s = 2; s < NBUF; ++s) issue(s);
  int buf = 0;
  for (int t = 0; t < nk; ++t) {
    if (t + NBUF <= nk) {
      asm volatile("s_waitcnt vmcnt(%0)" ::"i"((NBUF - 2) * CNT) : "memory");
    } else {
      asm volatile("s_waitcnt vmcnt(0)" ::: "memory");
    }
    __builtin_amdgcn_s_barrier();
    if (t + NBUF < nk) issue(buf);
    buf = (buf + 1 == NBUF) ? 0 : buf + 1;
  }
}

template <int BN, int NBUF, int EPI, int XA, int TL0 = 0, int TL1 = 0>
__device__ __forceinline__ void gemm_body(const char* __restrict__ tp0, const char* __restrict__ tp1, const u16* __restrict__ A, const u16* __restrict__ W,
                                          u16* __restrict__ C, const u16* __restrict__ bias,
                                          const u16* __restrict__ res, float* __restrict__ aux0,
                                          float* __restrict__ aux1, unsigned* __restrict__ flags,
                                          const int N, const int K) {
  constexpr int BM = 128;
  constexpr bool WIDE = BN > 64;
  constexpr bool SPLIT = (EPI == EPI_SPLIT_BIAS_RES);
  constexpr int NTHR = 512;
  constexpr int NG = BN / 64;
  constexpr bool HALF = (BN % 64) != 0;
  constexpr int NT = WIDE ? BN / 32 : 4;
  constexpr int ABYTES = BM * 128, WBYTES = 64 * BN * 2, STAGE = ABYTES + WBYTES;
  constexpr int NPA = ABYTES / 1024, NP = STAGE / 1024;
  constexpr int CMAX = (NP + NLW - 1) / NLW;
  constexpr int TSCR = NBUF * STAGE + 4096;
  constexpr int SIDE = NBUF * STAGE;
  extern __shared__ __attribute__((aligned(16))) char smem[];

  const int tid = threadIdx.x, lane = tid & 63;
  const int wid = __builtin_amdgcn_readfirstlane(tid >> 6);
  STAMP(EPI, 0);
  const int xc = blockIdx.x & 7, rr = blockIdx.x >> 3;
  int bm, bn, hk = 0;
  if (SPLIT) {
    hk = rr & 1;
    const int tt = rr >> 1;
    bm = (xc & 3) * 4 + (tt & 3);
    bn = (xc >> 2) * 4 + (tt >> 2);
  } else {
    constexpr int XB = 32 / XA, PM = 16 / XA;
    bm = (xc % PM) * XA + (rr % XA);
    bn = (xc / PM) * XB + (rr / XA);
  }
  const int m0 = bm * BM, n0 = bn * BN;
  const int nk = SPLIT ? K / 128 : K / 64;
  const int kbeg = hk * (K / 2);
  const int odd = HALF ? (bn & 1) : 0;
  const int cfull = odd ? 32 : 0, chalf = odd ? 0 : NG * 64;
  const unsigned lds0 = (unsigned)(uintptr_t)(__attribute__((address_space(3))) char*)smem;
  const int fr = lane & 15, g = lane >> 4;

  f32x4 acc[4][NT];
  const int wm = wid & 1, wx = (wid >> 1) & 1;
  int tcol[NT];

  if (wid >= 4) {
    const int lw = wid - 4;
    unsigned voff[CMAX];
#pragma unroll
    for (int i = 0; i < CMAX; ++i) {
      const int idx = lw + i * NLW;
      if (idx < NPA) {
        const int row = idx * 8 + (lane >> 3), ch = (lane & 7) ^ (row & 7);
        voff[i] = (unsigned)(((m0 + row) * K + ch * 8) * 2);
      } else if (idx < NPA + NG * 8) {
        const int w = idx - NPA, G = w >> 3, k = (w & 7) * 8 + (lane >> 3);
        const int ch = (lane & 7) ^ ((k & 2) | ((k >> 1) & 4));
        const int col = (EPI == EPI_QKV) ? G * DM + bn * 64 : n0 + cfull + G * 64;
        voff[i] = (unsigned)((k * N + col + ch * 8) * 2);
      } else {
        const int w = idx - NPA - NG * 8, k = w * 16 + (lane >> 2);
        const int ch = (lane & 3) ^ ((k >> 2) & 2);
        voff[i] = (unsigned)((k * N + n0 + chalf + ch * 8) * 2);
      }
    }
    const unsigned wstep = (unsigned)(64 * N * 2);
    const char* Ab = (const char*)(A + kbeg);
    const char* Wb = (const char*)(W + (size_t)kbeg * N);
    if (lw + (CMAX - 1) * NLW < NP)
      loader_loop<CMAX, NBUF, NPA, STAGE>(Ab, Wb, voff, lw, nk, wstep, lds0);
    else
      loader_loop<CMAX - 1, NBUF, NPA, STAGE>(Ab, Wb, voff, lw, nk, wstep, lds0);
  } else {
    if (EPI == EPI_LN_GELU) {
      if (tid < 128) {
        const f32x4* sp = (const f32x4*)(aux0 + (size_t)(m0 + tid) * 32);
        f32x4 p[8];
#pragma unroll
        for (int i = 0; i < 8; ++i) p[i] = sp[i];
        float mean = 0.f;
#pragma unroll
        for (int i = 0; i < 8; ++i) mean += p[i][0] + p[i][2];
        mean *= (1.0f / 16.0f);
        float m2 = 0.f;
#pragma unroll
        for (int i = 0; i < 8; ++i) {
          const float d0 = p[i][0] - mean, d1 = p[i][2] - mean;
          m2 += p[i][1] + p[i][3] + 64.0f * (d0 * d0 + d1 * d1);
        }
        const float var = m2 * (1.0f / DM);
        const float r1 = 1.0f / sqrtf(var + LN_EPS);
        const float r2 = 1.0f / sqrtf(var * r1 * r1 + LN_EPS);
        *(f32x2*)(smem + SIDE + tid * 8) = f32x2{mean, r1 * r2};
      } else if (tid - 128 < BN / 2) {
        const int c = (tid - 128) * 2;
        f32x2 cs = {0.f, 0.f};
#pragma unroll
        for (int kc = 0; kc < 16; ++kc) cs += *(const f32x2*)(aux1 + (size_t)kc * FF + n0 + c);
        const unsigned bb = *(const unsigned*)(bias + n0 + c);
        *(f32x4*)(smem + SIDE + 1024 + c * 8) = f32x4{cs[0], bflo(bb), cs[1], bfhi(bb)};
      }
    }
    const int q = (lane >> 2) & 3, pp = lane & 3;
    const int wk = WIDE ? 0 : wx;
    int aoff[2];
#pragma unroll
    for (int ks = 0; ks < 2; ++ks) aoff[ks] = (wm * 64 + fr) * 128 + (((ks * 4 + g) ^ (fr & 7)) << 4);
    int tb[NT];
    const int f1 = (q & 2) | ((g & 1) << 2);
#pragma unroll
    for (int j = 0; j < NT; ++j) {
      int G, tp;
      if (!WIDE) { G = 0; tp = j; }
      else if (EPI == EPI_QKV) { G = (j >> 1) ^ 2; G = G == 3 ? 1 : G; tp = wx * 2 + (j & 1); }
      else if (j < 4) { G = wx * (NG - 1); tp = j; }
      else { G = 1; tp = wx * 2 + (j - 4); }
      if (WIDE && HALF && j == NT - 1) {
        tb[j] = ABYTES + NG * 8192 + (8 * g + q) * 64 + (((2 * wx + (pp >> 1)) ^ ((g & 1) << 1)) << 4) + (pp & 1) * 8;
        tcol[j] = chalf + wx * 16;
      } else {
        tb[j] = ABYTES + G * 8192 + (8 * g + q) * 128 + (((2 * tp + (pp >> 1)) ^ f1) << 4) + (pp & 1) * 8;
        tcol[j] = cfull + G * 64 + tp * 16;
      }
    }
#pragma unroll
    for (int i = 0; i < 4; ++i)
#pragma unroll
      for (int j = 0; j < NT; ++j) acc[i][j] = f32x4{0.f, 0.f, 0.f, 0.f};

    bf16x8 xa[4], wa[NT], xb[4], wb[NT];
    auto rd = [&](bf16x8* xf, bf16x8* wf, int buf, int ks) {
      const char* base = smem + buf * STAGE;
#pragma unroll
      for (int j = 0; j < NT; ++j) {
        const int rs = (WIDE && HALF && j == NT - 1) ? 64 : 128;
        s16x4 lo = vtr((lds_cptr)(base + tb[j] + ks * 32 * rs));
        s16x4 hi = vtr((lds_cptr)(base + tb[j] + ks * 32 * rs + 4 * rs));
        wf[j] = __builtin_shufflevector(lo, hi, 0, 1, 2, 3, 4, 5, 6, 7);
      }
#pragma unroll
      for (int i = 0; i < 4; ++i) xf[i] = *(const bf16x8*)(base + aoff[ks] + i * 2048);
    };
    auto mm = [&](const bf16x8* xf, const bf16x8* wf) {
#pragma unroll
      for (int i = 0; i < 4; ++i)
#pragma unroll
        for (int j = 0; j < NT; ++j)
          acc[i][j] = __builtin_amdgcn_mfma_f32_16x16x32_bf16(wf[j], xf[i], acc[i][j], 0, 0, 0);
    };
#define INTERLEAVE() do { _Pragma("unroll") for (int z_ = 0; z_ < 2 * NT + 4; ++z_) { __builtin_amdgcn_sched_group_barrier(0x8, 1, 0); __builtin_amdgcn_sched_group_barrier(0x100, 1, 0); } __builtin_amdgcn_sched_group_barrier(0x8, 4 * NT - (2 * NT + 4), 0); } while (0)
    __builtin_amdgcn_s_waitcnt(0xC07F);
    __builtin_amdgcn_s_barrier();
    STAMP(EPI, 1);
    auto do_touch = [&](int i) {
      const int line = ((blockIdx.x * 4 + wid) * 2 + i) * 64 + lane;
      if (__builtin_amdgcn_readfirstlane(line) < TL0 + TL1) {
        const int lc = line < TL0 + TL1 ? line : TL0 + TL1 - 1;
        const char* p = lc < TL0 ? tp0 + (size_t)lc * 128 : tp1 + (size_t)(lc - TL0) * 128;
        unsigned sv;
        asm volatile("s_mov_b32 %0, m0\n\ts_mov_b32 m0, %2\n\ts_nop 0\n\tglobal_load_lds_dword %1, off\n\ts_mov_b32 m0, %0"
                     : "=&s"(sv) : "v"(p), "s"(lds0 + TSCR + wid * 256) : "memory");
      }
    };
    if (WIDE) {
      rd(xa, wa, 0, 0);
      int b0 = 0;
      for (int t = 0; t < nk; ++t) {
        if (TL0 + TL1 > 0) { if (t == 2) do_touch(0); if (t == 7) do_touch(1); }
        rd(xb, wb, b0, 1);
        b0 = (b0 + 1 == NBUF) ? 0 : b0 + 1;
        mm(xa, wa);
        INTERLEAVE();
        __builtin_amdgcn_sched_barrier(0);
        __builtin_amdgcn_s_waitcnt(0xC07F);
        __builtin_amdgcn_s_barrier();
        rd(xa, wa, b0, 0);
        mm(xb, wb);
        INTERLEAVE();
        __builtin_amdgcn_sched_barrier(0);
      }
    } else {
      rd(xa, wa, 0, wk);
      int b1 = 1;
      for (int t = 0; t < nk; t += 2) {
        __builtin_amdgcn_s_waitcnt(0xC07F);
        __builtin_amdgcn_s_barrier();
        rd(xb, wb, b1, wk);
        b1 = (b1 + 1 == NBUF) ? 0 : b1 + 1;
        mm(xa, wa);
        INTERLEAVE();
        __builtin_amdgcn_sched_barrier(0);
        __builtin_amdgcn_s_waitcnt(0xC07F);
        __builtin_amdgcn_s_barrier();
        rd(xa, wa, b1, wk);
        b1 = (b1 + 1 == NBUF) ? 0 : b1 + 1;
        mm(xb, wb);
        INTERLEAVE();
        __builtin_amdgcn_sched_barrier(0);
      }
    }
    __builtin_amdgcn_s_waitcnt(0xC07F);
    if (TL0 + TL1 > 0) asm volatile("s_waitcnt vmcnt(0)" ::: "memory");
    STAMP(EPI, 2);
    if (SPLIT) {
      const int tile = bm * 8 + bn;
      const auto rsrc = __builtin_amdgcn_make_buffer_rsrc(aux0, 0, 128 * 2 * 2 * 16384, 0x00020000);
      if (wm != hk) {
        const int slot = ((tile * 2 + hk) * 2 + wx);
#pragma unroll
        for (int i = 0; i < 4; ++i)
#pragma unroll
          for (int j = 0; j < NT; ++j)
            __builtin_amdgcn_raw_buffer_store_b128(__builtin_bit_cast(u32x4, acc[i][j]), rsrc,
                                                   slot * 16384 + (i * NT + j) * 1024 + lane * 16, 0, 16);
        asm volatile("s_waitcnt vmcnt(0)" ::: "memory");
        if (lane == 0) __hip_atomic_store(flags + slot * 32, 1u, __ATOMIC_RELAXED, __HIP_MEMORY_SCOPE_AGENT);
      } else {
        const int slot = ((tile * 2 + (hk ^ 1)) * 2 + wx);
        for (int spin = 0; spin < (1 << 24); ++spin) {
          if (__hip_atomic_load(flags + slot * 32, __ATOMIC_RELAXED, __HIP_MEMORY_SCOPE_AGENT) != 0u) break;
          __builtin_amdgcn_s_sleep(2);
        }
        asm volatile("" ::: "memory");
#pragma unroll
        for (int i = 0; i < 4; ++i) {
          u32x4 t[NT];
#pragma unroll
          for (int j = 0; j < NT; ++j)
            t[j] = __builtin_amdgcn_raw_buffer_load_b128(rsrc, slot * 16384 + (i * NT + j) * 1024 + lane * 16, 0, 16);
#pragma unroll
          for (int j = 0; j < NT; ++j) acc[i][j] += __builtin_bit_cast(f32x4, t[j]);
        }
      }
    }
  }
  __syncthreads();
  STAMP(EPI, 3);

  constexpr int CPR = BN / 8;
  if (EPI == EPI_QKV) {
    constexpr int KIMG = 0, VIMG = 49152, QIMG = 98304, RED = 114688, HIMG = 24576;
    u16* kvg = (u16*)aux0;
    u32x4 cw[8];
    const int t2 = tid - 256;
    const int cb = blockIdx.x < 224 ? blockIdx.x : 223;
    const int cg = cb % 14, kc = cb / 14;
    if (wid < 4) {
      auto put = [&](int j) {
        const int grp = tcol[j] >> 6, hd = (tcol[j] >> 5) & 1;
        const int dd = (tcol[j] & 31) + g * 4, chn = dd >> 3, hf = (dd >> 2) & 1;
        const int sw = (grp == 2) ? (((fr >> 2) & 1) << 1) : ((4 - ((fr >> 2) & 3)) & 3);
        const int base = (grp == 0) ? QIMG + hd * 8192 : ((grp == 1) ? KIMG : VIMG) + hd * HIMG + 256 * 64;
#pragma unroll
        for (int i = 0; i < 4; ++i) {
          u32x2 o;
          o[0] = pk2(acc[i][j][0], acc[i][j][1]);
          o[1] = pk2(acc[i][j][2], acc[i][j][3]);
          *(u32x2*)(smem + base + (wm * 64 + i * 16 + fr) * 64 + ((chn ^ sw) << 4) + hf * 8) = o;
        }
      };
#pragma unroll
      for (int j = 0; j < 4; ++j) put(j);
      if (bm < 15) {
#pragma unroll
        for (int kv = 0; kv < 2; ++kv)
#pragma unroll
          for (int pc = 0; pc < 4; ++pc) {
            const int row = wm * 64 + pc * 16 + (lane >> 2), slot = lane & 3;
            const int sw = kv ? (((row >> 2) & 1) << 1) : ((4 - ((row >> 2) & 3)) & 3);
            const u32x4 v = *(const u32x4*)(smem + (kv ? VIMG : KIMG) + wx * HIMG + (256 + row) * 64 + slot * 16);
            ST16(kvg + ((size_t)(kv * NH + bn * 2 + wx) * S_LEN + m0 + row) * 32 + (slot ^ sw) * 8, v);
          }
      }
      put(4);
      put(5);
      if (bm < 15) {
        asm volatile("s_waitcnt vmcnt(0)" ::: "memory");
        if (lane == 0)
          __hip_atomic_fetch_add(flags + (bm * 16 + bn) * 32, 1u, __ATOMIC_RELAXED, __HIP_MEMORY_SCOPE_AGENT);
      }
    } else {
      const u16* cp = res + (size_t)(kc * 64 + (t2 >> 5) * 8) * FF + cg * 256 + (t2 & 31) * 8;
#pragma unroll
      for (int i = 0; i < 8; ++i) cw[i] = *(const u32x4*)(cp + (size_t)i * FF);
      const int lw = wid - 4, pr = lw >> 1, kv = lw & 1;
      const int pb = bm - 2 + pr;
      const unsigned dst = lds0 + (kv ? VIMG : KIMG) + pr * 128 * 64;
      if (pb >= 0) {
        const unsigned* fp = flags + (pb * 16 + bn) * 32;
        for (int spin = 0; spin < (1 << 24); ++spin) {
          if (__hip_atomic_load(fp, __ATOMIC_RELAXED, __HIP_MEMORY_SCOPE_AGENT) >= 4u) break;
          __builtin_amdgcn_s_sleep(2);
        }
        asm volatile("" ::: "memory");
        const int kl = lane >> 2, slot = lane & 3;
        const int sw = kv ? (((kl >> 2) & 1) << 1) : ((4 - ((kl >> 2) & 3)) & 3);
        const unsigned vo = (unsigned)((kl * 32 + (slot ^ sw) * 8) * 2);
#pragma unroll
        for (int hd = 0; hd < 2; ++hd) {
          const u16* src = kvg + ((size_t)(kv * NH + bn * 2 + hd) * S_LEN + pb * 128) * 32;
#pragma unroll
          for (int pc = 0; pc < 8; ++pc) glds16_sc1(src + pc * 512, vo, dst + hd * HIMG + pc * 1024);
        }
      } else {
#pragma unroll
        for (int hd = 0; hd < 2; ++hd)
#pragma unroll
          for (int pc = 0; pc < 8; ++pc)
            *(u32x4*)(smem + (kv ? VIMG : KIMG) + pr * 128 * 64 + hd * HIMG + pc * 1024 + lane * 16) = u32x4{0u, 0u, 0u, 0u};
      }
      {
        float* red = (float*)(smem + RED);
        float sacc[8];
#pragma unroll
        for (int e = 0; e < 8; ++e) sacc[e] = 0.f;
#pragma unroll
        for (int i = 0; i < 8; ++i)
#pragma unroll
          for (int e = 0; e < 4; ++e) { sacc[2 * e] += bflo(cw[i][e]); sacc[2 * e + 1] += bfhi(cw[i][e]); }
        *(f32x4*)(red + (t2 >> 5) * 256 + (t2 & 31) * 8) = f32x4{sacc[0], sacc[1], sacc[2], sacc[3]};
        *(f32x4*)(red + (t2 >> 5) * 256 + (t2 & 31) * 8 + 4) = f32x4{sacc[4], sacc[5], sacc[6], sacc[7]};
      }
      asm volatile("s_waitcnt vmcnt(0)" ::: "memory");
    }
    STAMP(EPI, 5);
    __syncthreads();
    STAMP(EPI, 6);
    if (blockIdx.x < 224 && tid < 256) {
      const float* red = (const float*)(smem + RED);
      float t = 0.f;
#pragma unroll
      for (int i = 0; i < 8; ++i) t += red[i * 256 + tid];
      aux1[(size_t)kc * FF + cg * 256 + tid] = t;
    }
    const int qw = m0 + wid * 16;
    const int tmin = (qw < WIN) ? (WIN - qw) >> 4 : 0;
    const int qo = (wid * 16 + fr) * 64 + ((g ^ ((4 - ((fr >> 2) & 3)) & 3)) << 4);
    const bf16x8 qf0 = *(const bf16x8*)(smem + QIMG + qo), qf1 = *(const bf16x8*)(smem + QIMG + 8192 + qo);
    char* ost = smem + QIMG + wid * 1024;
#pragma unroll 1
    for (int hd = 0; hd < 2; ++hd) {
      const bf16x8 qf = hd ? qf1 : qf0;
      char* op = ost + hd * 8192 + fr * 64;
      if (bm < 2) attn_body<true>(smem + KIMG + hd * HIMG, smem + VIMG + hd * HIMG, qf, wid, lane, tmin, op);
      else attn_body<false>(smem + KIMG + hd * HIMG, smem + VIMG + hd * HIMG, qf, wid, lane, 0, op);
    }
#pragma unroll
    for (int i = 0; i < 2; ++i) {
      const int row = i * 8 + (lane >> 3), ch = lane & 7;
      const u32x4 v = *(const u32x4*)(ost + (ch >> 2) * 8192 + row * 64 + (ch & 3) * 16);
      ST16(C + (size_t)(qw + row) * DM + bn * 64 + ch * 8, v);
    }
    STAMP(EPI, 7);
  } else if (EPI == EPI_LN_GELU) {
    constexpr int RS = BN * 4 + 16;
    if (wid < 4) {
#pragma unroll
      for (int j = 0; j < NT; ++j)
#pragma unroll
        for (int i = 0; i < 4; ++i)
          *(f32x4*)(smem + (wm * 64 + i * 16 + fr) * RS + (tcol[j] + g * 4) * 4) = acc[i][j];
    }
    __syncthreads();
#pragma unroll
    for (int c = tid; c < BM * CPR; c += NTHR) {
      const int row = c / CPR, ch = c % CPR;
      const f32x4 a0 = *(const f32x4*)(smem + row * RS + ch * 32);
      const f32x4 a1 = *(const f32x4*)(smem + row * RS + ch * 32 + 16);
      const f32x2 rs = *(const f32x2*)(smem + SIDE + row * 8);
      float v[8];
#pragma unroll
      for (int r = 0; r < 4; ++r) {
        const f32x4 cb = *(const f32x4*)(smem + SIDE + 1024 + ch * 64 + r * 16);
        const float x0 = (r < 2) ? a0[2 * r] : a1[2 * r - 4];
        const float x1 = (r < 2) ? a0[2 * r + 1] : a1[2 * r - 3];
        v[2 * r] = (x0 - rs[0] * cb[0]) * rs[1] + cb[1];
        v[2 * r + 1] = (x1 - rs[0] * cb[2]) * rs[1] + cb[3];
      }
      u32x4 o;
#pragma unroll
      for (int r = 0; r < 4; ++r) {
        const f32x2 x = {v[2 * r], v[2 * r + 1]};
        const f32x2 x2 = x * x;
        const f32x2 w = x2 * f32x2{-0.10294324f, -0.10294324f} + f32x2{-2.302208198f, -2.302208198f};
        const f32x2 z = x * w;
        f32x2 e = {__builtin_amdgcn_exp2f(z[0]), __builtin_amdgcn_exp2f(z[1])};
        e = e + f32x2{1.0f, 1.0f};
        const f32x2 y = x * f32x2{__builtin_amdgcn_rcpf(e[0]), __builtin_amdgcn_rcpf(e[1])};
        o[r] = pk2(y[0], y[1]);
      }
      ST16(C + (size_t)(m0 + row) * N + n0 + ch * 8, o);
    }
  } else if (EPI == EPI_RES_STATS) {
    constexpr int RS = BN * 4 + 16;
    constexpr int PART = BM * RS;
    if (wid < 4) {
#pragma unroll
      for (int i = 0; i < 4; ++i)
#pragma unroll
        for (int j = 0; j < NT; ++j)
          *(f32x4*)(smem + wx * PART + (wm * 64 + i * 16 + fr) * RS + (j * 16 + g * 4) * 4) = acc[i][j];
    }
    __syncthreads();
#pragma unroll
    for (int c = tid; c < BM * CPR; c += NTHR) {
      const int row = c / CPR, ch = c % CPR;
      const f32x4 a0 = *(const f32x4*)(smem + row * RS + ch * 32);
      const f32x4 a1 = *(const f32x4*)(smem + row * RS + ch * 32 + 16);
      const f32x4 b0 = *(const f32x4*)(smem + PART + row * RS + ch * 32);
      const f32x4 b1 = *(const f32x4*)(smem + PART + row * RS + ch * 32 + 16);
      const u32x4 rv = *(const u32x4*)(res + (size_t)(m0 + row) * N + n0 + ch * 8);
      u32x4 o;
      o[0] = pk2(a0[0] + b0[0] + bflo(rv[0]), a0[1] + b0[1] + bfhi(rv[0]));
      o[1] = pk2(a0[2] + b0[2] + bflo(rv[1]), a0[3] + b0[3] + bfhi(rv[1]));
      o[2] = pk2(a1[0] + b1[0] + bflo(rv[2]), a1[1] + b1[1] + bfhi(rv[2]));
      o[3] = pk2(a1[2] + b1[2] + bflo(rv[3]), a1[3] + b1[3] + bfhi(rv[3]));
      ST16(C + (size_t)(m0 + row) * N + n0 + ch * 8, o);
      float s = 0.f;
#pragma unroll
      for (int r = 0; r < 4; ++r) s += bflo(o[r]) + bfhi(o[r]);
      const float mean = sum8(s) * (1.0f / 64.0f);
      float m2 = 0.f;
#pragma unroll
      for (int r = 0; r < 4; ++r) {
        const float d0 = bflo(o[r]) - mean, d1 = bfhi(o[r]) - mean;
        m2 += d0 * d0 + d1 * d1;
      }
      m2 = sum8(m2);
      if (ch == 0) *(f32x2*)(aux0 + ((size_t)(m0 + row) * 16 + bn) * 2) = f32x2{mean, m2};
    }
  } else {
    constexpr int RS = BN * 4 + 16;
    if (wid < 4 && wm == hk) {
#pragma unroll
      for (int i = 0; i < 4; ++i)
#pragma unroll
        for (int j = 0; j < NT; ++j)
          *(f32x4*)(smem + (i * 16 + fr) * RS + (tcol[j] + g * 4) * 4) = acc[i][j];
    }
    __syncthreads();
#pragma unroll
    for (int c = tid; c < 64 * CPR; c += NTHR) {
      const int row = c / CPR, ch = c % CPR;
      const f32x4 a0 = *(const f32x4*)(smem + row * RS + ch * 32);
      const f32x4 a1 = *(const f32x4*)(smem + row * RS + ch * 32 + 16);
      const u32x4 bb = *(const u32x4*)(bias + n0 + ch * 8);
      const size_t go = (size_t)(m0 + hk * 64 + row) * N + n0 + ch * 8;
      const u32x4 rv = *(const u32x4*)(res + go);
      u32x4 o;
      o[0] = pk2(a0[0] + bflo(bb[0]) + bflo(rv[0]), a0[1] + bfhi(bb[0]) + bfhi(rv[0]));
      o[1] = pk2(a0[2] + bflo(bb[1]) + bflo(rv[1]), a0[3] + bfhi(bb[1]) + bfhi(rv[1]));
      o[2] = pk2(a1[0] + bflo(bb[2]) + bflo(rv[2]), a1[1] + bfhi(bb[2]) + bfhi(rv[2]));
      o[3] = pk2(a1[2] + bflo(bb[3]) + bflo(rv[3]), a1[3] + bfhi(bb[3]) + bfhi(rv[3]));
      ST16(C + go, o);
    }
  }
}

__global__ __launch_bounds__(512, 1) void qkv_attn_kernel(const u16* __restrict__ A, const u16* __restrict__ W,
                                                         u16* __restrict__ o, const char* __restrict__ wo,
                                                         const u16* __restrict__ w1, float* __restrict__ kvg,
                                                         float* __restrict__ cspart, unsigned* __restrict__ flags) {
  gemm_body<192, 3, EPI_QKV, 8, 16384, 57344>(wo, (const char*)w1, A, W, o, nullptr, w1, kvg, cspart, flags, 3 * DM, DM);
}
__global__ __launch_bounds__(512, 1) void gemm_wo_kernel(const u16* __restrict__ A, const u16* __restrict__ W,
                                                        u16* __restrict__ C, const u16* __restrict__ res,
                                                        float* __restrict__ stats) {
  gemm_body<64, 6, EPI_RES_STATS, 4>(nullptr, nullptr, A, W, C, nullptr, res, stats, nullptr, nullptr, DM, DM);
}
__global__ __launch_bounds__(512, 1) void gemm_w1_kernel(const u16* __restrict__ A, const u16* __restrict__ W,
                                                        u16* __restrict__ C, const u16* __restrict__ bias,
                                                        float* __restrict__ stats, float* __restrict__ cspart,
                                                        const char* __restrict__ t0) {
  gemm_body<224, 3, EPI_LN_GELU, 8, 57344, 0>(t0, t0, A, W, C, bias, nullptr, stats, cspart, nullptr, FF, DM);
}
__global__ __launch_bounds__(512, 1) void gemm_w2_kernel(const u16* __restrict__ A, const u16* __restrict__ W,
                                                        u16* __restrict__ C, const u16* __restrict__ bias,
                                                        const u16* __restrict__ res, float* __restrict__ xchg,
                                                        unsigned* __restrict__ flags) {
  gemm_body<128, 4, EPI_SPLIT_BIAS_RES, 4>(nullptr, nullptr, A, W, C, bias, res, xchg, nullptr, flags, DM, FF);
}
extern "C" __global__ void zz_text_end() {}

constexpr int lds_bytes(int BN, int NBUF, bool side) { return NBUF * (128 * 64 * 2 + 64 * BN * 2) + (side ? 5120 : 0); }

extern "C" void kernel_launch(void* const* d_in, const int* in_sizes, int n_in, void* d_out, int out_size,
                              void* d_ws, size_t ws_size, hipStream_t stream) {
  const u16* x = (const u16*)d_in[0];
  const u16* wqkv = (const u16*)d_in[1];
  const u16* wo = (const u16*)d_in[2];
  const u16* w1 = (const u16*)d_in[4];
  const u16* b1 = (const u16*)d_in[5];
  const u16* w2 = (const u16*)d_in[6];
  const u16* b2 = (const u16*)d_in[7];
  u16* out = (u16*)d_out;
  char* ws = (char*)d_ws;
  u16* xn = (u16*)(ws);
  float* xchg = (float*)(ws);
  u16* h = (u16*)(ws + (8u << 20));
  u16* qkv = (u16*)(ws + (12u << 20));
  u16* o = (u16*)(ws + (20u << 20));
  u16* hh = (u16*)(ws + (12u << 20));
  float* stats = (float*)(ws + (26u << 20));
  float* cspart = (float*)(ws + (26u << 20) + (256u << 10));
  unsigned* flags = (unsigned*)(ws + (26u << 20) + (512u << 10));

  (void)hipFuncSetAttribute((const void*)qkv_attn_kernel, hipFuncAttributeMaxDynamicSharedMemorySize, lds_bytes(192, 3, true));
  (void)hipFuncSetAttribute((const void*)gemm_wo_kernel, hipFuncAttributeMaxDynamicSharedMemorySize, lds_bytes(64, 6, false));
  (void)hipFuncSetAttribute((const void*)gemm_w1_kernel, hipFuncAttributeMaxDynamicSharedMemorySize, lds_bytes(224, 3, true));
  (void)hipFuncSetAttribute((const void*)gemm_w2_kernel, hipFuncAttributeMaxDynamicSharedMemorySize, lds_bytes(128, 4, false));
  prep_kernel<<<S_LEN / 4, 256, 0, stream>>>(x, xn, (const char*)wqkv, (const char*)b1, (const char*)b2, flags);
  qkv_attn_kernel<<<256, 512, lds_bytes(192, 3, true), stream>>>(xn, wqkv, o, (const char*)wo, w1, (float*)qkv, cspart, flags + 512 * 32);
  gemm_wo_kernel<<<256, 512, lds_bytes(64, 6, false), stream>>>(o, wo, h, x, stats);
  gemm_w1_kernel<<<256, 512, lds_bytes(224, 3, true), stream>>>(h, w1, hh, b1, stats, cspart, (const char*)w2);
  gemm_w2_kernel<<<256, 512, lds_bytes(128, 4, false), stream>>>(hh, w2, out, b2, h, xchg, flags);
}
```

```cpp
#include <hip/hip_runtime.h>
#include <hip/hip_bf16.h>
#include <stdint.h>

typedef unsigned short u16;
typedef __attribute__((ext_vector_type(8))) short bf16x8;
typedef __attribute__((ext_vector_type(4))) short s16x4;
typedef __attribute__((ext_vector_type(4))) float f32x4;
typedef __attribute__((ext_vector_type(2))) float f32x2;
typedef __attribute__((ext_vector_type(4))) unsigned u32x4;
typedef __attribute__((ext_vector_type(2))) unsigned u32x2;
typedef __attribute__((ext_vector_type(2))) __bf16 bf16x2_t;
typedef __attribute__((address_space(3))) const char* lds_cptr;

#define STAMP(kid, i)
__device__ __forceinline__ void st16_sc1(void* p, __attribute__((ext_vector_type(4))) unsigned v) { asm volatile("global_store_dwordx4 %0, %1, off sc1\n\ts_nop 1" :: "v"(p), "v"(v) : "memory"); }
#define ST16(p, v) st16_sc1((void*)(p), (v))
constexpr int S_LEN = 2048, DM = 1024, NH = 32, DH = 32, WIN = 256, FF = 3584;
constexpr float LN_EPS = 1e-5f;

__device__ __forceinline__ float bf2f(u16 v) { return __uint_as_float(((unsigned)v) << 16); }
__device__ __forceinline__ float bflo(unsigned v) { return __uint_as_float(v << 16); }
__device__ __forceinline__ float bfhi(unsigned v) { return __uint_as_float(v & 0xffff0000u); }
__device__ __forceinline__ unsigned pk2(float lo, float hi) {
  return __builtin_bit_cast(unsigned, __builtin_convertvector(f32x2{lo, hi}, bf16x2_t));
}
__device__ __forceinline__ void glds16(const void* sbase, unsigned voff, unsigned lds_addr) {
  unsigned sv;
  asm volatile("s_mov_b32 %0, m0\n\ts_mov_b32 m0, %3\n\ts_nop 0\n\tglobal_load_lds_dwordx4 %1, %2\n\ts_mov_b32 m0, %0"
               : "=&s"(sv) : "v"(voff), "s"(sbase), "s"(lds_addr) : "memory");
}
__device__ __forceinline__ void glds16_sc1(const void* sbase, unsigned voff, unsigned lds_addr) {
  unsigned sv;
  asm volatile("s_mov_b32 %0, m0\n\ts_mov_b32 m0, %3\n\ts_nop 0\n\tglobal_load_lds_dwordx4 %1, %2 sc1\n\ts_mov_b32 m0, %0"
               : "=&s"(sv) : "v"(voff), "s"(sbase), "s"(lds_addr) : "memory");
}
__device__ __forceinline__ s16x4 vtr(lds_cptr p) {
  return __builtin_bit_cast(s16x4, __builtin_amdgcn_ds_read_tr16_b64_v4i16((__attribute__((address_space(3))) s16x4*)p));
}
template <int CTRL>
__device__ __forceinline__ float dpp_add(float v) {
  return v + __int_as_float(__builtin_amdgcn_update_dpp(0, __float_as_int(v), CTRL, 0xf, 0xf, false));
}
__device__ __forceinline__ float sum8(float v) {
  v = dpp_add<0xB1>(v);
  v = dpp_add<0x4E>(v);
  return dpp_add<0x141>(v);
}
__device__ __forceinline__ float xsum16_32(float m) {
  auto a = __builtin_amdgcn_permlane32_swap(__float_as_uint(m), __float_as_uint(m), false, false);
  m = __uint_as_float(a[0]) + __uint_as_float(a[1]);
  auto b = __builtin_amdgcn_permlane16_swap(__float_as_uint(m), __float_as_uint(m), false, false);
  return __uint_as_float(b[0]) + __uint_as_float(b[1]);
}
__device__ __forceinline__ float xmax16_32(float m) {
  auto a = __builtin_amdgcn_permlane32_swap(__float_as_uint(m), __float_as_uint(m), false, false);
  m = fmaxf(__uint_as_float(a[0]), __uint_as_float(a[1]));
  auto b = __builtin_amdgcn_permlane16_swap(__float_as_uint(m), __float_as_uint(m), false, false);
  return fmaxf(__uint_as_float(b[0]), __uint_as_float(b[1]));
}
__device__ __forceinline__ float wave_sum(float v) {
  v = sum8(v);
  v = dpp_add<0x140>(v);
  return xsum16_32(v);
}

__global__ __launch_bounds__(256) void prep_kernel(const u16* __restrict__ in, u16* __restrict__ out,
                                                   const char* __restrict__ wqkv, const char* __restrict__ b1,
                                                   const char* __restrict__ b2, unsigned* __restrict__ flags) {
  const int lane = threadIdx.x & 63;
  const int row = blockIdx.x * 4 + (threadIdx.x >> 6);
  const u32x4* src = (const u32x4*)(in + (size_t)row * DM);
  u32x4 a = src[lane], b = src[lane + 64];
  const int gi = blockIdx.x * 256 + threadIdx.x;
  unsigned w0 = 0;
  if (gi < 49152) w0 = *(const unsigned*)(wqkv + (size_t)gi * 128);
  else if (gi < 49152 + 56) w0 = *(const unsigned*)(b1 + (size_t)(gi - 49152) * 128);
  else if (gi < 49152 + 72) w0 = *(const unsigned*)(b2 + (size_t)(gi - 49152 - 56) * 128);
  if (gi < 768) flags[gi * 32] = 0u;
  unsigned w1c = 0;
  if (threadIdx.x < 64 && blockIdx.x < 128) {
    unsigned long long pc, pe;
    asm volatile("s_getpc_b64 %0" : "=s"(pc));
    asm volatile("s_getpc_b64 s[40:41]\n\ts_add_u32 s40, s40, zz_text_end@rel32@lo+4\n\t"
                 "s_addc_u32 s41, s41, zz_text_end@rel32@hi+12\n\ts_mov_b64 %0, s[40:41]"
                 : "=s"(pe) : : "s40", "s41", "scc");
    const unsigned long long a = (pc & ~127ull) + (unsigned long long)((blockIdx.x >> 3) * 64 + lane) * 128;
    if (a + 4 <= pe) w1c = *(const unsigned*)a;
  }
  float v[16];
#pragma unroll
  for (int i = 0; i < 4; ++i) {
    v[2 * i] = bflo(a[i]);
    v[2 * i + 1] = bfhi(a[i]);
    v[8 + 2 * i] = bflo(b[i]);
    v[8 + 2 * i + 1] = bfhi(b[i]);
  }
  float s = 0.f;
#pragma unroll
  for (int i = 0; i < 16; ++i) s += v[i];
  const float mean = wave_sum(s) * (1.0f / DM);
  float q = 0.f;
#pragma unroll
  for (int i = 0; i < 16; ++i) { v[i] -= mean; q += v[i] * v[i]; }
  const float var = wave_sum(q) * (1.0f / DM);
  const float r = 1.0f / sqrtf(var + LN_EPS);
  u32x4 oa, ob;
#pragma unroll
  for (int i = 0; i < 4; ++i) {
    oa[i] = pk2(v[2 * i] * r, v[2 * i + 1] * r);
    ob[i] = pk2(v[8 + 2 * i] * r, v[8 + 2 * i + 1] * r);
  }
  u32x4* dst = (u32x4*)(out + (size_t)row * DM);
  dst[lane] = oa;
  dst[lane + 64] = ob;
  asm volatile("" ::"v"(w0), "v"(w1c));
}

__device__ __forceinline__ float rowmax17(const f32x4* s) {
  float a = fmaxf(fmaxf(s[0][0], s[0][1]), fmaxf(s[0][2], s[0][3]));
#pragma unroll
  for (int t = 1; t < 17; ++t) {
    a = fmaxf(fmaxf(a, s[t][0]), s[t][1]);
    a = fmaxf(fmaxf(a, s[t][2]), s[t][3]);
  }
  return a;
}

template <bool EARLY>
__device__ __forceinline__ void attn_body(const char* ks, const char* vs, bf16x8 qf, int wid, int lane, int tmin,
                                          char* op) {
  const int fr = lane & 15, g = lane >> 4;
  const float NEG = -1e30f;
  f32x4 s[17];
  const char* kp = ks + (wid * 16 + fr) * 64 + ((g ^ ((4 - ((fr >> 2) & 3)) & 3)) << 4);
#pragma unroll
  for (int t = 0; t < 17; ++t) {
    bf16x8 kf = *(const bf16x8*)(kp + t * 1024);
    s[t] = __builtin_amdgcn_mfma_f32_16x16x32_bf16(kf, qf, f32x4{0.f, 0.f, 0.f, 0.f}, 0, 0, 0);
  }
#pragma unroll
  for (int r = 0; r < 4; ++r) {
    s[0][r] = (4 * g + r > fr) ? s[0][r] : NEG;
    s[16][r] = (4 * g + r <= fr) ? s[16][r] : NEG;
  }
  if (EARLY) {
#pragma unroll
    for (int t = 0; t < 16; ++t)
      if (t < tmin) s[t] = f32x4{NEG, NEG, NEG, NEG};
  }
  const float mx = xmax16_32(rowmax17(s));
  const float c = 0.17677669529663687f * 1.4426950408889634f;
  const f32x2 c2 = {c, c};
  const f32x2 nmc2 = {-mx * c, -mx * c};
  f32x2 sum2 = {0.f, 0.f};
  unsigned pk[18][2];
#pragma unroll
  for (int t = 0; t < 17; ++t) {
#pragma unroll
    for (int hh = 0; hh < 2; ++hh) {
      const f32x2 z = f32x2{s[t][2 * hh], s[t][2 * hh + 1]} * c2 + nmc2;
      const f32x2 p = {__builtin_amdgcn_exp2f(z[0]), __builtin_amdgcn_exp2f(z[1])};
      sum2 += p;
      pk[t][hh] = pk2(p[0], p[1]);
    }
  }
  float sum = sum2[0] + sum2[1];
  pk[17][0] = 0u;
  pk[17][1] = 0u;
  sum = xsum16_32(sum);
  const int q4 = (lane >> 2) & 3, pp = lane & 3;
  const char* vp0 = vs + (wid * 16 + 4 * g + q4) * 64 + ((g & 1) << 5) + pp * 8;
  const char* vp1 = vs + (wid * 16 + 4 * g + q4) * 64 + (((g & 1) ^ 1) << 5) + pp * 8;
  f32x4 o0 = {0.f, 0.f, 0.f, 0.f}, o1 = {0.f, 0.f, 0.f, 0.f};
#pragma unroll
  for (int tp = 0; tp < 9; ++tp) {
    const int t0 = 2 * tp, t1 = (2 * tp + 1 < 17) ? 2 * tp + 1 : 16;
    u32x4 pb = {pk[2 * tp][0], pk[2 * tp][1], pk[2 * tp + 1][0], pk[2 * tp + 1][1]};
    bf16x8 pf = __builtin_bit_cast(bf16x8, pb);
    s16x4 a0 = vtr((lds_cptr)(vp0 + t0 * 1024)), a1 = vtr((lds_cptr)(vp0 + t1 * 1024));
    s16x4 b0 = vtr((lds_cptr)(vp1 + t0 * 1024)), b1 = vtr((lds_cptr)(vp1 + t1 * 1024));
    o0 = __builtin_amdgcn_mfma_f32_16x16x32_bf16(__builtin_shufflevector(a0, a1, 0, 1, 2, 3, 4, 5, 6, 7), pf, o0, 0, 0, 0);
    o1 = __builtin_amdgcn_mfma_f32_16x16x32_bf16(__builtin_shufflevector(b0, b1, 0, 1, 2, 3, 4, 5, 6, 7), pf, o1, 0, 0, 0);
  }
  const float inv = 1.0f / sum;
  u32x2 w0, w1;
  w0[0] = pk2(o0[0] * inv, o0[1] * inv);
  w0[1] = pk2(o0[2] * inv, o0[3] * inv);
  w1[0] = pk2(o1[0] * inv, o1[1] * inv);
  w1[1] = pk2(o1[2] * inv, o1[3] * inv);
  *(u32x2*)(op + 8 * g) = w0;
  *(u32x2*)(op + 32 + 8 * g) = w1;
}

enum { EPI_QKV = 0, EPI_RES_STATS = 1, EPI_LN_GELU = 2, EPI_SPLIT_BIAS_RES = 3 };
constexpr int NLW = 4;

template <int CNT, int NBUF, int NPA, int STAGE>
__device__ __forceinline__ void loader_loop(const char* Ab, const char* Wb, const unsigned* voff, int lw, int nk,
                                            unsigned wstep, unsigned lds0) {
  const char* sp[CNT];
  unsigned st[CNT];
#pragma unroll
  for (int i = 0; i < CNT; ++i) {
    const bool isA = (lw + i * NLW) < NPA;
    sp[i] = isA ? Ab : Wb;
    st[i] = isA ? 128u : wstep;
  }
  auto issue = [&](int buf) {
#pragma unroll
    for (int i = 0; i < CNT; ++i) {
      glds16(sp[i], voff[i], lds0 + buf * STAGE + (lw + i * NLW) * 1024);
      sp[i] += st[i];
    }
  };
  issue(0);
  issue(1);
  asm volatile("s_waitcnt vmcnt(%0)" ::"i"(CNT) : "memory");
  __builtin_amdgcn_s_barrier();
#pragma unroll
  for (int s = 2; s < NBUF; ++s) issue(s);
  int buf = 0;
  for (int t = 0; t < nk; ++t) {
    if (t + NBUF <= nk) {
      asm volatile("s_waitcnt vmcnt(%0)" ::"i"((NBUF - 2) * CNT) : "memory");
    } else {
      asm volatile("s_waitcnt vmcnt(0)" ::: "memory");
    }
    __builtin_amdgcn_s_barrier();
    if (t + NBUF < nk) issue(buf);
    buf = (buf + 1 == NBUF) ? 0 : buf + 1;
  }
}

template <int BN, int NBUF, int EPI, int XA, int TL0 = 0, int TL1 = 0>
__device__ __forceinline__ void gemm_body(const char* __restrict__ tp0, const char* __restrict__ tp1, const u16* __restrict__ A, const u16* __restrict__ W,
                                          u16* __restrict__ C, const u16* __restrict__ bias,
                                          const u16* __restrict__ res, float* __restrict__ aux0,
                                          float* __restrict__ aux1, unsigned* __restrict__ flags,
                                          const int N, const int K) {
  constexpr int BM = 128;
  constexpr bool WIDE = BN > 64;
  constexpr bool SPLIT = (EPI == EPI_SPLIT_BIAS_RES);
  constexpr int NTHR = 512;
  constexpr int NG = BN / 64;
  constexpr bool HALF = (BN % 64) != 0;
  constexpr int NT = WIDE ? BN / 32 : 4;
  constexpr int ABYTES = BM * 128, WBYTES = 64 * BN * 2, STAGE = ABYTES + WBYTES;
  constexpr int NPA = ABYTES / 1024, NP = STAGE / 1024;
  constexpr int CMAX = (NP + NLW - 1) / NLW;
  constexpr int TSCR = NBUF * STAGE + 4096;
  constexpr int SIDE = NBUF * STAGE;
  extern __shared__ __attribute__((aligned(16))) char smem[];

  const int tid = threadIdx.x, lane = tid & 63;
  const int wid = __builtin_amdgcn_readfirstlane(tid >> 6);
  STAMP(EPI, 0);
  const int xc = blockIdx.x & 7, rr = blockIdx.x >> 3;
  int bm, bn, hk = 0;
  if (SPLIT) {
    hk = rr & 1;
    const int tt = rr >> 1;
    bm = (xc & 3) * 4 + (tt & 3);
    bn = (xc >> 2) * 4 + (tt >> 2);
  } else {
    constexpr int XB = 32 / XA, PM = 16 / XA;
    bm = (xc % PM) * XA + (rr % XA);
    bn = (xc / PM) * XB + (rr / XA);
  }
  const int m0 = bm * BM, n0 = bn * BN;
  const int nk = SPLIT ? K / 128 : K / 64;
  const int kbeg = hk * (K / 2);
  const int odd = HALF ? (bn & 1) : 0;
  const int cfull = odd ? 32 : 0, chalf = odd ? 0 : NG * 64;
  const unsigned lds0 = (unsigned)(uintptr_t)(__attribute__((address_space(3))) char*)smem;
  const int fr = lane & 15, g = lane >> 4;

  f32x4 acc[4][NT];
  const int wm = wid & 1, wx = (wid >> 1) & 1;
  int tcol[NT];

  if (wid >= 4) {
    const int lw = wid - 4;
    unsigned voff[CMAX];
#pragma unroll
    for (int i = 0; i < CMAX; ++i) {
      const int idx = lw + i * NLW;
      if (idx < NPA) {
        const int row = idx * 8 + (lane >> 3), ch = (lane & 7) ^ (row & 7);
        voff[i] = (unsigned)(((m0 + row) * K + ch * 8) * 2);
      } else if (idx < NPA + NG * 8) {
        const int w = idx - NPA, G = w >> 3, k = (w & 7) * 8 + (lane >> 3);
        const int ch = (lane & 7) ^ ((k & 2) | ((k >> 1) & 4));
        const int col = (EPI == EPI_QKV) ? G * DM + bn * 64 : n0 + cfull + G * 64;
        voff[i] = (unsigned)((k * N + col + ch * 8) * 2);
      } else {
        const int w = idx - NPA - NG * 8, k = w * 16 + (lane >> 2);
        const int ch = (lane & 3) ^ ((k >> 2) & 2);
        voff[i] = (unsigned)((k * N + n0 + chalf + ch * 8) * 2);
      }
    }
    const unsigned wstep = (unsigned)(64 * N * 2);
    const char* Ab = (const char*)(A + kbeg);
    const char* Wb = (const char*)(W + (size_t)kbeg * N);
    if (lw + (CMAX - 1) * NLW < NP)
      loader_loop<CMAX, NBUF, NPA, STAGE>(Ab, Wb, voff, lw, nk, wstep, lds0);
    else
      loader_loop<CMAX - 1, NBUF, NPA, STAGE>(Ab, Wb, voff, lw, nk, wstep, lds0);
  } else {
    if (EPI == EPI_LN_GELU) {
      if (tid < 128) {
        const f32x4* sp = (const f32x4*)(aux0 + (size_t)(m0 + tid) * 32);
        f32x4 p[8];
#pragma unroll
        for (int i = 0; i < 8; ++i) p[i] = sp[i];
        float mean = 0.f;
#pragma unroll
        for (int i = 0; i < 8; ++i) mean += p[i][0] + p[i][2];
        mean *= (1.0f / 16.0f);
        float m2 = 0.f;
#pragma unroll
        for (int i = 0; i < 8; ++i) {
          const float d0 = p[i][0] - mean, d1 = p[i][2] - mean;
          m2 += p[i][1] + p[i][3] + 64.0f * (d0 * d0 + d1 * d1);
        }
        const float var = m2 * (1.0f / DM);
        const float r1 = 1.0f / sqrtf(var + LN_EPS);
        const float r2 = 1.0f / sqrtf(var * r1 * r1 + LN_EPS);
        *(f32x2*)(smem + SIDE + tid * 8) = f32x2{mean, r1 * r2};
      } else if (tid - 128 < BN / 2) {
        const int c = (tid - 128) * 2;
        f32x2 cs = {0.f, 0.f};
#pragma unroll
        for (int kc = 0; kc < 16; ++kc) cs += *(const f32x2*)(aux1 + (size_t)kc * FF + n0 + c);
        const unsigned bb = *(const unsigned*)(bias + n0 + c);
        *(f32x4*)(smem + SIDE + 1024 + c * 8) = f32x4{cs[0], bflo(bb), cs[1], bfhi(bb)};
      }
    }
    const int q = (lane >> 2) & 3, pp = lane & 3;
    const int wk = WIDE ? 0 : wx;
    int aoff[2];
#pragma unroll
    for (int ks = 0; ks < 2; ++ks) aoff[ks] = (wm * 64 + fr) * 128 + (((ks * 4 + g) ^ (fr & 7)) << 4);
    int tb[NT];
    const int f1 = (q & 2) | ((g & 1) << 2);
#pragma unroll
    for (int j = 0; j < NT; ++j) {
      int G, tp;
      if (!WIDE) { G = 0; tp = j; }
      else if (EPI == EPI_QKV) { G = (j >> 1) ^ 2; G = G == 3 ? 1 : G; tp = wx * 2 + (j & 1); }
      else if (j < 4) { G = wx * (NG - 1); tp = j; }
      else { G = 1; tp = wx * 2 + (j - 4); }
      if (WIDE && HALF && j == NT - 1) {
        tb[j] = ABYTES + NG * 8192 + (8 * g + q) * 64 + (((2 * wx + (pp >> 1)) ^ ((g & 1) << 1)) << 4) + (pp & 1) * 8;
        tcol[j] = chalf + wx * 16;
      } else {
        tb[j] = ABYTES + G * 8192 + (8 * g + q) * 128 + (((2 * tp + (pp >> 1)) ^ f1) << 4) + (pp & 1) * 8;
        tcol[j] = cfull + G * 64 + tp * 16;
      }
    }
#pragma unroll
    for (int i = 0; i < 4; ++i)
#pragma unroll
      for (int j = 0; j < NT; ++j) acc[i][j] = f32x4{0.f, 0.f, 0.f, 0.f};

    bf16x8 xa[4], wa[NT], xb[4], wb[NT];
    auto rd = [&](bf16x8* xf, bf16x8* wf, int buf, int ks) {
      const char* base = smem + buf * STAGE;
#pragma unroll
      for (int j = 0; j < NT; ++j) {
        const int rs = (WIDE && HALF && j == NT - 1) ? 64 : 128;
        s16x4 lo = vtr((lds_cptr)(base + tb[j] + ks * 32 * rs));
        s16x4 hi = vtr((lds_cptr)(base + tb[j] + ks * 32 * rs + 4 * rs));
        wf[j] = __builtin_shufflevector(lo, hi, 0, 1, 2, 3, 4, 5, 6, 7);
      }
#pragma unroll
      for (int i = 0; i < 4; ++i) xf[i] = *(const bf16x8*)(base + aoff[ks] + i * 2048);
    };
    auto mm = [&](const bf16x8* xf, const bf16x8* wf) {
#pragma unroll
      for (int i = 0; i < 4; ++i)
#pragma unroll
        for (int j = 0; j < NT; ++j)
          acc[i][j] = __builtin_amdgcn_mfma_f32_16x16x32_bf16(wf[j], xf[i], acc[i][j], 0, 0, 0);
    };
#define INTERLEAVE() do { _Pragma("unroll") for (int z_ = 0; z_ < 2 * NT + 4; ++z_) { __builtin_amdgcn_sched_group_barrier(0x8, 1, 0); __builtin_amdgcn_sched_group_barrier(0x100, 1, 0); } __builtin_amdgcn_sched_group_barrier(0x8, 4 * NT - (2 * NT + 4), 0); } while (0)
    __builtin_amdgcn_s_waitcnt(0xC07F);
    __builtin_amdgcn_s_barrier();
    STAMP(EPI, 1);
    auto do_touch = [&](int i) {
      const int line = ((blockIdx.x * 4 + wid) * 2 + i) * 64 + lane;
      if (__builtin_amdgcn_readfirstlane(line) < TL0 + TL1) {
        const int lc = line < TL0 + TL1 ? line : TL0 + TL1 - 1;
        const char* p = lc < TL0 ? tp0 + (size_t)lc * 128 : tp1 + (size_t)(lc - TL0) * 128;
        unsigned sv;
        asm volatile("s_mov_b32 %0, m0\n\ts_mov_b32 m0, %2\n\ts_nop 0\n\tglobal_load_lds_dword %1, off\n\ts_mov_b32 m0, %0"
                     : "=&s"(sv) : "v"(p), "s"(lds0 + TSCR + wid * 256) : "memory");
      }
    };
    if (WIDE) {
      rd(xa, wa, 0, 0);
      int b0 = 0;
      for (int t = 0; t < nk; ++t) {
        if (TL0 + TL1 > 0) { if (t == 2) do_touch(0); if (t == 7) do_touch(1); }
        rd(xb, wb, b0, 1);
        b0 = (b0 + 1 == NBUF) ? 0 : b0 + 1;
        mm(xa, wa);
        INTERLEAVE();
        __builtin_amdgcn_sched_barrier(0);
        __builtin_amdgcn_s_waitcnt(0xC07F);
        __builtin_amdgcn_s_barrier();
        rd(xa, wa, b0, 0);
        mm(xb, wb);
        INTERLEAVE();
        __builtin_amdgcn_sched_barrier(0);
      }
    } else {
      rd(xa, wa, 0, wk);
      int b1 = 1;
      for (int t = 0; t < nk; t += 2) {
        __builtin_amdgcn_s_waitcnt(0xC07F);
        __builtin_amdgcn_s_barrier();
        rd(xb, wb, b1, wk);
        b1 = (b1 + 1 == NBUF) ? 0 : b1 + 1;
        mm(xa, wa);
        INTERLEAVE();
        __builtin_amdgcn_sched_barrier(0);
        __builtin_amdgcn_s_waitcnt(0xC07F);
        __builtin_amdgcn_s_barrier();
        rd(xa, wa, b1, wk);
        b1 = (b1 + 1 == NBUF) ? 0 : b1 + 1;
        mm(xb, wb);
        INTERLEAVE();
        __builtin_amdgcn_sched_barrier(0);
      }
    }
    __builtin_amdgcn_s_waitcnt(0xC07F);
    if (TL0 + TL1 > 0) asm volatile("s_waitcnt vmcnt(0)" ::: "memory");
    STAMP(EPI, 2);
    if (SPLIT) {
      const int tile = bm * 8 + bn;
      const auto rsrc = __builtin_amdgcn_make_buffer_rsrc(aux0, 0, 128 * 2 * 2 * 16384, 0x00020000);
      if (wm != hk) {
        const int slot = ((tile * 2 + hk) * 2 + wx);
#pragma unroll
        for (int i = 0; i < 4; ++i)
#pragma unroll
          for (int j = 0; j < NT; ++j)
            __builtin_amdgcn_raw_buffer_store_b128(__builtin_bit_cast(u32x4, acc[i][j]), rsrc,
                                                   slot * 16384 + (i * NT + j) * 1024 + lane * 16, 0, 16);
        asm volatile("s_waitcnt vmcnt(0)" ::: "memory");
        if (lane == 0) __hip_atomic_store(flags + slot * 32, 1u, __ATOMIC_RELAXED, __HIP_MEMORY_SCOPE_AGENT);
      } else {
        const int slot = ((tile * 2 + (hk ^ 1)) * 2 + wx);
        for (int spin = 0; spin < (1 << 24); ++spin) {
          if (__hip_atomic_load(flags + slot * 32, __ATOMIC_RELAXED, __HIP_MEMORY_SCOPE_AGENT) != 0u) break;
          __builtin_amdgcn_s_sleep(2);
        }
        asm volatile("" ::: "memory");
#pragma unroll
        for (int i = 0; i < 4; ++i) {
          u32x4 t[NT];
#pragma unroll
          for (int j = 0; j < NT; ++j)
            t[j] = __builtin_amdgcn_raw_buffer_load_b128(rsrc, slot * 16384 + (i * NT + j) * 1024 + lane * 16, 0, 16);
#pragma unroll
          for (int j = 0; j < NT; ++j) acc[i][j] += __builtin_bit_cast(f32x4, t[j]);
        }
      }
    }
  }
  __syncthreads();
  STAMP(EPI, 3);

  constexpr int CPR = BN / 8;
  if (EPI == EPI_QKV) {
    constexpr int KIMG = 0, VIMG = 49152, QIMG = 98304, RED = 114688, HIMG = 24576;
    u16* kvg = (u16*)aux0;
    u32x4 cw[8];
    const int t2 = tid - 256;
    const int cb = blockIdx.x < 224 ? blockIdx.x : 223;
    const int cg = cb % 14, kc = cb / 14;
    if (wid < 4) {
      auto put = [&](int j) {
        const int grp = tcol[j] >> 6, hd = (tcol[j] >> 5) & 1;
        const int dd = (tcol[j] & 31) + g * 4, chn = dd >> 3, hf = (dd >> 2) & 1;
        const int sw = (grp == 2) ? (((fr >> 2) & 1) << 1) : ((4 - ((fr >> 2) & 3)) & 3);
        const int base = (grp == 0) ? QIMG + hd * 8192 : ((grp == 1) ? KIMG : VIMG) + hd * HIMG + 256 * 64;
#pragma unroll
        for (int i = 0; i < 4; ++i) {
          u32x2 o;
          o[0] = pk2(acc[i][j][0], acc[i][j][1]);
          o[1] = pk2(acc[i][j][2], acc[i][j][3]);
          *(u32x2*)(smem + base + (wm * 64 + i * 16 + fr) * 64 + ((chn ^ sw) << 4) + hf * 8) = o;
        }
      };
#pragma unroll
      for (int j = 0; j < 4; ++j) put(j);
      if (bm < 15) {
#pragma unroll
        for (int kv = 0; kv < 2; ++kv)
#pragma unroll
          for (int pc = 0; pc < 4; ++pc) {
            const int row = wm * 64 + pc * 16 + (lane >> 2), slot = lane & 3;
            const int sw = kv ? (((row >> 2) & 1) << 1) : ((4 - ((row >> 2) & 3)) & 3);
            const u32x4 v = *(const u32x4*)(smem + (kv ? VIMG : KIMG) + wx * HIMG + (256 + row) * 64 + slot * 16);
            ST16(kvg + ((size_t)(kv * NH + bn * 2 + wx) * S_LEN + m0 + row) * 32 + (slot ^ sw) * 8, v);
          }
      }
      put(4);
      put(5);
      if (bm < 15) {
        asm volatile("s_waitcnt vmcnt(0)" ::: "memory");
        if (lane == 0)
          __hip_atomic_fetch_add(flags + (bm * 16 + bn) * 32, 1u, __ATOMIC_RELAXED, __HIP_MEMORY_SCOPE_AGENT);
      }
    } else {
      const u16* cp = res + (size_t)(kc * 64 + (t2 >> 5) * 8) * FF + cg * 256 + (t2 & 31) * 8;
#pragma unroll
      for (int i = 0; i < 8; ++i) cw[i] = *(const u32x4*)(cp + (size_t)i * FF);
      const int lw = wid - 4, pr = lw >> 1, kv = lw & 1;
      const int pb = bm - 2 + pr;
      const unsigned dst = lds0 + (kv ? VIMG : KIMG) + pr * 128 * 64;
      if (pb >= 0) {
        const unsigned* fp = flags + (pb * 16 + bn) * 32;
        for (int spin = 0; spin < (1 << 24); ++spin) {
          if (__hip_atomic_load(fp, __ATOMIC_RELAXED, __HIP_MEMORY_SCOPE_AGENT) >= 4u) break;
          __builtin_amdgcn_s_sleep(2);
        }
        asm volatile("" ::: "memory");
        const int kl = lane >> 2, slot = lane & 3;
        const int sw = kv ? (((kl >> 2) & 1) << 1) : ((4 - ((kl >> 2) & 3)) & 3);
        const unsigned vo = (unsigned)((kl * 32 + (slot ^ sw) * 8) * 2);
#pragma unroll
        for (int hd = 0; hd < 2; ++hd) {
          const u16* src = kvg + ((size_t)(kv * NH + bn * 2 + hd) * S_LEN + pb * 128) * 32;
#pragma unroll
          for (int pc = 0; pc < 8; ++pc) glds16_sc1(src + pc * 512, vo, dst + hd * HIMG + pc * 1024);
        }
      } else {
#pragma unroll
        for (int hd = 0; hd < 2; ++hd)
#pragma unroll
          for (int pc = 0; pc < 8; ++pc)
            *(u32x4*)(smem + (kv ? VIMG : KIMG) + pr * 128 * 64 + hd * HIMG + pc * 1024 + lane * 16) = u32x4{0u, 0u, 0u, 0u};
      }
      {
        float* red = (float*)(smem + RED);
        float sacc[8];
#pragma unroll
        for (int e = 0; e < 8; ++e) sacc[e] = 0.f;
#pragma unroll
        for (int i = 0; i < 8; ++i)
#pragma unroll
          for (int e = 0; e < 4; ++e) { sacc[2 * e] += bflo(cw[i][e]); sacc[2 * e + 1] += bfhi(cw[i][e]); }
        *(f32x4*)(red + (t2 >> 5) * 256 + (t2 & 31) * 8) = f32x4{sacc[0], sacc[1], sacc[2], sacc[3]};
        *(f32x4*)(red + (t2 >> 5) * 256 + (t2 & 31) * 8 + 4) = f32x4{sacc[4], sacc[5], sacc[6], sacc[7]};
      }
      asm volatile("s_waitcnt vmcnt(0)" ::: "memory");
    }
    STAMP(EPI, 5);
    __syncthreads();
    STAMP(EPI, 6);
    if (wid < 4) {
      const int li = (rr * 4 + wid) * 64 + lane;
      const char* p = tp1 + (size_t)(li >> 3) * 2048 + (xc >> 2) * 1024 + (li & 7) * 128;
      unsigned sv;
      asm volatile("s_mov_b32 %0, m0\n\ts_mov_b32 m0, %2\n\ts_nop 0\n\tglobal_load_lds_dword %1, off\n\ts_mov_b32 m0, %0"
                   : "=&s"(sv) : "v"(p), "s"(lds0 + TSCR + wid * 256) : "memory");
    }
    if (blockIdx.x < 224 && tid < 256) {
      const float* red = (const float*)(smem + RED);
      float t = 0.f;
#pragma unroll
      for (int i = 0; i < 8; ++i) t += red[i * 256 + tid];
      aux1[(size_t)kc * FF + cg * 256 + tid] = t;
    }
    const int qw = m0 + wid * 16;
    const int tmin = (qw < WIN) ? (WIN - qw) >> 4 : 0;
    const int qo = (wid * 16 + fr) * 64 + ((g ^ ((4 - ((fr >> 2) & 3)) & 3)) << 4);
    const bf16x8 qf0 = *(const bf16x8*)(smem + QIMG + qo), qf1 = *(const bf16x8*)(smem + QIMG + 8192 + qo);
    char* ost = smem + QIMG + wid * 1024;
#pragma unroll 1
    for (int hd = 0; hd < 2; ++hd) {
      const bf16x8 qf = hd ? qf1 : qf0;
      char* op = ost + hd * 8192 + fr * 64;
      if (bm < 2) attn_body<true>(smem + KIMG + hd * HIMG, smem + VIMG + hd * HIMG, qf, wid, lane, tmin, op);
      else attn_body<false>(smem + KIMG + hd * HIMG, smem + VIMG + hd * HIMG, qf, wid, lane, 0, op);
    }
#pragma unroll
    for (int i = 0; i < 2; ++i) {
      const int row = i * 8 + (lane >> 3), ch = lane & 7;
      const u32x4 v = *(const u32x4*)(ost + (ch >> 2) * 8192 + row * 64 + (ch & 3) * 16);
      ST16(C + (size_t)(qw + row) * DM + bn * 64 + ch * 8, v);
    }
    STAMP(EPI, 7);
  } else if (EPI == EPI_LN_GELU) {
    constexpr int RS = BN * 4 + 16;
    if (wid < 4) {
#pragma unroll
      for (int j = 0; j < NT; ++j)
#pragma unroll
        for (int i = 0; i < 4; ++i)
          *(f32x4*)(smem + (wm * 64 + i * 16 + fr) * RS + (tcol[j] + g * 4) * 4) = acc[i][j];
    }
    __syncthreads();
#pragma unroll
    for (int c = tid; c < BM * CPR; c += NTHR) {
      const int row = c / CPR, ch = c % CPR;
      const f32x4 a0 = *(const f32x4*)(smem + row * RS + ch * 32);
      const f32x4 a1 = *(const f32x4*)(smem + row * RS + ch * 32 + 16);
      const f32x2 rs = *(const f32x2*)(smem + SIDE + row * 8);
      float v[8];
#pragma unroll
      for (int r = 0; r < 4; ++r) {
        const f32x4 cb = *(const f32x4*)(smem + SIDE + 1024 + ch * 64 + r * 16);
        const float x0 = (r < 2) ? a0[2 * r] : a1[2 * r - 4];
        const float x1 = (r < 2) ? a0[2 * r + 1] : a1[2 * r - 3];
        v[2 * r] = (x0 - rs[0] * cb[0]) * rs[1] + cb[1];
        v[2 * r + 1] = (x1 - rs[0] * cb[2]) * rs[1] + cb[3];
      }
      u32x4 o;
#pragma unroll
      for (int r = 0; r < 4; ++r) {
        const f32x2 x = {v[2 * r], v[2 * r + 1]};
        const f32x2 x2 = x * x;
        const f32x2 w = x2 * f32x2{-0.10294324f, -0.10294324f} + f32x2{-2.302208198f, -2.302208198f};
        const f32x2 z = x * w;
        f32x2 e = {__builtin_amdgcn_exp2f(z[0]), __builtin_amdgcn_exp2f(z[1])};
        e = e + f32x2{1.0f, 1.0f};
        const f32x2 y = x * f32x2{__builtin_amdgcn_rcpf(e[0]), __builtin_amdgcn_rcpf(e[1])};
        o[r] = pk2(y[0], y[1]);
      }
      ST16(C + (size_t)(m0 + row) * N + n0 + ch * 8, o);
    }
  } else if (EPI == EPI_RES_STATS) {
    constexpr int RS = BN * 4 + 16;
    constexpr int PART = BM * RS;
    if (wid < 4) {
#pragma unroll
      for (int i = 0; i < 4; ++i)
#pragma unroll
        for (int j = 0; j < NT; ++j)
          *(f32x4*)(smem + wx * PART + (wm * 64 + i * 16 + fr) * RS + (j * 16 + g * 4) * 4) = acc[i][j];
    }
    __syncthreads();
#pragma unroll
    for (int c = tid; c < BM * CPR; c += NTHR) {
      const int row = c / CPR, ch = c % CPR;
      const f32x4 a0 = *(const f32x4*)(smem + row * RS + ch * 32);
      const f32x4 a1 = *(const f32x4*)(smem + row * RS + ch * 32 + 16);
      const f32x4 b0 = *(const f32x4*)(smem + PART + row * RS + ch * 32);
      const f32x4 b1 = *(const f32x4*)(smem + PART + row * RS + ch * 32 + 16);
      const u32x4 rv = *(const u32x4*)(res + (size_t)(m0 + row) * N + n0 + ch * 8);
      u32x4 o;
      o[0] = pk2(a0[0] + b0[0] + bflo(rv[0]), a0[1] + b0[1] + bfhi(rv[0]));
      o[1] = pk2(a0[2] + b0[2] + bflo(rv[1]), a0[3] + b0[3] + bfhi(rv[1]));
      o[2] = pk2(a1[0] + b1[0] + bflo(rv[2]), a1[1] + b1[1] + bfhi(rv[2]));
      o[3] = pk2(a1[2] + b1[2] + bflo(rv[3]), a1[3] + b1[3] + bfhi(rv[3]));
      ST16(C + (size_t)(m0 + row) * N + n0 + ch * 8, o);
      float s = 0.f;
#pragma unroll
      for (int r = 0; r < 4; ++r) s += bflo(o[r]) + bfhi(o[r]);
      const float mean = sum8(s) * (1.0f / 64.0f);
      float m2 = 0.f;
#pragma unroll
      for (int r = 0; r < 4; ++r) {
        const float d0 = bflo(o[r]) - mean, d1 = bfhi(o[r]) - mean;
        m2 += d0 * d0 + d1 * d1;
      }
      m2 = sum8(m2);
      if (ch == 0) *(f32x2*)(aux0 + ((size_t)(m0 + row) * 16 + bn) * 2) = f32x2{mean, m2};
    }
  } else {
    constexpr int RS = BN * 4 + 16;
    if (wid < 4 && wm == hk) {
#pragma unroll
      for (int i = 0; i < 4; ++i)
#pragma unroll
        for (int j = 0; j < NT; ++j)
          *(f32x4*)(smem + (i * 16 + fr) * RS + (tcol[j] + g * 4) * 4) = acc[i][j];
    }
    __syncthreads();
#pragma unroll
    for (int c = tid; c < 64 * CPR; c += NTHR) {
      const int row = c / CPR, ch = c % CPR;
      const f32x4 a0 = *(const f32x4*)(smem + row * RS + ch * 32);
      const f32x4 a1 = *(const f32x4*)(smem + row * RS + ch * 32 + 16);
      const u32x4 bb = *(const u32x4*)(bias + n0 + ch * 8);
      const size_t go = (size_t)(m0 + hk * 64 + row) * N + n0 + ch * 8;
      const u32x4 rv = *(const u32x4*)(res + go);
      u32x4 o;
      o[0] = pk2(a0[0] + bflo(bb[0]) + bflo(rv[0]), a0[1] + bfhi(bb[0]) + bfhi(rv[0]));
      o[1] = pk2(a0[2] + bflo(bb[1]) + bflo(rv[1]), a0[3] + bfhi(bb[1]) + bfhi(rv[1]));
      o[2] = pk2(a1[0] + bflo(bb[2]) + bflo(rv[2]), a1[1] + bfhi(bb[2]) + bfhi(rv[2]));
      o[3] = pk2(a1[2] + bflo(bb[3]) + bflo(rv[3]), a1[3] + bfhi(bb[3]) + bfhi(rv[3]));
      ST16(C + go, o);
    }
  }
}

__global__ __launch_bounds__(512, 1) void qkv_attn_kernel(const u16* __restrict__ A, const u16* __restrict__ W,
                                                         u16* __restrict__ o, const char* __restrict__ wo,
                                                         const u16* __restrict__ w1, float* __restrict__ kvg,
                                                         float* __restrict__ cspart, unsigned* __restrict__ flags) {
  gemm_body<192, 3, EPI_QKV, 8, 57344, 0>((const char*)w1, wo, A, W, o, nullptr, w1, kvg, cspart, flags, 3 * DM, DM);
}
__global__ __launch_bounds__(512, 1) void gemm_wo_kernel(const u16* __restrict__ A, const u16* __restrict__ W,
                                                        u16* __restrict__ C, const u16* __restrict__ res,
                                                        float* __restrict__ stats) {
  gemm_body<64, 6, EPI_RES_STATS, 4>(nullptr, nullptr, A, W, C, nullptr, res, stats, nullptr, nullptr, DM, DM);
}
__global__ __launch_bounds__(512, 1) void gemm_w1_kernel(const u16* __restrict__ A, const u16* __restrict__ W,
                                                        u16* __restrict__ C, const u16* __restrict__ bias,
                                                        float* __restrict__ stats, float* __restrict__ cspart,
                                                        const char* __restrict__ t0) {
  gemm_body<224, 3, EPI_LN_GELU, 8, 57344, 0>(t0, t0, A, W, C, bias, nullptr, stats, cspart, nullptr, FF, DM);
}
__global__ __launch_bounds__(512, 1) void gemm_w2_kernel(const u16* __restrict__ A, const u16* __restrict__ W,
                                                        u16* __restrict__ C, const u16* __restrict__ bias,
                                                        const u16* __restrict__ res, float* __restrict__ xchg,
                                                        unsigned* __restrict__ flags) {
  gemm_body<128, 4, EPI_SPLIT_BIAS_RES, 4>(nullptr, nullptr, A, W, C, bias, res, xchg, nullptr, flags, DM, FF);
}
extern "C" __global__ void zz_text_end() {}

constexpr int lds_bytes(int BN, int NBUF, bool side) { return NBUF * (128 * 64 * 2 + 64 * BN * 2) + (side ? 5120 : 0); }

extern "C" void kernel_launch(void* const* d_in, const int* in_sizes, int n_in, void* d_out, int out_size,
                              void* d_ws, size_t ws_size, hipStream_t stream) {
  const u16* x = (const u16*)d_in[0];
  const u16* wqkv = (const u16*)d_in[1];
  const u16* wo = (const u16*)d_in[2];
  const u16* w1 = (const u16*)d_in[4];
  const u16* b1 = (const u16*)d_in[5];
  const u16* w2 = (const u16*)d_in[6];
  const u16* b2 = (const u16*)d_in[7];
  u16* out = (u16*)d_out;
  char* ws = (char*)d_ws;
  u16* xn = (u16*)(ws);
  float* xchg = (float*)(ws);
  u16* h = (u16*)(ws + (8u << 20));
  u16* qkv = (u16*)(ws + (12u << 20));
  u16* o = (u16*)(ws + (20u << 20));
  u16* hh = (u16*)(ws + (12u << 20));
  float* stats = (float*)(ws + (26u << 20));
  float* cspart = (float*)(ws + (26u << 20) + (256u << 10));
  unsigned* flags = (unsigned*)(ws + (26u << 20) + (512u << 10));

  (void)hipFuncSetAttribute((const void*)qkv_attn_kernel, hipFuncAttributeMaxDynamicSharedMemorySize, lds_bytes(192, 3, true));
  (void)hipFuncSetAttribute((const void*)gemm_wo_kernel, hipFuncAttributeMaxDynamicSharedMemorySize, lds_bytes(64, 6, false));
  (void)hipFuncSetAttribute((const void*)gemm_w1_kernel, hipFuncAttributeMaxDynamicSharedMemorySize, lds_bytes(224, 3, true));
  (void)hipFuncSetAttribute((const void*)gemm_w2_kernel, hipFuncAttributeMaxDynamicSharedMemorySize, lds_bytes(128, 4, false));
  prep_kernel<<<S_LEN / 4, 256, 0, stream>>>(x, xn, (const char*)wqkv, (const char*)b1, (const char*)b2, flags);
  qkv_attn_kernel<<<256, 512, lds_bytes(192, 3, true), stream>>>(xn, wqkv, o, (const char*)wo, w1, (float*)qkv, cspart, flags + 512 * 32);
  gemm_wo_kernel<<<256, 512, lds_bytes(64, 6, false), stream>>>(o, wo, h, x, stats);
  gemm_w1_kernel<<<256, 512, lds_bytes(224, 3, true), stream>>>(h, w1, hh, b1, stats, cspart, (const char*)w2);
  gemm_w2_kernel<<<256, 512, lds_bytes(128, 4, false), stream>>>(hh, w2, out, b2, h, xchg, flags);
}
```

```cpp
#include <hip/hip_runtime.h>
#include <hip/hip_bf16.h>
#include <stdint.h>

typedef unsigned short u16;
typedef __attribute__((ext_vector_type(8))) short bf16x8;
typedef __attribute__((ext_vector_type(4))) short s16x4;
typedef __attribute__((ext_vector_type(4))) float f32x4;
typedef __attribute__((ext_vector_type(2))) float f32x2;
typedef __attribute__((ext_vector_type(4))) unsigned u32x4;
typedef __attribute__((ext_vector_type(2))) unsigned u32x2;
typedef __attribute__((ext_vector_type(2))) __bf16 bf16x2_t;
typedef __attribute__((address_space(3))) const char* lds_cptr;

#define STAMP(kid, i)
__device__ __forceinline__ void st16_sc1(void* p, __attribute__((ext_vector_type(4))) unsigned v) { asm volatile("global_store_dwordx4 %0, %1, off sc1\n\ts_nop 1" :: "v"(p), "v"(v) : "memory"); }
#define ST16(p, v) st16_sc1((void*)(p), (v))
constexpr int S_LEN = 2048, DM = 1024, NH = 32, DH = 32, WIN = 256, FF = 3584;
constexpr float LN_EPS = 1e-5f;

__device__ __forceinline__ float bf2f(u16 v) { return __uint_as_float(((unsigned)v) << 16); }
__device__ __forceinline__ float bflo(unsigned v) { return __uint_as_float(v << 16); }
__device__ __forceinline__ float bfhi(unsigned v) { return __uint_as_float(v & 0xffff0000u); }
__device__ __forceinline__ unsigned pk2(float lo, float hi) {
  return __builtin_bit_cast(unsigned, __builtin_convertvector(f32x2{lo, hi}, bf16x2_t));
}
__device__ __forceinline__ void glds16(const void* sbase, unsigned voff, unsigned lds_addr) {
  unsigned sv;
  asm volatile("s_mov_b32 %0, m0\n\ts_mov_b32 m0, %3\n\ts_nop 0\n\tglobal_load_lds_dwordx4 %1, %2\n\ts_mov_b32 m0, %0"
               : "=&s"(sv) : "v"(voff), "s"(sbase), "s"(lds_addr) : "memory");
}
__device__ __forceinline__ void glds16_sc1(const void* sbase, unsigned voff, unsigned lds_addr) {
  unsigned sv;
  asm volatile("s_mov_b32 %0, m0\n\ts_mov_b32 m0, %3\n\ts_nop 0\n\tglobal_load_lds_dwordx4 %1, %2 sc1\n\ts_mov_b32 m0, %0"
               : "=&s"(sv) : "v"(voff), "s"(sbase), "s"(lds_addr) : "memory");
}
__device__ __forceinline__ void touch_lds(const char* p, unsigned lds_scratch) {
  unsigned sv;
  asm volatile("s_mov_b32 %0, m0\n\ts_mov_b32 m0, %2\n\ts_nop 0\n\tglobal_load_lds_dword %1, off\n\ts_mov_b32 m0, %0"
               : "=&s"(sv) : "v"(p), "s"(lds_scratch) : "memory");
}
__device__ __forceinline__ s16x4 vtr(lds_cptr p) {
  return __builtin_bit_cast(s16x4, __builtin_amdgcn_ds_read_tr16_b64_v4i16((__attribute__((address_space(3))) s16x4*)p));
}
template <int CTRL>
__device__ __forceinline__ float dpp_add(float v) {
  return v + __int_as_float(__builtin_amdgcn_update_dpp(0, __float_as_int(v), CTRL, 0xf, 0xf, false));
}
__device__ __forceinline__ float sum8(float v) {
  v = dpp_add<0xB1>(v);
  v = dpp_add<0x4E>(v);
  return dpp_add<0x141>(v);
}
__device__ __forceinline__ float xsum16_32(float m) {
  auto a = __builtin_amdgcn_permlane32_swap(__float_as_uint(m), __float_as_uint(m), false, false);
  m = __uint_as_float(a[0]) + __uint_as_float(a[1]);
  auto b = __builtin_amdgcn_permlane16_swap(__float_as_uint(m), __float_as_uint(m), false, false);
  return __uint_as_float(b[0]) + __uint_as_float(b[1]);
}
__device__ __forceinline__ float xmax16_32(float m) {
  auto a = __builtin_amdgcn_permlane32_swap(__float_as_uint(m), __float_as_uint(m), false, false);
  m = fmaxf(__uint_as_float(a[0]), __uint_as_float(a[1]));
  auto b = __builtin_amdgcn_permlane16_swap(__float_as_uint(m), __float_as_uint(m), false, false);
  return fmaxf(__uint_as_float(b[0]), __uint_as_float(b[1]));
}
__device__ __forceinline__ float wave_sum(float v) {
  v = sum8(v);
  v = dpp_add<0x140>(v);
  return xsum16_32(v);
}

__global__ __launch_bounds__(256) void prep_kernel(const u16* __restrict__ in, u16* __restrict__ out,
                                                   const char* __restrict__ wqkv, const char* __restrict__ b1,
                                                   const char* __restrict__ b2, unsigned* __restrict__ flags) {
  const int lane = threadIdx.x & 63;
  const int row = blockIdx.x * 4 + (threadIdx.x >> 6);
  const u32x4* src = (const u32x4*)(in + (size_t)row * DM);
  u32x4 a = src[lane], b = src[lane + 64];
  const int gi = blockIdx.x * 256 + threadIdx.x;
  unsigned w0 = 0;
  if (gi < 49152) w0 = *(const unsigned*)(wqkv + (size_t)gi * 128);
  else if (gi < 49152 + 56) w0 = *(const unsigned*)(b1 + (size_t)(gi - 49152) * 128);
  else if (gi < 49152 + 72) w0 = *(const unsigned*)(b2 + (size_t)(gi - 49152 - 56) * 128);
  if (gi < 768) flags[gi * 32] = 0u;
  unsigned w1c = 0;
  if (threadIdx.x < 64 && blockIdx.x < 128) {
    unsigned long long pc, pe;
    asm volatile("s_getpc_b64 %0" : "=s"(pc));
    asm volatile("s_getpc_b64 s[40:41]\n\ts_add_u32 s40, s40, zz_text_end@rel32@lo+4\n\t"
                 "s_addc_u32 s41, s41, zz_text_end@rel32@hi+12\n\ts_mov_b64 %0, s[40:41]"
                 : "=s"(pe) : : "s40", "s41", "scc");
    const unsigned long long a = (pc & ~127ull) + (unsigned long long)((blockIdx.x >> 3) * 64 + lane) * 128;
    if (a + 4 <= pe) w1c = *(const unsigned*)a;
  }
  float v[16];
#pragma unroll
  for (int i = 0; i < 4; ++i) {
    v[2 * i] = bflo(a[i]);
    v[2 * i + 1] = bfhi(a[i]);
    v[8 + 2 * i] = bflo(b[i]);
    v[8 + 2 * i + 1] = bfhi(b[i]);
  }
  float s = 0.f;
#pragma unroll
  for (int i = 0; i < 16; ++i) s += v[i];
  const float mean = wave_sum(s) * (1.0f / DM);
  float q = 0.f;
#pragma unroll
  for (int i = 0; i < 16; ++i) { v[i] -= mean; q += v[i] * v[i]; }
  const float var = wave_sum(q) * (1.0f / DM);
  const float r = 1.0f / sqrtf(var + LN_EPS);
  u32x4 oa, ob;
#pragma unroll
  for (int i = 0; i < 4; ++i) {
    oa[i] = pk2(v[2 * i] * r, v[2 * i + 1] * r);
    ob[i] = pk2(v[8 + 2 * i] * r, v[8 + 2 * i + 1] * r);
  }
  u32x4* dst = (u32x4*)(out + (size_t)row * DM);
  dst[lane] = oa;
  dst[lane + 64] = ob;
  asm volatile("" ::"v"(w0), "v"(w1c));
}

__device__ __forceinline__ float rowmax17(const f32x4* s) {
  float a = fmaxf(fmaxf(s[0][0], s[0][1]), fmaxf(s[0][2], s[0][3]));
#pragma unroll
  for (int t = 1; t < 17; ++t) {
    a = fmaxf(fmaxf(a, s[t][0]), s[t][1]);
    a = fmaxf(fmaxf(a, s[t][2]), s[t][3]);
  }
  return a;
}

template <bool EARLY>
__device__ __forceinline__ void attn_body(const char* ks, const char* vs, bf16x8 qf, int wid, int lane, int tmin,
                                          char* op) {
  const int fr = lane & 15, g = lane >> 4;
  const float NEG = -1e30f;
  f32x4 s[17];
  const char* kp = ks + (wid * 16 + fr) * 64 + ((g ^ ((4 - ((fr >> 2) & 3)) & 3)) << 4);
#pragma unroll
  for (int t = 0; t < 17; ++t) {
    bf16x8 kf = *(const bf16x8*)(kp + t * 1024);
    s[t] = __builtin_amdgcn_mfma_f32_16x16x32_bf16(kf, qf, f32x4{0.f, 0.f, 0.f, 0.f}, 0, 0, 0);
  }
#pragma unroll
  for (int r = 0; r < 4; ++r) {
    s[0][r] = (4 * g + r > fr) ? s[0][r] : NEG;
    s[16][r] = (4 * g + r <= fr) ? s[16][r] : NEG;
  }
  if (EARLY) {
#pragma unroll
    for (int t = 0; t < 16; ++t)
      if (t < tmin) s[t] = f32x4{NEG, NEG, NEG, NEG};
  }
  const float mx = xmax16_32(rowmax17(s));
  const float c = 0.17677669529663687f * 1.4426950408889634f;
  const f32x2 c2 = {c, c};
  const f32x2 nmc2 = {-mx * c, -mx * c};
  f32x2 sum2 = {0.f, 0.f};
  unsigned pk[18][2];
#pragma unroll
  for (int t = 0; t < 17; ++t) {
#pragma unroll
    for (int hh = 0; hh < 2; ++hh) {
      const f32x2 z = f32x2{s[t][2 * hh], s[t][2 * hh + 1]} * c2 + nmc2;
      const f32x2 p = {__builtin_amdgcn_exp2f(z[0]), __builtin_amdgcn_exp2f(z[1])};
      sum2 += p;
      pk[t][hh] = pk2(p[0], p[1]);
    }
  }
  float sum = sum2[0] + sum2[1];
  pk[17][0] = 0u;
  pk[17][1] = 0u;
  sum = xsum16_32(sum);
  const int q4 = (lane >> 2) & 3, pp = lane & 3;
  const char* vp0 = vs + (wid * 16 + 4 * g + q4) * 64 + ((g & 1) << 5) + pp * 8;
  const char* vp1 = vs + (wid * 16 + 4 * g + q4) * 64 + (((g & 1) ^ 1) << 5) + pp * 8;
  f32x4 o0 = {0.f, 0.f, 0.f, 0.f}, o1 = {0.f, 0.f, 0.f, 0.f};
#pragma unroll
  for (int tp = 0; tp < 9; ++tp) {
    const int t0 = 2 * tp, t1 = (2 * tp + 1 < 17) ? 2 * tp + 1 : 16;
    u32x4 pb = {pk[2 * tp][0], pk[2 * tp][1], pk[2 * tp + 1][0], pk[2 * tp + 1][1]};
    bf16x8 pf = __builtin_bit_cast(bf16x8, pb);
    s16x4 a0 = vtr((lds_cptr)(vp0 + t0 * 1024)), a1 = vtr((lds_cptr)(vp0 + t1 * 1024));
    s16x4 b0 = vtr((lds_cptr)(vp1 + t0 * 1024)), b1 = vtr((lds_cptr)(vp1 + t1 * 1024));
    o0 = __builtin_amdgcn_mfma_f32_16x16x32_bf16(__builtin_shufflevector(a0, a1, 0, 1, 2, 3, 4, 5, 6, 7), pf, o0, 0, 0, 0);
    o1 = __builtin_amdgcn_mfma_f32_16x16x32_bf16(__builtin_shufflevector(b0, b1, 0, 1, 2, 3, 4, 5, 6, 7), pf, o1, 0, 0, 0);
  }
  const float inv = 1.0f / sum;
  u32x2 w0, w1;
  w0[0] = pk2(o0[0] * inv, o0[1] * inv);
  w0[1] = pk2(o0[2] * inv, o0[3] * inv);
  w1[0] = pk2(o1[0] * inv, o1[1] * inv);
  w1[1] = pk2(o1[2] * inv, o1[3] * inv);
  *(u32x2*)(op + 8 * g) = w0;
  *(u32x2*)(op + 32 + 8 * g) = w1;
}

enum { EPI_QKV = 0, EPI_RES_STATS = 1, EPI_LN_GELU = 2, EPI_SPLIT_BIAS_RES = 3 };
constexpr int NLW = 4;

template <int CNT, int NBUF, int NPA, int STAGE>
__device__ __forceinline__ void loader_loop(const char* Ab, const char* Wb, const unsigned* voff, int lw, int nk,
                                            unsigned wstep, unsigned lds0) {
  const char* sp[CNT];
  unsigned st[CNT];
#pragma unroll
  for (int i = 0; i < CNT; ++i) {
    const bool isA = (lw + i * NLW) < NPA;
    sp[i] = isA ? Ab : Wb;
    st[i] = isA ? 128u : wstep;
  }
  auto issue = [&](int buf) {
#pragma unroll
    for (int i = 0; i < CNT; ++i) {
      glds16(sp[i], voff[i], lds0 + buf * STAGE + (lw + i * NLW) * 1024);
      sp[i] += st[i];
    }
  };
  issue(0);
  issue(1);
  asm volatile("s_waitcnt vmcnt(%0)" ::"i"(CNT) : "memory");
  __builtin_amdgcn_s_barrier();
#pragma unroll
  for (int s = 2; s < NBUF; ++s) issue(s);
  int buf = 0;
  for (int t = 0; t < nk; ++t) {
    if (t + NBUF <= nk) {
      asm volatile("s_waitcnt vmcnt(%0)" ::"i"((NBUF - 2) * CNT) : "memory");
    } else {
      asm volatile("s_waitcnt vmcnt(0)" ::: "memory");
    }
    __builtin_amdgcn_s_barrier();
    if (t + NBUF < nk) issue(buf);
    buf = (buf + 1 == NBUF) ? 0 : buf + 1;
  }
}

template <int BN, int NBUF, int EPI, int XA, int TL0 = 0, int TL1 = 0>
__device__ __forceinline__ void gemm_body(const char* __restrict__ tp0, const char* __restrict__ tp1, const u16* __restrict__ A, const u16* __restrict__ W,
                                          u16* __restrict__ C, const u16* __restrict__ bias,
                                          const u16* __restrict__ res, float* __restrict__ aux0,
                                          float* __restrict__ aux1, unsigned* __restrict__ flags,
                                          const int N, const int K) {
  constexpr int BM = 128;
  constexpr bool WIDE = BN > 64;
  constexpr bool SPLIT = (EPI == EPI_SPLIT_BIAS_RES);
  constexpr int NTHR = 512;
  constexpr int NG = BN / 64;
  constexpr bool HALF = (BN % 64) != 0;
  constexpr int NT = WIDE ? BN / 32 : 4;
  constexpr int ABYTES = BM * 128, WBYTES = 64 * BN * 2, STAGE = ABYTES + WBYTES;
  constexpr int NPA = ABYTES / 1024, NP = STAGE / 1024;
  constexpr int CMAX = (NP + NLW - 1) / NLW;
  constexpr int TSCR = NBUF * STAGE + 4096;
  constexpr int SIDE = NBUF * STAGE;
  extern __shared__ __attribute__((aligned(16))) char smem[];

  const int tid = threadIdx.x, lane = tid & 63;
  const int wid = __builtin_amdgcn_readfirstlane(tid >> 6);
  STAMP(EPI, 0);
  const int xc = blockIdx.x & 7, rr = blockIdx.x >> 3;
  int bm, bn, hk = 0;
  if (SPLIT) {
    hk = rr & 1;
    const int tt = rr >> 1;
    bm = (xc & 3) * 4 + (tt & 3);
    bn = (xc >> 2) * 4 + (tt >> 2);
  } else {
    constexpr int XB = 32 / XA, PM = 16 / XA;
    bm = (xc % PM) * XA + (rr % XA);
    bn = (xc / PM) * XB + (rr / XA);
  }
  const int m0 = bm * BM, n0 = bn * BN;
  const int nk = SPLIT ? K / 128 : K / 64;
  const int kbeg = hk * (K / 2);
  const int odd = HALF ? (bn & 1) : 0;
  const int cfull = odd ? 32 : 0, chalf = odd ? 0 : NG * 64;
  const unsigned lds0 = (unsigned)(uintptr_t)(__attribute__((address_space(3))) char*)smem;
  const int fr = lane & 15, g = lane >> 4;

  f32x4 acc[4][NT];
  const int wm = wid & 1, wx = (wid >> 1) & 1;
  int tcol[NT];

  if (wid >= 4) {
    const int lw = wid - 4;
    unsigned voff[CMAX];
#pragma unroll
    for (int i = 0; i < CMAX; ++i) {
      const int idx = lw + i * NLW;
      if (idx < NPA) {
        const int row = idx * 8 + (lane >> 3), ch = (lane & 7) ^ (row & 7);
        voff[i] = (unsigned)(((m0 + row) * K + ch * 8) * 2);
      } else if (idx < NPA + NG * 8) {
        const int w = idx - NPA, G = w >> 3, k = (w & 7) * 8 + (lane >> 3);
        const int ch = (lane & 7) ^ ((k & 2) | ((k >> 1) & 4));
        const int col = (EPI == EPI_QKV) ? G * DM + bn * 64 : n0 + cfull + G * 64;
        voff[i] = (unsigned)((k * N + col + ch * 8) * 2);
      } else {
        const int w = idx - NPA - NG * 8, k = w * 16 + (lane >> 2);
        const int ch = (lane & 3) ^ ((k >> 2) & 2);
        voff[i] = (unsigned)((k * N + n0 + chalf + ch * 8) * 2);
      }
    }
    const unsigned wstep = (unsigned)(64 * N * 2);
    const char* Ab = (const char*)(A + kbeg);
    const char* Wb = (const char*)(W + (size_t)kbeg * N);
    if (lw + (CMAX - 1) * NLW < NP)
      loader_loop<CMAX, NBUF, NPA, STAGE>(Ab, Wb, voff, lw, nk, wstep, lds0);
    else
      loader_loop<CMAX - 1, NBUF, NPA, STAGE>(Ab, Wb, voff, lw, nk, wstep, lds0);
  } else {
    if (EPI == EPI_LN_GELU) {
      if (tid < 128) {
        const f32x4* sp = (const f32x4*)(aux0 + (size_t)(m0 + tid) * 32);
        f32x4 p[8];
#pragma unroll
        for (int i = 0; i < 8; ++i) p[i] = sp[i];
        float mean = 0.f;
#pragma unroll
        for (int i = 0; i < 8; ++i) mean += p[i][0] + p[i][2];
        mean *= (1.0f / 16.0f);
        float m2 = 0.f;
#pragma unroll
        for (int i = 0; i < 8; ++i) {
          const float d0 = p[i][0] - mean, d1 = p[i][2] - mean;
          m2 += p[i][1] + p[i][3] + 64.0f * (d0 * d0 + d1 * d1);
        }
        const float var = m2 * (1.0f / DM);
        const float r1 = 1.0f / sqrtf(var + LN_EPS);
        const float r2 = 1.0f / sqrtf(var * r1 * r1 + LN_EPS);
        *(f32x2*)(smem + SIDE + tid * 8) = f32x2{mean, r1 * r2};
      } else if (tid - 128 < BN / 2) {
        const int c = (tid - 128) * 2;
        f32x2 cs = {0.f, 0.f};
#pragma unroll
        for (int kc = 0; kc < 16; ++kc) cs += *(const f32x2*)(aux1 + (size_t)kc * FF + n0 + c);
        const unsigned bb = *(const unsigned*)(bias + n0 + c);
        *(f32x4*)(smem + SIDE + 1024 + c * 8) = f32x4{cs[0], bflo(bb), cs[1], bfhi(bb)};
      }
    }
    const int q = (lane >> 2) & 3, pp = lane & 3;
    const int wk = WIDE ? 0 : wx;
    int aoff[2];
#pragma unroll
    for (int ks = 0; ks < 2; ++ks) aoff[ks] = (wm * 64 + fr) * 128 + (((ks * 4 + g) ^ (fr & 7)) << 4);
    int tb[NT];
    const int f1 = (q & 2) | ((g & 1) << 2);
#pragma unroll
    for (int j = 0; j < NT; ++j) {
      int G, tp;
      if (!WIDE) { G = 0; tp = j; }
      else if (EPI == EPI_QKV) { G = (j >> 1) ^ 2; G = G == 3 ? 1 : G; tp = wx * 2 + (j & 1); }
      else if (j < 4) { G = wx * (NG - 1); tp = j; }
      else { G = 1; tp = wx * 2 + (j - 4); }
      if (WIDE && HALF && j == NT - 1) {
        tb[j] = ABYTES + NG * 8192 + (8 * g + q) * 64 + (((2 * wx + (pp >> 1)) ^ ((g & 1) << 1)) << 4) + (pp & 1) * 8;
        tcol[j] = chalf + wx * 16;
      } else {
        tb[j] = ABYTES + G * 8192 + (8 * g + q) * 128 + (((2 * tp + (pp >> 1)) ^ f1) << 4) + (pp & 1) * 8;
        tcol[j] = cfull + G * 64 + tp * 16;
      }
    }
#pragma unroll
    for (int i = 0; i < 4; ++i)
#pragma unroll
      for (int j = 0; j < NT; ++j) acc[i][j] = f32x4{0.f, 0.f, 0.f, 0.f};

    bf16x8 xa[4], wa[NT], xb[4], wb[NT];
    auto rd = [&](bf16x8* xf, bf16x8* wf, int buf, int ks) {
      const char* base = smem + buf * STAGE;
#pragma unroll
      for (int j = 0; j < NT; ++j) {
        const int rs = (WIDE && HALF && j == NT - 1) ? 64 : 128;
        s16x4 lo = vtr((lds_cptr)(base + tb[j] + ks * 32 * rs));
        s16x4 hi = vtr((lds_cptr)(base + tb[j] + ks * 32 * rs + 4 * rs));
        wf[j] = __builtin_shufflevector(lo, hi, 0, 1, 2, 3, 4, 5, 6, 7);
      }
#pragma unroll
      for (int i = 0; i < 4; ++i) xf[i] = *(const bf16x8*)(base + aoff[ks] + i * 2048);
    };
    auto mm = [&](const bf16x8* xf, const bf16x8* wf) {
#pragma unroll
      for (int i = 0; i < 4; ++i)
#pragma unroll
        for (int j = 0; j < NT; ++j)
          acc[i][j] = __builtin_amdgcn_mfma_f32_16x16x32_bf16(wf[j], xf[i], acc[i][j], 0, 0, 0);
    };
#define INTERLEAVE() do { _Pragma("unroll") for (int z_ = 0; z_ < 2 * NT + 4; ++z_) { __builtin_amdgcn_sched_group_barrier(0x8, 1, 0); __builtin_amdgcn_sched_group_barrier(0x100, 1, 0); } __builtin_amdgcn_sched_group_barrier(0x8, 4 * NT - (2 * NT + 4), 0); } while (0)
    __builtin_amdgcn_s_waitcnt(0xC07F);
    __builtin_amdgcn_s_barrier();
    STAMP(EPI, 1);
    auto do_touch = [&](int i) {
      const int line = ((blockIdx.x * 4 + wid) * 2 + i) * 64 + lane;
      if (__builtin_amdgcn_readfirstlane(line) < TL0 + TL1) {
        const int lc = line < TL0 + TL1 ? line : TL0 + TL1 - 1;
        const char* p = lc < TL0 ? tp0 + (size_t)lc * 128 : tp1 + (size_t)(lc - TL0) * 128;
        unsigned sv;
        asm volatile("s_mov_b32 %0, m0\n\ts_mov_b32 m0, %2\n\ts_nop 0\n\tglobal_load_lds_dword %1, off\n\ts_mov_b32 m0, %0"
                     : "=&s"(sv) : "v"(p), "s"(lds0 + TSCR + wid * 256) : "memory");
      }
    };
    if (WIDE) {
      rd(xa, wa, 0, 0);
      int b0 = 0;
      for (int t = 0; t < nk; ++t) {
        if (TL0 + TL1 > 0) { if (t == 2) do_touch(0); if (t == 7) do_touch(1); }
        rd(xb, wb, b0, 1);
        b0 = (b0 + 1 == NBUF) ? 0 : b0 + 1;
        mm(xa, wa);
        INTERLEAVE();
        __builtin_amdgcn_sched_barrier(0);
        __builtin_amdgcn_s_waitcnt(0xC07F);
        __builtin_amdgcn_s_barrier();
        rd(xa, wa, b0, 0);
        mm(xb, wb);
        INTERLEAVE();
        __builtin_amdgcn_sched_barrier(0);
      }
    } else {
      rd(xa, wa, 0, wk);
      int b1 = 1;
      for (int t = 0; t < nk; t += 2) {
        __builtin_amdgcn_s_waitcnt(0xC07F);
        __builtin_amdgcn_s_barrier();
        rd(xb, wb, b1, wk);
        b1 = (b1 + 1 == NBUF) ? 0 : b1 + 1;
        mm(xa, wa);
        INTERLEAVE();
        __builtin_amdgcn_sched_barrier(0);
        __builtin_amdgcn_s_waitcnt(0xC07F);
        __builtin_amdgcn_s_barrier();
        rd(xa, wa, b1, wk);
        b1 = (b1 + 1 == NBUF) ? 0 : b1 + 1;
        mm(xb, wb);
        INTERLEAVE();
        __builtin_amdgcn_sched_barrier(0);
      }
    }
    __builtin_amdgcn_s_waitcnt(0xC07F);
    if (TL0 + TL1 > 0) asm volatile("s_waitcnt vmcnt(0)" ::: "memory");
    STAMP(EPI, 2);
    if (SPLIT) {
      const int tile = bm * 8 + bn;
      const auto rsrc = __builtin_amdgcn_make_buffer_rsrc(aux0, 0, 128 * 2 * 2 * 16384, 0x00020000);
      if (wm != hk) {
        const int slot = ((tile * 2 + hk) * 2 + wx);
#pragma unroll
        for (int i = 0; i < 4; ++i)
#pragma unroll
          for (int j = 0; j < NT; ++j)
            __builtin_amdgcn_raw_buffer_store_b128(__builtin_bit_cast(u32x4, acc[i][j]), rsrc,
                                                   slot * 16384 + (i * NT + j) * 1024 + lane * 16, 0, 16);
        asm volatile("s_waitcnt vmcnt(0)" ::: "memory");
        if (lane == 0) __hip_atomic_store(flags + slot * 32, 1u, __ATOMIC_RELAXED, __HIP_MEMORY_SCOPE_AGENT);
      } else {
        const int slot = ((tile * 2 + (hk ^ 1)) * 2 + wx);
        for (int spin = 0; spin < (1 << 24); ++spin) {
          if (__hip_atomic_load(flags + slot * 32, __ATOMIC_RELAXED, __HIP_MEMORY_SCOPE_AGENT) != 0u) break;
          __builtin_amdgcn_s_sleep(2);
        }
        asm volatile("" ::: "memory");
#pragma unroll
        for (int i = 0; i < 4; ++i) {
          u32x4 t[NT];
#pragma unroll
          for (int j = 0; j < NT; ++j)
            t[j] = __builtin_amdgcn_raw_buffer_load_b128(rsrc, slot * 16384 + (i * NT + j) * 1024 + lane * 16, 0, 16);
#pragma unroll
          for (int j = 0; j < NT; ++j) acc[i][j] += __builtin_bit_cast(f32x4, t[j]);
        }
      }
    }
  }
  __syncthreads();
  STAMP(EPI, 3);

  constexpr int CPR = BN / 8;
  if (EPI == EPI_QKV) {
    constexpr int KIMG = 0, VIMG = 49152, QIMG = 98304, RED = 114688, HIMG = 24576;
    u16* kvg = (u16*)aux0;
    u32x4 cw[8];
    const int t2 = tid - 256;
    const int cb = blockIdx.x < 224 ? blockIdx.x : 223;
    const int cg = cb % 14, kc = cb / 14;
    if (wid < 4) {
      auto put = [&](int j) {
        const int grp = tcol[j] >> 6, hd = (tcol[j] >> 5) & 1;
        const int dd = (tcol[j] & 31) + g * 4, chn = dd >> 3, hf = (dd >> 2) & 1;
        const int sw = (grp == 2) ? (((fr >> 2) & 1) << 1) : ((4 - ((fr >> 2) & 3)) & 3);
        const int base = (grp == 0) ? QIMG + hd * 8192 : ((grp == 1) ? KIMG : VIMG) + hd * HIMG + 256 * 64;
#pragma unroll
        for (int i = 0; i < 4; ++i) {
          u32x2 o;
          o[0] = pk2(acc[i][j][0], acc[i][j][1]);
          o[1] = pk2(acc[i][j][2], acc[i][j][3]);
          *(u32x2*)(smem + base + (wm * 64 + i * 16 + fr) * 64 + ((chn ^ sw) << 4) + hf * 8) = o;
        }
      };
#pragma unroll
      for (int j = 0; j < 4; ++j) put(j);
      if (bm < 15) {
#pragma unroll
        for (int kv = 0; kv < 2; ++kv)
#pragma unroll
          for (int pc = 0; pc < 4; ++pc) {
            const int row = wm * 64 + pc * 16 + (lane >> 2), slot = lane & 3;
            const int sw = kv ? (((row >> 2) & 1) << 1) : ((4 - ((row >> 2) & 3)) & 3);
            const u32x4 v = *(const u32x4*)(smem + (kv ? VIMG : KIMG) + wx * HIMG + (256 + row) * 64 + slot * 16);
            ST16(kvg + ((size_t)(kv * NH + bn * 2 + wx) * S_LEN + m0 + row) * 32 + (slot ^ sw) * 8, v);
          }
      }
      put(4);
      put(5);
      if (bm < 15) {
        asm volatile("s_waitcnt vmcnt(0)" ::: "memory");
        if (lane == 0)
          __hip_atomic_fetch_add(flags + (bm * 16 + bn) * 32, 1u, __ATOMIC_RELAXED, __HIP_MEMORY_SCOPE_AGENT);
      }
    } else {
      const u16* cp = res + (size_t)(kc * 64 + (t2 >> 5) * 8) * FF + cg * 256 + (t2 & 31) * 8;
#pragma unroll
      for (int i = 0; i < 8; ++i) cw[i] = *(const u32x4*)(cp + (size_t)i * FF);
      const int lw = wid - 4, pr = lw >> 1, kv = lw & 1;
      const int pb = bm - 2 + pr;
      const unsigned dst = lds0 + (kv ? VIMG : KIMG) + pr * 128 * 64;
      if (pb >= 0) {
        const unsigned* fp = flags + (pb * 16 + bn) * 32;
        for (int spin = 0; spin < (1 << 24); ++spin) {
          if (__hip_atomic_load(fp, __ATOMIC_RELAXED, __HIP_MEMORY_SCOPE_AGENT) >= 4u) break;
          __builtin_amdgcn_s_sleep(2);
        }
        asm volatile("" ::: "memory");
        const int kl = lane >> 2, slot = lane & 3;
        const int sw = kv ? (((kl >> 2) & 1) << 1) : ((4 - ((kl >> 2) & 3)) & 3);
        const unsigned vo = (unsigned)((kl * 32 + (slot ^ sw) * 8) * 2);
#pragma unroll
        for (int hd = 0; hd < 2; ++hd) {
          const u16* src = kvg + ((size_t)(kv * NH + bn * 2 + hd) * S_LEN + pb * 128) * 32;
#pragma unroll
          for (int pc = 0; pc < 8; ++pc) glds16_sc1(src + pc * 512, vo, dst + hd * HIMG + pc * 1024);
        }
      } else {
#pragma unroll
        for (int hd = 0; hd < 2; ++hd)
#pragma unroll
          for (int pc = 0; pc < 8; ++pc)
            *(u32x4*)(smem + (kv ? VIMG : KIMG) + pr * 128 * 64 + hd * HIMG + pc * 1024 + lane * 16) = u32x4{0u, 0u, 0u, 0u};
      }
      {
        float* red = (float*)(smem + RED);
        float sacc[8];
#pragma unroll
        for (int e = 0; e < 8; ++e) sacc[e] = 0.f;
#pragma unroll
        for (int i = 0; i < 8; ++i)
#pragma unroll
          for (int e = 0; e < 4; ++e) { sacc[2 * e] += bflo(cw[i][e]); sacc[2 * e + 1] += bfhi(cw[i][e]); }
        *(f32x4*)(red + (t2 >> 5) * 256 + (t2 & 31) * 8) = f32x4{sacc[0], sacc[1], sacc[2], sacc[3]};
        *(f32x4*)(red + (t2 >> 5) * 256 + (t2 & 31) * 8 + 4) = f32x4{sacc[4], sacc[5], sacc[6], sacc[7]};
      }
      asm volatile("s_waitcnt vmcnt(0)" ::: "memory");
    }
    STAMP(EPI, 5);
    __syncthreads();
    STAMP(EPI, 6);
    if (wid < 4) {
      const int li = (rr * 4 + wid) * 64 + lane;
      const char* p = tp1 + (size_t)(li >> 3) * 2048 + (xc >> 2) * 1024 + (li & 7) * 128;
      unsigned sv;
      asm volatile("s_mov_b32 %0, m0\n\ts_mov_b32 m0, %2\n\ts_nop 0\n\tglobal_load_lds_dword %1, off\n\ts_mov_b32 m0, %0"
                   : "=&s"(sv) : "v"(p), "s"(lds0 + TSCR + wid * 256) : "memory");
    }
    if (blockIdx.x < 224 && tid < 256) {
      const float* red = (const float*)(smem + RED);
      float t = 0.f;
#pragma unroll
      for (int i = 0; i < 8; ++i) t += red[i * 256 + tid];
      aux1[(size_t)kc * FF + cg * 256 + tid] = t;
    }
    const int qw = m0 + wid * 16;
    const int tmin = (qw < WIN) ? (WIN - qw) >> 4 : 0;
    const int qo = (wid * 16 + fr) * 64 + ((g ^ ((4 - ((fr >> 2) & 3)) & 3)) << 4);
    const bf16x8 qf0 = *(const bf16x8*)(smem + QIMG + qo), qf1 = *(const bf16x8*)(smem + QIMG + 8192 + qo);
    char* ost = smem + QIMG + wid * 1024;
#pragma unroll 1
    for (int hd = 0; hd < 2; ++hd) {
      const bf16x8 qf = hd ? qf1 : qf0;
      char* op = ost + hd * 8192 + fr * 64;
      if (bm < 2) attn_body<true>(smem + KIMG + hd * HIMG, smem + VIMG + hd * HIMG, qf, wid, lane, tmin, op);
      else attn_body<false>(smem + KIMG + hd * HIMG, smem + VIMG + hd * HIMG, qf, wid, lane, 0, op);
    }
#pragma unroll
    for (int i = 0; i < 2; ++i) {
      const int row = i * 8 + (lane >> 3), ch = lane & 7;
      const u32x4 v = *(const u32x4*)(ost + (ch >> 2) * 8192 + row * 64 + (ch & 3) * 16);
      ST16(C + (size_t)(qw + row) * DM + bn * 64 + ch * 8, v);
    }
    STAMP(EPI, 7);
  } else if (EPI == EPI_LN_GELU) {
    constexpr int RS = BN * 4 + 16;
    if (wid < 4) {
#pragma unroll
      for (int j = 0; j < NT; ++j)
#pragma unroll
        for (int i = 0; i < 4; ++i)
          *(f32x4*)(smem + (wm * 64 + i * 16 + fr) * RS + (tcol[j] + g * 4) * 4) = acc[i][j];
    }
    __syncthreads();
#pragma unroll
    for (int c = tid; c < BM * CPR; c += NTHR) {
      const int row = c / CPR, ch = c % CPR;
      const f32x4 a0 = *(const f32x4*)(smem + row * RS + ch * 32);
      const f32x4 a1 = *(const f32x4*)(smem + row * RS + ch * 32 + 16);
      const f32x2 rs = *(const f32x2*)(smem + SIDE + row * 8);
      float v[8];
#pragma unroll
      for (int r = 0; r < 4; ++r) {
        const f32x4 cb = *(const f32x4*)(smem + SIDE + 1024 + ch * 64 + r * 16);
        const float x0 = (r < 2) ? a0[2 * r] : a1[2 * r - 4];
        const float x1 = (r < 2) ? a0[2 * r + 1] : a1[2 * r - 3];
        v[2 * r] = (x0 - rs[0] * cb[0]) * rs[1] + cb[1];
        v[2 * r + 1] = (x1 - rs[0] * cb[2]) * rs[1] + cb[3];
      }
      u32x4 o;
#pragma unroll
      for (int r = 0; r < 4; ++r) {
        const f32x2 x = {v[2 * r], v[2 * r + 1]};
        const f32x2 x2 = x * x;
        const f32x2 w = x2 * f32x2{-0.10294324f, -0.10294324f} + f32x2{-2.302208198f, -2.302208198f};
        const f32x2 z = x * w;
        f32x2 e = {__builtin_amdgcn_exp2f(z[0]), __builtin_amdgcn_exp2f(z[1])};
        e = e + f32x2{1.0f, 1.0f};
        const f32x2 y = x * f32x2{__builtin_amdgcn_rcpf(e[0]), __builtin_amdgcn_rcpf(e[1])};
        o[r] = pk2(y[0], y[1]);
      }
      ST16(C + (size_t)(m0 + row) * N + n0 + ch * 8, o);
    }
  } else if (EPI == EPI_RES_STATS) {
    constexpr int RS = BN * 4 + 16;
    constexpr int PART = BM * RS;
    if (wid < 4) {
#pragma unroll
      for (int i = 0; i < 4; ++i)
#pragma unroll
        for (int j = 0; j < NT; ++j)
          *(f32x4*)(smem + wx * PART + (wm * 64 + i * 16 + fr) * RS + (j * 16 + g * 4) * 4) = acc[i][j];
    }
    __syncthreads();
#pragma unroll
    for (int c = tid; c < BM * CPR; c += NTHR) {
      const int row = c / CPR, ch = c % CPR;
      const f32x4 a0 = *(const f32x4*)(smem + row * RS + ch * 32);
      const f32x4 a1 = *(const f32x4*)(smem + row * RS + ch * 32 + 16);
      const f32x4 b0 = *(const f32x4*)(smem + PART + row * RS + ch * 32);
      const f32x4 b1 = *(const f32x4*)(smem + PART + row * RS + ch * 32 + 16);
      const u32x4 rv = *(const u32x4*)(res + (size_t)(m0 + row) * N + n0 + ch * 8);
      u32x4 o;
      o[0] = pk2(a0[0] + b0[0] + bflo(rv[0]), a0[1] + b0[1] + bfhi(rv[0]));
      o[1] = pk2(a0[2] + b0[2] + bflo(rv[1]), a0[3] + b0[3] + bfhi(rv[1]));
      o[2] = pk2(a1[0] + b1[0] + bflo(rv[2]), a1[1] + b1[1] + bfhi(rv[2]));
      o[3] = pk2(a1[2] + b1[2] + bflo(rv[3]), a1[3] + b1[3] + bfhi(rv[3]));
      ST16(C + (size_t)(m0 + row) * N + n0 + ch * 8, o);
      float s = 0.f;
#pragma unroll
      for (int r = 0; r < 4; ++r) s += bflo(o[r]) + bfhi(o[r]);
      const float mean = sum8(s) * (1.0f / 64.0f);
      float m2 = 0.f;
#pragma unroll
      for (int r = 0; r < 4; ++r) {
        const float d0 = bflo(o[r]) - mean, d1 = bfhi(o[r]) - mean;
        m2 += d0 * d0 + d1 * d1;
      }
      m2 = sum8(m2);
      if (ch == 0) *(f32x2*)(aux0 + ((size_t)(m0 + row) * 16 + bn) * 2) = f32x2{mean, m2};
    }
  } else {
    constexpr int RS = BN * 4 + 16;
    if (wid < 4 && wm == hk) {
#pragma unroll
      for (int i = 0; i < 4; ++i)
#pragma unroll
        for (int j = 0; j < NT; ++j)
          *(f32x4*)(smem + (i * 16 + fr) * RS + (tcol[j] + g * 4) * 4) = acc[i][j];
    }
    __syncthreads();
#pragma unroll
    for (int c = tid; c < 64 * CPR; c += NTHR) {
      const int row = c / CPR, ch = c % CPR;
      const f32x4 a0 = *(const f32x4*)(smem + row * RS + ch * 32);
      const f32x4 a1 = *(const f32x4*)(smem + row * RS + ch * 32 + 16);
      const u32x4 bb = *(const u32x4*)(bias + n0 + ch * 8);
      const size_t go = (size_t)(m0 + hk * 64 + row) * N + n0 + ch * 8;
      const u32x4 rv = *(const u32x4*)(res + go);
      u32x4 o;
      o[0] = pk2(a0[0] + bflo(bb[0]) + bflo(rv[0]), a0[1] + bfhi(bb[0]) + bfhi(rv[0]));
      o[1] = pk2(a0[2] + bflo(bb[1]) + bflo(rv[1]), a0[3] + bfhi(bb[1]) + bfhi(rv[1]));
      o[2] = pk2(a1[0] + bflo(bb[2]) + bflo(rv[2]), a1[1] + bfhi(bb[2]) + bfhi(rv[2]));
      o[3] = pk2(a1[2] + bflo(bb[3]) + bflo(rv[3]), a1[3] + bfhi(bb[3]) + bfhi(rv[3]));
      ST16(C + go, o);
    }
  }
  asm volatile("s_waitcnt vmcnt(0)" ::: "memory");
  STAMP(EPI, 4);
}

__global__ __launch_bounds__(512, 1) void qkv_attn_kernel(const u16* __restrict__ A, const u16* __restrict__ W,
                                                         u16* __restrict__ o, const char* __restrict__ wo,
                                                         const u16* __restrict__ w1, float* __restrict__ kvg,
                                                         float* __restrict__ cspart, unsigned* __restrict__ flags) {
  gemm_body<192, 3, EPI_QKV, 8, 57344, 0>((const char*)w1, wo, A, W, o, nullptr, w1, kvg, cspart, flags, 3 * DM, DM);
}
__global__ __launch_bounds__(512, 1) void gemm_wo_kernel(const u16* __restrict__ A, const u16* __restrict__ W,
                                                        u16* __restrict__ C, const u16* __restrict__ res,
                                                        float* __restrict__ stats) {
  gemm_body<64, 6, EPI_RES_STATS, 4>(nullptr, nullptr, A, W, C, nullptr, res, stats, nullptr, nullptr, DM, DM);
}
__global__ __launch_bounds__(512, 1) void gemm_w1_kernel(const u16* __restrict__ A, const u16* __restrict__ W,
                                                        u16* __restrict__ C, const u16* __restrict__ bias,
                                                        float* __restrict__ stats, float* __restrict__ cspart,
                                                        const char* __restrict__ t0) {
  gemm_body<224, 3, EPI_LN_GELU, 8, 57344, 0>(t0, t0, A, W, C, bias, nullptr, stats, cspart, nullptr, FF, DM);
}
__global__ __launch_bounds__(512, 1) void gemm_w2_kernel(const u16* __restrict__ A, const u16* __restrict__ W,
                                                        u16* __restrict__ C, const u16* __restrict__ bias,
                                                        const u16* __restrict__ res, float* __restrict__ xchg,
                                                        unsigned* __restrict__ flags) {
  gemm_body<128, 4, EPI_SPLIT_BIAS_RES, 4>(nullptr, nullptr, A, W, C, bias, res, xchg, nullptr, flags, DM, FF);
}
extern "C" __global__ void zz_text_end() {}

constexpr int lds_bytes(int BN, int NBUF, bool side) { return NBUF * (128 * 64 * 2 + 64 * BN * 2) + (side ? 5120 : 0); }

extern "C" void kernel_launch(void* const* d_in, const int* in_sizes, int n_in, void* d_out, int out_size,
                              void* d_ws, size_t ws_size, hipStream_t stream) {
  const u16* x = (const u16*)d_in[0];
  const u16* wqkv = (const u16*)d_in[1];
  const u16* wo = (const u16*)d_in[2];
  const u16* w1 = (const u16*)d_in[4];
  const u16* b1 = (const u16*)d_in[5];
  const u16* w2 = (const u16*)d_in[6];
  const u16* b2 = (const u16*)d_in[7];
  u16* out = (u16*)d_out;
  char* ws = (char*)d_ws;
  u16* xn = (u16*)(ws);
  float* xchg = (float*)(ws);
  u16* h = (u16*)(ws + (8u << 20));
  u16* qkv = (u16*)(ws + (12u << 20));
  u16* o = (u16*)(ws + (20u << 20));
  u16* hh = (u16*)(ws + (12u << 20));
  float* stats = (float*)(ws + (26u << 20));
  float* cspart = (float*)(ws + (26u << 20) + (256u << 10));
  unsigned* flags = (unsigned*)(ws + (26u << 20) + (512u << 10));

  (void)hipFuncSetAttribute((const void*)qkv_attn_kernel, hipFuncAttributeMaxDynamicSharedMemorySize, lds_bytes(192, 3, true));
  (void)hipFuncSetAttribute((const void*)gemm_wo_kernel, hipFuncAttributeMaxDynamicSharedMemorySize, lds_bytes(64, 6, false));
  (void)hipFuncSetAttribute((const void*)gemm_w1_kernel, hipFuncAttributeMaxDynamicSharedMemorySize, lds_bytes(224, 3, true));
  (void)hipFuncSetAttribute((const void*)gemm_w2_kernel, hipFuncAttributeMaxDynamicSharedMemorySize, lds_bytes(128, 4, false));
  prep_kernel<<<S_LEN / 4, 256, 0, stream>>>(x, xn, (const char*)wqkv, (const char*)b1, (const char*)b2, flags);
  qkv_attn_kernel<<<256, 512, lds_bytes(192, 3, true), stream>>>(xn, wqkv, o, (const char*)wo, w1, (float*)qkv, cspart, flags + 512 * 32);
  gemm_wo_kernel<<<256, 512, lds_bytes(64, 6, false), stream>>>(o, wo, h, x, stats);
  gemm_w1_kernel<<<256, 512, lds_bytes(224, 3, true), stream>>>(h, w1, hh, b1, stats, cspart, (const char*)w2);
  gemm_w2_kernel<<<256, 512, lds_bytes(128, 4, false), stream>>>(hh, w2, out, b2, h, xchg, flags);
}
```

```cpp
#include <hip/hip_runtime.h>
#include <hip/hip_bf16.h>
#include <stdint.h>

typedef unsigned short u16;
typedef __attribute__((ext_vector_type(8))) short bf16x8;
typedef __attribute__((ext_vector_type(4))) short s16x4;
typedef __attribute__((ext_vector_type(4))) float f32x4;
typedef __attribute__((ext_vector_type(2))) float f32x2;
typedef __attribute__((ext_vector_type(16))) float f32x16;
typedef __attribute__((ext_vector_type(4))) unsigned u32x4;
typedef __attribute__((ext_vector_type(2))) unsigned u32x2;
typedef __attribute__((ext_vector_type(2))) __bf16 bf16x2_t;
typedef __attribute__((address_space(3))) const char* lds_cptr;

#define STAMP(kid, i)
__device__ __forceinline__ void st16_sc1(void* p, __attribute__((ext_vector_type(4))) unsigned v) { asm volatile("global_store_dwordx4 %0, %1, off sc1\n\ts_nop 1" :: "v"(p), "v"(v) : "memory"); }
#define ST16(p, v) st16_sc1((void*)(p), (v))
constexpr int S_LEN = 2048, DM = 1024, NH = 32, DH = 32, WIN = 256, FF = 3584;
constexpr float LN_EPS = 1e-5f;

__device__ __forceinline__ float bf2f(u16 v) { return __uint_as_float(((unsigned)v) << 16); }
__device__ __forceinline__ float bflo(unsigned v) { return __uint_as_float(v << 16); }
__device__ __forceinline__ float bfhi(unsigned v) { return __uint_as_float(v & 0xffff0000u); }
__device__ __forceinline__ unsigned pk2(float lo, float hi) {
  return __builtin_bit_cast(unsigned, __builtin_convertvector(f32x2{lo, hi}, bf16x2_t));
}
__device__ __forceinline__ void glds16(const void* sbase, unsigned voff, unsigned lds_addr) {
  unsigned sv;
  asm volatile("s_mov_b32 %0, m0\n\ts_mov_b32 m0, %3\n\ts_nop 0\n\tglobal_load_lds_dwordx4 %1, %2\n\ts_mov_b32 m0, %0"
               : "=&s"(sv) : "v"(voff), "s"(sbase), "s"(lds_addr) : "memory");
}
__device__ __forceinline__ void glds16_sc1(const void* sbase, unsigned voff, unsigned lds_addr) {
  unsigned sv;
  asm volatile("s_mov_b32 %0, m0\n\ts_mov_b32 m0, %3\n\ts_nop 0\n\tglobal_load_lds_dwordx4 %1, %2 sc1\n\ts_mov_b32 m0, %0"
               : "=&s"(sv) : "v"(voff), "s"(sbase), "s"(lds_addr) : "memory");
}
__device__ __forceinline__ void touch_lds(const char* p, unsigned lds_scratch) {
  unsigned sv;
  asm volatile("s_mov_b32 %0, m0\n\ts_mov_b32 m0, %2\n\ts_nop 0\n\tglobal_load_lds_dword %1, off\n\ts_mov_b32 m0, %0"
               : "=&s"(sv) : "v"(p), "s"(lds_scratch) : "memory");
}
__device__ __forceinline__ s16x4 vtr(lds_cptr p) {
  return __builtin_bit_cast(s16x4, __builtin_amdgcn_ds_read_tr16_b64_v4i16((__attribute__((address_space(3))) s16x4*)p));
}
template <int CTRL>
__device__ __forceinline__ float dpp_add(float v) {
  return v + __int_as_float(__builtin_amdgcn_update_dpp(0, __float_as_int(v), CTRL, 0xf, 0xf, false));
}
__device__ __forceinline__ float sum8(float v) {
  v = dpp_add<0xB1>(v);
  v = dpp_add<0x4E>(v);
  return dpp_add<0x141>(v);
}
__device__ __forceinline__ float xsum16_32(float m) {
  auto a = __builtin_amdgcn_permlane32_swap(__float_as_uint(m), __float_as_uint(m), false, false);
  m = __uint_as_float(a[0]) + __uint_as_float(a[1]);
  auto b = __builtin_amdgcn_permlane16_swap(__float_as_uint(m), __float_as_uint(m), false, false);
  return __uint_as_float(b[0]) + __uint_as_float(b[1]);
}
__device__ __forceinline__ float xmax16_32(float m) {
  auto a = __builtin_amdgcn_permlane32_swap(__float_as_uint(m), __float_as_uint(m), false, false);
  m = fmaxf(__uint_as_float(a[0]), __uint_as_float(a[1]));
  auto b = __builtin_amdgcn_permlane16_swap(__float_as_uint(m), __float_as_uint(m), false, false);
  return fmaxf(__uint_as_float(b[0]), __uint_as_float(b[1]));
}
__device__ __forceinline__ float wave_sum(float v) {
  v = sum8(v);
  v = dpp_add<0x140>(v);
  return xsum16_32(v);
}

__global__ __launch_bounds__(256) void prep_kernel(const u16* __restrict__ in, u16* __restrict__ out,
                                                   const char* __restrict__ wqkv, const char* __restrict__ b1,
                                                   const char* __restrict__ b2, unsigned* __restrict__ flags) {
  const int lane = threadIdx.x & 63;
  const int row = blockIdx.x * 4 + (threadIdx.x >> 6);
  const u32x4* src = (const u32x4*)(in + (size_t)row * DM);
  u32x4 a = src[lane], b = src[lane + 64];
  const int gi = blockIdx.x * 256 + threadIdx.x;
  unsigned w0 = 0;
  if (gi < 49152) w0 = *(const unsigned*)(wqkv + (size_t)gi * 128);
  else if (gi < 49152 + 56) w0 = *(const unsigned*)(b1 + (size_t)(gi - 49152) * 128);
  else if (gi < 49152 + 72) w0 = *(const unsigned*)(b2 + (size_t)(gi - 49152 - 56) * 128);
  if (gi < 768) flags[gi * 32] = 0u;
  unsigned w1c = 0;
  if (threadIdx.x < 64 && blockIdx.x < 128) {
    unsigned long long pc, pe;
    asm volatile("s_getpc_b64 %0" : "=s"(pc));
    asm volatile("s_getpc_b64 s[40:41]\n\ts_add_u32 s40, s40, zz_text_end@rel32@lo+4\n\t"
                 "s_addc_u32 s41, s41, zz_text_end@rel32@hi+12\n\ts_mov_b64 %0, s[40:41]"
                 : "=s"(pe) : : "s40", "s41", "scc");
    const unsigned long long a = (pc & ~127ull) + (unsigned long long)((blockIdx.x >> 3) * 64 + lane) * 128;
    if (a + 4 <= pe) w1c = *(const unsigned*)a;
  }
  float v[16];
#pragma unroll
  for (int i = 0; i < 4; ++i) {
    v[2 * i] = bflo(a[i]);
    v[2 * i + 1] = bfhi(a[i]);
    v[8 + 2 * i] = bflo(b[i]);
    v[8 + 2 * i + 1] = bfhi(b[i]);
  }
  float s = 0.f;
#pragma unroll
  for (int i = 0; i < 16; ++i) s += v[i];
  const float mean = wave_sum(s) * (1.0f / DM);
  float q = 0.f;
#pragma unroll
  for (int i = 0; i < 16; ++i) { v[i] -= mean; q += v[i] * v[i]; }
  const float var = wave_sum(q) * (1.0f / DM);
  const float r = 1.0f / sqrtf(var + LN_EPS);
  u32x4 oa, ob;
#pragma unroll
  for (int i = 0; i < 4; ++i) {
    oa[i] = pk2(v[2 * i] * r, v[2 * i + 1] * r);
    ob[i] = pk2(v[8 + 2 * i] * r, v[8 + 2 * i + 1] * r);
  }
  u32x4* dst = (u32x4*)(out + (size_t)row * DM);
  dst[lane] = oa;
  dst[lane + 64] = ob;
  asm volatile("" ::"v"(w0), "v"(w1c));
}

__device__ __forceinline__ float rowmax17(const f32x4* s) {
  float a = fmaxf(fmaxf(s[0][0], s[0][1]), fmaxf(s[0][2], s[0][3]));
#pragma unroll
  for (int t = 1; t < 17; ++t) {
    a = fmaxf(fmaxf(a, s[t][0]), s[t][1]);
    a = fmaxf(fmaxf(a, s[t][2]), s[t][3]);
  }
  return a;
}

template <bool EARLY>
__device__ __forceinline__ void attn_body(const char* ks, const char* vs, bf16x8 qf, int wid, int lane, int tmin,
                                          char* op) {
  const int fr = lane & 15, g = lane >> 4;
  const float NEG = -1e30f;
  f32x4 s[17];
  const char* kp = ks + (wid * 16 + fr) * 64 + ((g ^ ((4 - ((fr >> 2) & 3)) & 3)) << 4);
#pragma unroll
  for (int t = 0; t < 17; ++t) {
    bf16x8 kf = *(const bf16x8*)(kp + t * 1024);
    s[t] = __builtin_amdgcn_mfma_f32_16x16x32_bf16(kf, qf, f32x4{0.f, 0.f, 0.f, 0.f}, 0, 0, 0);
  }
#pragma unroll
  for (int r = 0; r < 4; ++r) {
    s[0][r] = (4 * g + r > fr) ? s[0][r] : NEG;
    s[16][r] = (4 * g + r <= fr) ? s[16][r] : NEG;
  }
  if (EARLY) {
#pragma unroll
    for (int t = 0; t < 16; ++t)
      if (t < tmin) s[t] = f32x4{NEG, NEG, NEG, NEG};
  }
  const float mx = xmax16_32(rowmax17(s));
  const float c = 0.17677669529663687f * 1.4426950408889634f;
  const f32x2 c2 = {c, c};
  const f32x2 nmc2 = {-mx * c, -mx * c};
  f32x2 sum2 = {0.f, 0.f};
  unsigned pk[18][2];
#pragma unroll
  for (int t = 0; t < 17; ++t) {
#pragma unroll
    for (int hh = 0; hh < 2; ++hh) {
      const f32x2 z = f32x2{s[t][2 * hh], s[t][2 * hh + 1]} * c2 + nmc2;
      const f32x2 p = {__builtin_amdgcn_exp2f(z[0]), __builtin_amdgcn_exp2f(z[1])};
      sum2 += p;
      pk[t][hh] = pk2(p[0], p[1]);
    }
  }
  float sum = sum2[0] + sum2[1];
  pk[17][0] = 0u;
  pk[17][1] = 0u;
  sum = xsum16_32(sum);
  const int q4 = (lane >> 2) & 3, pp = lane & 3;
  const char* vp0 = vs + (wid * 16 + 4 * g + q4) * 64 + ((g & 1) << 5) + pp * 8;
  const char* vp1 = vs + (wid * 16 + 4 * g + q4) * 64 + (((g & 1) ^ 1) << 5) + pp * 8;
  f32x4 o0 = {0.f, 0.f, 0.f, 0.f}, o1 = {0.f, 0.f, 0.f, 0.f};
#pragma unroll
  for (int tp = 0; tp < 9; ++tp) {
    const int t0 = 2 * tp, t1 = (2 * tp + 1 < 17) ? 2 * tp + 1 : 16;
    u32x4 pb = {pk[2 * tp][0], pk[2 * tp][1], pk[2 * tp + 1][0], pk[2 * tp + 1][1]};
    bf16x8 pf = __builtin_bit_cast(bf16x8, pb);
    s16x4 a0 = vtr((lds_cptr)(vp0 + t0 * 1024)), a1 = vtr((lds_cptr)(vp0 + t1 * 1024));
    s16x4 b0 = vtr((lds_cptr)(vp1 + t0 * 1024)), b1 = vtr((lds_cptr)(vp1 + t1 * 1024));
    o0 = __builtin_amdgcn_mfma_f32_16x16x32_bf16(__builtin_shufflevector(a0, a1, 0, 1, 2, 3, 4, 5, 6, 7), pf, o0, 0, 0, 0);
    o1 = __builtin_amdgcn_mfma_f32_16x16x32_bf16(__builtin_shufflevector(b0, b1, 0, 1, 2, 3, 4, 5, 6, 7), pf, o1, 0, 0, 0);
  }
  const float inv = 1.0f / sum;
  u32x2 w0, w1;
  w0[0] = pk2(o0[0] * inv, o0[1] * inv);
  w0[1] = pk2(o0[2] * inv, o0[3] * inv);
  w1[0] = pk2(o1[0] * inv, o1[1] * inv);
  w1[1] = pk2(o1[2] * inv, o1[3] * inv);
  *(u32x2*)(op + 8 * g) = w0;
  *(u32x2*)(op + 32 + 8 * g) = w1;
}

enum { EPI_QKV = 0, EPI_RES_STATS = 1, EPI_LN_GELU = 2, EPI_SPLIT_BIAS_RES = 3 };
constexpr int NLW = 4;

template <int CNT, int NBUF, int NPA, int STAGE>
__device__ __forceinline__ void loader_loop(const char* Ab, const char* Wb, const unsigned* voff, int lw, int nk,
                                            unsigned wstep, unsigned lds0) {
  const char* sp[CNT];
  unsigned st[CNT];
#pragma unroll
  for (int i = 0; i < CNT; ++i) {
    const bool isA = (lw + i * NLW) < NPA;
    sp[i] = isA ? Ab : Wb;
    st[i] = isA ? 128u : wstep;
  }
  auto issue = [&](int buf) {
#pragma unroll
    for (int i = 0; i < CNT; ++i) {
      glds16(sp[i], voff[i], lds0 + buf * STAGE + (lw + i * NLW) * 1024);
      sp[i] += st[i];
    }
  };
  issue(0);
  issue(1);
  asm volatile("s_waitcnt vmcnt(%0)" ::"i"(CNT) : "memory");
  __builtin_amdgcn_s_barrier();
#pragma unroll
  for (int s = 2; s < NBUF; ++s) issue(s);
  int buf = 0;
  for (int t = 0; t < nk; ++t) {
    if (t + NBUF <= nk) {
      asm volatile("s_waitcnt vmcnt(%0)" ::"i"((NBUF - 2) * CNT) : "memory");
    } else {
      asm volatile("s_waitcnt vmcnt(0)" ::: "memory");
    }
    __builtin_amdgcn_s_barrier();
    if (t + NBUF < nk) issue(buf);
    buf = (buf + 1 == NBUF) ? 0 : buf + 1;
  }
}

template <int BN, int NBUF, int EPI, int XA, int TL0 = 0, int TL1 = 0>
__device__ __forceinline__ void gemm_body(const char* __restrict__ tp0, const char* __restrict__ tp1, const u16* __restrict__ A, const u16* __restrict__ W,
                                          u16* __restrict__ C, const u16* __restrict__ bias,
                                          const u16* __restrict__ res, float* __restrict__ aux0,
                                          float* __restrict__ aux1, unsigned* __restrict__ flags,
                                          const int N, const int K) {
  constexpr int BM = 128;
  constexpr bool WIDE = BN > 64;
  constexpr bool SPLIT = (EPI == EPI_SPLIT_BIAS_RES);
  constexpr int NTHR = 512;
  constexpr int NG = BN / 64;
  constexpr bool HALF = (BN % 64) != 0;
  constexpr int NT = WIDE ? BN / 32 : 4;
  constexpr bool M32 = !HALF;
  constexpr int NT32 = NT / 2;
  constexpr int ABYTES = BM * 128, WBYTES = 64 * BN * 2, STAGE = ABYTES + WBYTES;
  constexpr int NPA = ABYTES / 1024, NP = STAGE / 1024;
  constexpr int CMAX = (NP + NLW - 1) / NLW;
  constexpr int TSCR = NBUF * STAGE + 4096;
  constexpr int SIDE = NBUF * STAGE;
  extern __shared__ __attribute__((aligned(16))) char smem[];

  const int tid = threadIdx.x, lane = tid & 63;
  const int wid = __builtin_amdgcn_readfirstlane(tid >> 6);
  STAMP(EPI, 0);
  const int xc = blockIdx.x & 7, rr = blockIdx.x >> 3;
  int bm, bn, hk = 0;
  if (SPLIT) {
    hk = rr & 1;
    const int tt = rr >> 1;
    bm = (xc & 3) * 4 + (tt & 3);
    bn = (xc >> 2) * 4 + (tt >> 2);
  } else {
    constexpr int XB = 32 / XA, PM = 16 / XA;
    bm = (xc % PM) * XA + (rr % XA);
    bn = (xc / PM) * XB + (rr / XA);
  }
  const int m0 = bm * BM, n0 = bn * BN;
  const int nk = SPLIT ? K / 128 : K / 64;
  const int kbeg = hk * (K / 2);
  const int odd = HALF ? (bn & 1) : 0;
  const int cfull = odd ? 32 : 0, chalf = odd ? 0 : NG * 64;
  const unsigned lds0 = (unsigned)(uintptr_t)(__attribute__((address_space(3))) char*)smem;
  const int fr = lane & 15, g = lane >> 4;

  f32x4 acc[4][NT];
  const int wm = wid & 1, wx = (wid >> 1) & 1;
  int tcol[NT];
  auto qrow = [&](int a, int b) { return M32 ? (a >> 1) * 32 + (lane & 31) : a * 16 + fr; };
  auto qcol = [&](int a, int b) { return M32 ? tcol[b] + 8 * ((a & 1) * 2 + (b & 1)) + 4 * (lane >> 5) : tcol[b] + 4 * g; };

  if (wid >= 4) {
    const int lw = wid - 4;
    unsigned voff[CMAX];
#pragma unroll
    for (int i = 0; i < CMAX; ++i) {
      const int idx = lw + i * NLW;
      if (idx < NPA) {
        const int row = idx * 8 + (lane >> 3), ch = (lane & 7) ^ ((row >> 1) & 7);
        voff[i] = (unsigned)(((m0 + row) * K + ch * 8) * 2);
      } else if (idx < NPA + NG * 8) {
        const int w = idx - NPA, G = w >> 3, k = (w & 7) * 8 + (lane >> 3);
        const int ch = (lane & 7) ^ (((k & 2) << 1) | ((k >> 2) & 2));
        const int col = (EPI == EPI_QKV) ? G * DM + bn * 64 : n0 + cfull + G * 64;
        voff[i] = (unsigned)((k * N + col + ch * 8) * 2);
      } else {
        const int w = idx - NPA - NG * 8, k = w * 16 + (lane >> 2);
        const int ch = (lane & 3) ^ ((k >> 2) & 2);
        voff[i] = (unsigned)((k * N + n0 + chalf + ch * 8) * 2);
      }
    }
    const unsigned wstep = (unsigned)(64 * N * 2);
    const char* Ab = (const char*)(A + kbeg);
    const char* Wb = (const char*)(W + (size_t)kbeg * N);
    if (lw + (CMAX - 1) * NLW < NP)
      loader_loop<CMAX, NBUF, NPA, STAGE>(Ab, Wb, voff, lw, nk, wstep, lds0);
    else
      loader_loop<CMAX - 1, NBUF, NPA, STAGE>(Ab, Wb, voff, lw, nk, wstep, lds0);
  } else {
    if (EPI == EPI_LN_GELU) {
      if (tid < 128) {
        const f32x4* sp = (const f32x4*)(aux0 + (size_t)(m0 + tid) * 32);
        f32x4 p[8];
#pragma unroll
        for (int i = 0; i < 8; ++i) p[i] = sp[i];
        float mean = 0.f;
#pragma unroll
        for (int i = 0; i < 8; ++i) mean += p[i][0] + p[i][2];
        mean *= (1.0f / 16.0f);
        float m2 = 0.f;
#pragma unroll
        for (int i = 0; i < 8; ++i) {
          const float d0 = p[i][0] - mean, d1 = p[i][2] - mean;
          m2 += p[i][1] + p[i][3] + 64.0f * (d0 * d0 + d1 * d1);
        }
        const float var = m2 * (1.0f / DM);
        const float r1 = 1.0f / sqrtf(var + LN_EPS);
        const float r2 = 1.0f / sqrtf(var * r1 * r1 + LN_EPS);
        *(f32x2*)(smem + SIDE + tid * 8) = f32x2{mean, r1 * r2};
      } else if (tid - 128 < BN / 2) {
        const int c = (tid - 128) * 2;
        f32x2 cs = {0.f, 0.f};
#pragma unroll
        for (int kc = 0; kc < 16; ++kc) cs += *(const f32x2*)(aux1 + (size_t)kc * FF + n0 + c);
        const unsigned bb = *(const unsigned*)(bias + n0 + c);
        *(f32x4*)(smem + SIDE + 1024 + c * 8) = f32x4{cs[0], bflo(bb), cs[1], bfhi(bb)};
      }
    }
    const int q = (lane >> 2) & 3, pp = lane & 3;
    const int wk = WIDE ? 0 : wx;
    auto do_touch = [&](int i) {
      const int line = ((blockIdx.x * 4 + wid) * 2 + i) * 64 + lane;
      if (__builtin_amdgcn_readfirstlane(line) < TL0 + TL1) {
        const int lc = line < TL0 + TL1 ? line : TL0 + TL1 - 1;
        touch_lds(lc < TL0 ? tp0 + (size_t)lc * 128 : tp1 + (size_t)(lc - TL0) * 128, lds0 + TSCR + wid * 256);
      }
    };
    if constexpr (M32) {
      const int r32 = lane & 31, h = lane >> 5;
      int aoff[2][2];
#pragma unroll
      for (int ks = 0; ks < 2; ++ks)
#pragma unroll
        for (int st = 0; st < 2; ++st)
          aoff[ks][st] = (wm * 64 + r32) * 128 + (((ks * 4 + st * 2 + h) ^ ((r32 >> 1) & 7)) << 4);
      int tb[NT32];
      const int f1 = ((q & 2) << 1) | (h << 1);
#pragma unroll
      for (int j = 0; j < NT32; ++j) {
        int G, tq;
        if (!WIDE) { G = 0; tq = j; }
        else if (EPI == EPI_QKV) { G = 2 - j; tq = wx; }
        else { G = wx; tq = j; }
        tb[j] = ABYTES + G * 8192 + (8 * h + q) * 128 + (((4 * tq + 2 * (g & 1) + (pp >> 1)) ^ f1) << 4) + (pp & 1) * 8;
        tcol[2 * j] = tcol[2 * j + 1] = G * 64 + tq * 32;
      }
      f32x16 c32[2][NT32];
#pragma unroll
      for (int i = 0; i < 2; ++i)
#pragma unroll
        for (int j = 0; j < NT32; ++j) c32[i][j] = f32x16{};
      bf16x8 xa[2][2], wa[2][NT32], xb[2][2], wb[2][NT32];
      auto rd = [&](bf16x8 (*xf)[2], bf16x8 (*wf)[NT32], int buf, int ks) {
        const char* base = smem + buf * STAGE;
#pragma unroll
        for (int st = 0; st < 2; ++st) {
#pragma unroll
          for (int j = 0; j < NT32; ++j) {
            s16x4 lo = vtr((lds_cptr)(base + tb[j] + ks * 4096 + st * 2048));
            s16x4 hi = vtr((lds_cptr)(base + tb[j] + ks * 4096 + st * 2048 + 512));
            wf[st][j] = __builtin_shufflevector(lo, hi, 0, 1, 2, 3, 4, 5, 6, 7);
          }
#pragma unroll
          for (int i = 0; i < 2; ++i) xf[st][i] = *(const bf16x8*)(base + aoff[ks][st] + i * 4096);
        }
      };
      auto mm = [&](bf16x8 (*xf)[2], bf16x8 (*wf)[NT32]) {
#pragma unroll
        for (int st = 0; st < 2; ++st)
#pragma unroll
          for (int i = 0; i < 2; ++i)
#pragma unroll
            for (int j = 0; j < NT32; ++j)
              c32[i][j] = __builtin_amdgcn_mfma_f32_32x32x16_bf16(wf[st][j], xf[st][i], c32[i][j], 0, 0, 0);
      };
#define INTERLEAVE32() do { _Pragma("unroll") for (int z_ = 0; z_ < 4 * NT32; ++z_) { __builtin_amdgcn_sched_group_barrier(0x8, 1, 0); __builtin_amdgcn_sched_group_barrier(0x100, 2, 0); } } while (0)
      __builtin_amdgcn_s_waitcnt(0xC07F);
      __builtin_amdgcn_s_barrier();
      STAMP(EPI, 1);
      if (WIDE) {
        rd(xa, wa, 0, 0);
        int b0 = 0;
        for (int t = 0; t < nk; ++t) {
          if (TL0 + TL1 > 0) { if (t == 2) do_touch(0); if (t == 7) do_touch(1); }
          rd(xb, wb, b0, 1);
          b0 = (b0 + 1 == NBUF) ? 0 : b0 + 1;
          mm(xa, wa);
          INTERLEAVE32();
          __builtin_amdgcn_sched_barrier(0);
          __builtin_amdgcn_s_waitcnt(0xC07F);
          __builtin_amdgcn_s_barrier();
          rd(xa, wa, b0, 0);
          mm(xb, wb);
          INTERLEAVE32();
          __builtin_amdgcn_sched_barrier(0);
        }
      } else {
        rd(xa, wa, 0, wk);
        int b1 = 1;
        for (int t = 0; t < nk; t += 2) {
          __builtin_amdgcn_s_waitcnt(0xC07F);
          __builtin_amdgcn_s_barrier();
          rd(xb, wb, b1, wk);
          b1 = (b1 + 1 == NBUF) ? 0 : b1 + 1;
          mm(xa, wa);
          INTERLEAVE32();
          __builtin_amdgcn_sched_barrier(0);
          __builtin_amdgcn_s_waitcnt(0xC07F);
          __builtin_amdgcn_s_barrier();
          rd(xa, wa, b1, wk);
          b1 = (b1 + 1 == NBUF) ? 0 : b1 + 1;
          mm(xb, wb);
          INTERLEAVE32();
          __builtin_amdgcn_sched_barrier(0);
        }
      }
#pragma unroll
      for (int a2 = 0; a2 < 4; ++a2)
#pragma unroll
        for (int b2 = 0; b2 < NT; ++b2) {
          const int qd = (a2 & 1) * 2 + (b2 & 1);
#pragma unroll
          for (int r = 0; r < 4; ++r) acc[a2][b2][r] = c32[a2 >> 1][b2 >> 1][qd * 4 + r];
        }
    } else {
      int aoff[2];
#pragma unroll
      for (int ks = 0; ks < 2; ++ks) aoff[ks] = (wm * 64 + fr) * 128 + (((ks * 4 + g) ^ ((fr >> 1) & 7)) << 4);
      int tb[NT];
      const int f1 = ((q & 2) << 1) | ((g & 1) << 1);
#pragma unroll
      for (int j = 0; j < NT; ++j) {
        int G, tp;
        if (j < 4) { G = wx * (NG - 1); tp = j; }
        else { G = 1; tp = wx * 2 + (j - 4); }
        if (HALF && j == NT - 1) {
          tb[j] = ABYTES + NG * 8192 + (8 * g + q) * 64 + (((2 * wx + (pp >> 1)) ^ ((g & 1) << 1)) << 4) + (pp & 1) * 8;
          tcol[j] = chalf + wx * 16;
        } else {
          tb[j] = ABYTES + G * 8192 + (8 * g + q) * 128 + (((2 * tp + (pp >> 1)) ^ f1) << 4) + (pp & 1) * 8;
          tcol[j] = cfull + G * 64 + tp * 16;
        }
      }
#pragma unroll
      for (int i = 0; i < 4; ++i)
#pragma unroll
        for (int j = 0; j < NT; ++j) acc[i][j] = f32x4{0.f, 0.f, 0.f, 0.f};
      bf16x8 xa[4], wa[NT], xb[4], wb[NT];
      auto rd = [&](bf16x8* xf, bf16x8* wf, int buf, int ks) {
        const char* base = smem + buf * STAGE;
#pragma unroll
        for (int j = 0; j < NT; ++j) {
          const int rs = (HALF && j == NT - 1) ? 64 : 128;
          s16x4 lo = vtr((lds_cptr)(base + tb[j] + ks * 32 * rs));
          s16x4 hi = vtr((lds_cptr)(base + tb[j] + ks * 32 * rs + 4 * rs));
          wf[j] = __builtin_shufflevector(lo, hi, 0, 1, 2, 3, 4, 5, 6, 7);
        }
#pragma unroll
        for (int i = 0; i < 4; ++i) xf[i] = *(const bf16x8*)(base + aoff[ks] + i * 2048);
      };
      auto mm = [&](const bf16x8* xf, const bf16x8* wf) {
#pragma unroll
        for (int i = 0; i < 4; ++i)
#pragma unroll
          for (int j = 0; j < NT; ++j)
            acc[i][j] = __builtin_amdgcn_mfma_f32_16x16x32_bf16(wf[j], xf[i], acc[i][j], 0, 0, 0);
      };
#define INTERLEAVE() do { _Pragma("unroll") for (int z_ = 0; z_ < 2 * NT + 4; ++z_) { __builtin_amdgcn_sched_group_barrier(0x8, 1, 0); __builtin_amdgcn_sched_group_barrier(0x100, 1, 0); } __builtin_amdgcn_sched_group_barrier(0x8, 4 * NT - (2 * NT + 4), 0); } while (0)
      __builtin_amdgcn_s_waitcnt(0xC07F);
      __builtin_amdgcn_s_barrier();
      STAMP(EPI, 1);
      rd(xa, wa, 0, 0);
      int b0 = 0;
      for (int t = 0; t < nk; ++t) {
        if (TL0 + TL1 > 0) { if (t == 2) do_touch(0); if (t == 7) do_touch(1); }
        rd(xb, wb, b0, 1);
        b0 = (b0 + 1 == NBUF) ? 0 : b0 + 1;
        mm(xa, wa);
        INTERLEAVE();
        __builtin_amdgcn_sched_barrier(0);
        __builtin_amdgcn_s_waitcnt(0xC07F);
        __builtin_amdgcn_s_barrier();
        rd(xa, wa, b0, 0);
        mm(xb, wb);
        INTERLEAVE();
        __builtin_amdgcn_sched_barrier(0);
      }
    }
    __builtin_amdgcn_s_waitcnt(0xC07F);
    if (TL0 + TL1 > 0) asm volatile("s_waitcnt vmcnt(0)" ::: "memory");
    STAMP(EPI, 2);
    if (SPLIT) {
      const int tile = bm * 8 + bn;
      const auto rsrc = __builtin_amdgcn_make_buffer_rsrc(aux0, 0, 128 * 2 * 2 * 16384, 0x00020000);
      if (wm != hk) {
        const int slot = ((tile * 2 + hk) * 2 + wx);
#pragma unroll
        for (int i = 0; i < 4; ++i)
#pragma unroll
          for (int j = 0; j < NT; ++j)
            __builtin_amdgcn_raw_buffer_store_b128(__builtin_bit_cast(u32x4, acc[i][j]), rsrc,
                                                   slot * 16384 + (i * NT + j) * 1024 + lane * 16, 0, 16);
        asm volatile("s_waitcnt vmcnt(0)" ::: "memory");
        if (lane == 0) __hip_atomic_store(flags + slot * 32, 1u, __ATOMIC_RELAXED, __HIP_MEMORY_SCOPE_AGENT);
      } else {
        const int slot = ((tile * 2 + (hk ^ 1)) * 2 + wx);
        for (int spin = 0; spin < (1 << 24); ++spin) {
          if (__hip_atomic_load(flags + slot * 32, __ATOMIC_RELAXED, __HIP_MEMORY_SCOPE_AGENT) != 0u) break;
          __builtin_amdgcn_s_sleep(2);
        }
        asm volatile("" ::: "memory");
#pragma unroll
        for (int i = 0; i < 4; ++i) {
          u32x4 t[NT];
#pragma unroll
          for (int j = 0; j < NT; ++j)
            t[j] = __builtin_amdgcn_raw_buffer_load_b128(rsrc, slot * 16384 + (i * NT + j) * 1024 + lane * 16, 0, 16);
#pragma unroll
          for (int j = 0; j < NT; ++j) acc[i][j] += __builtin_bit_cast(f32x4, t[j]);
        }
      }
    }
  }
  __syncthreads();
  STAMP(EPI, 3);

  constexpr int CPR = BN / 8;
  if (EPI == EPI_QKV) {
    constexpr int KIMG = 0, VIMG = 49152, QIMG = 98304, RED = 114688, HIMG = 24576;
    u16* kvg = (u16*)aux0;
    u32x4 cw[8];
    const int t2 = tid - 256;
    const int cb = blockIdx.x < 224 ? blockIdx.x : 223;
    const int cg = cb % 14, kc = cb / 14;
    if (wid < 4) {
      auto put = [&](int j) {
        const int grp = tcol[j] >> 6, hd = (tcol[j] >> 5) & 1;
        const int base = (grp == 0) ? QIMG + hd * 8192 : ((grp == 1) ? KIMG : VIMG) + hd * HIMG + 256 * 64;
#pragma unroll
        for (int i = 0; i < 4; ++i) {
          const int row = wm * 64 + qrow(i, j);
          const int dd = qcol(i, j) & 31, chn = dd >> 3, hf = (dd >> 2) & 1;
          const int sw = (grp == 2) ? (((row >> 2) & 1) << 1) : ((4 - ((row >> 2) & 3)) & 3);
          u32x2 o;
          o[0] = pk2(acc[i][j][0], acc[i][j][1]);
          o[1] = pk2(acc[i][j][2], acc[i][j][3]);
          *(u32x2*)(smem + base + row * 64 + ((chn ^ sw) << 4) + hf * 8) = o;
        }
      };
#pragma unroll
      for (int j = 0; j < 4; ++j) put(j);
      if (bm < 15) {
#pragma unroll
        for (int kv = 0; kv < 2; ++kv)
#pragma unroll
          for (int pc = 0; pc < 4; ++pc) {
            const int row = wm * 64 + pc * 16 + (lane >> 2), slot = lane & 3;
            const int sw = kv ? (((row >> 2) & 1) << 1) : ((4 - ((row >> 2) & 3)) & 3);
            const u32x4 v = *(const u32x4*)(smem + (kv ? VIMG : KIMG) + wx * HIMG + (256 + row) * 64 + slot * 16);
            ST16(kvg + ((size_t)(kv * NH + bn * 2 + wx) * S_LEN + m0 + row) * 32 + (slot ^ sw) * 8, v);
          }
      }
      put(4);
      put(5);
      if (bm < 15) {
        asm volatile("s_waitcnt vmcnt(0)" ::: "memory");
        if (lane == 0)
          __hip_atomic_fetch_add(flags + (bm * 16 + bn) * 32, 1u, __ATOMIC_RELAXED, __HIP_MEMORY_SCOPE_AGENT);
      }
    } else {
      const u16* cp = res + (size_t)(kc * 64 + (t2 >> 5) * 8) * FF + cg * 256 + (t2 & 31) * 8;
#pragma unroll
      for (int i = 0; i < 8; ++i) cw[i] = *(const u32x4*)(cp + (size_t)i * FF);
      const int lw = wid - 4, pr = lw >> 1, kv = lw & 1;
      const int pb = bm - 2 + pr;
      const unsigned dst = lds0 + (kv ? VIMG : KIMG) + pr * 128 * 64;
      if (pb >= 0) {
        const unsigned* fp = flags + (pb * 16 + bn) * 32;
        for (int spin = 0; spin < (1 << 24); ++spin) {
          if (__hip_atomic_load(fp, __ATOMIC_RELAXED, __HIP_MEMORY_SCOPE_AGENT) >= 4u) break;
          __builtin_amdgcn_s_sleep(2);
        }
        asm volatile("" ::: "memory");
        const int kl = lane >> 2, slot = lane & 3;
        const int sw = kv ? (((kl >> 2) & 1) << 1) : ((4 - ((kl >> 2) & 3)) & 3);
        const unsigned vo = (unsigned)((kl * 32 + (slot ^ sw) * 8) * 2);
#pragma unroll
        for (int hd = 0; hd < 2; ++hd) {
          const u16* src = kvg + ((size_t)(kv * NH + bn * 2 + hd) * S_LEN + pb * 128) * 32;
#pragma unroll
          for (int pc = 0; pc < 8; ++pc) glds16_sc1(src + pc * 512, vo, dst + hd * HIMG + pc * 1024);
        }
      } else {
#pragma unroll
        for (int hd = 0; hd < 2; ++hd)
#pragma unroll
          for (int pc = 0; pc < 8; ++pc)
            *(u32x4*)(smem + (kv ? VIMG : KIMG) + pr * 128 * 64 + hd * HIMG + pc * 1024 + lane * 16) = u32x4{0u, 0u, 0u, 0u};
      }
      {
        float* red = (float*)(smem + RED);
        float sacc[8];
#pragma unroll
        for (int e = 0; e < 8; ++e) sacc[e] = 0.f;
#pragma unroll
        for (int i = 0; i < 8; ++i)
#pragma unroll
          for (int e = 0; e < 4; ++e) { sacc[2 * e] += bflo(cw[i][e]); sacc[2 * e + 1] += bfhi(cw[i][e]); }
        *(f32x4*)(red + (t2 >> 5) * 256 + (t2 & 31) * 8) = f32x4{sacc[0], sacc[1], sacc[2], sacc[3]};
        *(f32x4*)(red + (t2 >> 5) * 256 + (t2 & 31) * 8 + 4) = f32x4{sacc[4], sacc[5], sacc[6], sacc[7]};
      }
      asm volatile("s_waitcnt vmcnt(0)" ::: "memory");
    }
    STAMP(EPI, 5);
    __syncthreads();
    STAMP(EPI, 6);
    if (wid < 4) {
      const int li = (rr * 4 + wid) * 64 + lane;
      const char* p = tp1 + (size_t)(li >> 3) * 2048 + (xc >> 2) * 1024 + (li & 7) * 128;
      unsigned sv;
      asm volatile("s_mov_b32 %0, m0\n\ts_mov_b32 m0, %2\n\ts_nop 0\n\tglobal_load_lds_dword %1, off\n\ts_mov_b32 m0, %0"
                   : "=&s"(sv) : "v"(p), "s"(lds0 + TSCR + wid * 256) : "memory");
    }
    if (blockIdx.x < 224 && tid < 256) {
      const float* red = (const float*)(smem + RED);
      float t = 0.f;
#pragma unroll
      for (int i = 0; i < 8; ++i) t += red[i * 256 + tid];
      aux1[(size_t)kc * FF + cg * 256 + tid] = t;
    }
    const int qw = m0 + wid * 16;
    const int tmin = (qw < WIN) ? (WIN - qw) >> 4 : 0;
    const int qo = (wid * 16 + fr) * 64 + ((g ^ ((4 - ((fr >> 2) & 3)) & 3)) << 4);
    const bf16x8 qf0 = *(const bf16x8*)(smem + QIMG + qo), qf1 = *(const bf16x8*)(smem + QIMG + 8192 + qo);
    char* ost = smem + QIMG + wid * 1024;
#pragma unroll 1
    for (int hd = 0; hd < 2; ++hd) {
      const bf16x8 qf = hd ? qf1 : qf0;
      char* op = ost + hd * 8192 + fr * 64;
      if (bm < 2) attn_body<true>(smem + KIMG + hd * HIMG, smem + VIMG + hd * HIMG, qf, wid, lane, tmin, op);
      else attn_body<false>(smem + KIMG + hd * HIMG, smem + VIMG + hd * HIMG, qf, wid, lane, 0, op);
    }
#pragma unroll
    for (int i = 0; i < 2; ++i) {
      const int row = i * 8 + (lane >> 3), ch = lane & 7;
      const u32x4 v = *(const u32x4*)(ost + (ch >> 2) * 8192 + row * 64 + (ch & 3) * 16);
      ST16(C + (size_t)(qw + row) * DM + bn * 64 + ch * 8, v);
    }
    STAMP(EPI, 7);
  } else if (EPI == EPI_LN_GELU) {
    constexpr int RS = BN * 4 + 16;
    if (wid < 4) {
#pragma unroll
      for (int j = 0; j < NT; ++j)
#pragma unroll
        for (int i = 0; i < 4; ++i)
          *(f32x4*)(smem + (wm * 64 + qrow(i, j)) * RS + qcol(i, j) * 4) = acc[i][j];
    }
    __syncthreads();
#pragma unroll
    for (int c = tid; c < BM * CPR; c += NTHR) {
      const int row = c / CPR, ch = c % CPR;
      const f32x4 a0 = *(const f32x4*)(smem + row * RS + ch * 32);
      const f32x4 a1 = *(const f32x4*)(smem + row * RS + ch * 32 + 16);
      const f32x2 rs = *(const f32x2*)(smem + SIDE + row * 8);
      float v[8];
#pragma unroll
      for (int r = 0; r < 4; ++r) {
        const f32x4 cb = *(const f32x4*)(smem + SIDE + 1024 + ch * 64 + r * 16);
        const float x0 = (r < 2) ? a0[2 * r] : a1[2 * r - 4];
        const float x1 = (r < 2) ? a0[2 * r + 1] : a1[2 * r - 3];
        v[2 * r] = (x0 - rs[0] * cb[0]) * rs[1] + cb[1];
        v[2 * r + 1] = (x1 - rs[0] * cb[2]) * rs[1] + cb[3];
      }
      u32x4 o;
#pragma unroll
      for (int r = 0; r < 4; ++r) {
        const f32x2 x = {v[2 * r], v[2 * r + 1]};
        const f32x2 x2 = x * x;
        const f32x2 w = x2 * f32x2{-0.10294324f, -0.10294324f} + f32x2{-2.302208198f, -2.302208198f};
        const f32x2 z = x * w;
        f32x2 e = {__builtin_amdgcn_exp2f(z[0]), __builtin_amdgcn_exp2f(z[1])};
        e = e + f32x2{1.0f, 1.0f};
        const f32x2 y = x * f32x2{__builtin_amdgcn_rcpf(e[0]), __builtin_amdgcn_rcpf(e[1])};
        o[r] = pk2(y[0], y[1]);
      }
      ST16(C + (size_t)(m0 + row) * N + n0 + ch * 8, o);
    }
  } else if (EPI == EPI_RES_STATS) {
    constexpr int RS = BN * 4 + 16;
    constexpr int PART = BM * RS;
    if (wid < 4) {
#pragma unroll
      for (int i = 0; i < 4; ++i)
#pragma unroll
        for (int j = 0; j < NT; ++j)
          *(f32x4*)(smem + wx * PART + (wm * 64 + qrow(i, j)) * RS + qcol(i, j) * 4) = acc[i][j];
    }
    __syncthreads();
#pragma unroll
    for (int c = tid; c < BM * CPR; c += NTHR) {
      const int row = c / CPR, ch = c % CPR;
      const f32x4 a0 = *(const f32x4*)(smem + row * RS + ch * 32);
      const f32x4 a1 = *(const f32x4*)(smem + row * RS + ch * 32 + 16);
      const f32x4 b0 = *(const f32x4*)(smem + PART + row * RS + ch * 32);
      const f32x4 b1 = *(const f32x4*)(smem + PART + row * RS + ch * 32 + 16);
      const u32x4 rv = *(const u32x4*)(res + (size_t)(m0 + row) * N + n0 + ch * 8);
      u32x4 o;
      o[0] = pk2(a0[0] + b0[0] + bflo(rv[0]), a0[1] + b0[1] + bfhi(rv[0]));
      o[1] = pk2(a0[2] + b0[2] + bflo(rv[1]), a0[3] + b0[3] + bfhi(rv[1]));
      o[2] = pk2(a1[0] + b1[0] + bflo(rv[2]), a1[1] + b1[1] + bfhi(rv[2]));
      o[3] = pk2(a1[2] + b1[2] + bflo(rv[3]), a1[3] + b1[3] + bfhi(rv[3]));
      ST16(C + (size_t)(m0 + row) * N + n0 + ch * 8, o);
      float s = 0.f;
#pragma unroll
      for (int r = 0; r < 4; ++r) s += bflo(o[r]) + bfhi(o[r]);
      const float mean = sum8(s) * (1.0f / 64.0f);
      float m2 = 0.f;
#pragma unroll
      for (int r = 0; r < 4; ++r) {
        const float d0 = bflo(o[r]) - mean, d1 = bfhi(o[r]) - mean;
        m2 += d0 * d0 + d1 * d1;
      }
      m2 = sum8(m2);
      if (ch == 0) *(f32x2*)(aux0 + ((size_t)(m0 + row) * 16 + bn) * 2) = f32x2{mean, m2};
    }
  } else {
    constexpr int RS = BN * 4 + 16;
    if (wid < 4 && wm == hk) {
#pragma unroll
      for (int i = 0; i < 4; ++i)
#pragma unroll
        for (int j = 0; j < NT; ++j)
          *(f32x4*)(smem + qrow(i, j) * RS + qcol(i, j) * 4) = acc[i][j];
    }
    __syncthreads();
#pragma unroll
    for (int c = tid; c < 64 * CPR; c += NTHR) {
      const int row = c / CPR, ch = c % CPR;
      const f32x4 a0 = *(const f32x4*)(smem + row * RS + ch * 32);
      const f32x4 a1 = *(const f32x4*)(smem + row * RS + ch * 32 + 16);
      const u32x4 bb = *(const u32x4*)(bias + n0 + ch * 8);
      const size_t go = (size_t)(m0 + hk * 64 + row) * N + n0 + ch * 8;
      const u32x4 rv = *(const u32x4*)(res + go);
      u32x4 o;
      o[0] = pk2(a0[0] + bflo(bb[0]) + bflo(rv[0]), a0[1] + bfhi(bb[0]) + bfhi(rv[0]));
      o[1] = pk2(a0[2] + bflo(bb[1]) + bflo(rv[1]), a0[3] + bfhi(bb[1]) + bfhi(rv[1]));
      o[2] = pk2(a1[0] + bflo(bb[2]) + bflo(rv[2]), a1[1] + bfhi(bb[2]) + bfhi(rv[2]));
      o[3] = pk2(a1[2] + bflo(bb[3]) + bflo(rv[3]), a1[3] + bfhi(bb[3]) + bfhi(rv[3]));
      ST16(C + go, o);
    }
  }
  asm volatile("s_waitcnt vmcnt(0)" ::: "memory");
  STAMP(EPI, 4);
}

__global__ __launch_bounds__(512, 1) void qkv_attn_kernel(const u16* __restrict__ A, const u16* __restrict__ W,
                                                         u16* __restrict__ o, const char* __restrict__ wo,
                                                         const u16* __restrict__ w1, float* __restrict__ kvg,
                                                         float* __restrict__ cspart, unsigned* __restrict__ flags) {
  gemm_body<192, 3, EPI_QKV, 8, 57344, 0>((const char*)w1, wo, A, W, o, nullptr, w1, kvg, cspart, flags, 3 * DM, DM);
}
__global__ __launch_bounds__(512, 1) void gemm_wo_kernel(const u16* __restrict__ A, const u16* __restrict__ W,
                                                        u16* __restrict__ C, const u16* __restrict__ res,
                                                        float* __restrict__ stats) {
  gemm_body<64, 6, EPI_RES_STATS, 4>(nullptr, nullptr, A, W, C, nullptr, res, stats, nullptr, nullptr, DM, DM);
}
__global__ __launch_bounds__(512, 1) void gemm_w1_kernel(const u16* __restrict__ A, const u16* __restrict__ W,
                                                        u16* __restrict__ C, const u16* __restrict__ bias,
                                                        float* __restrict__ stats, float* __restrict__ cspart,
                                                        const char* __restrict__ t0) {
  gemm_body<224, 3, EPI_LN_GELU, 8, 57344, 0>(t0, t0, A, W, C, bias, nullptr, stats, cspart, nullptr, FF, DM);
}
__global__ __launch_bounds__(512, 1) void gemm_w2_kernel(const u16* __restrict__ A, const u16* __restrict__ W,
                                                        u16* __restrict__ C, const u16* __restrict__ bias,
                                                        const u16* __restrict__ res, float* __restrict__ xchg,
                                                        unsigned* __restrict__ flags) {
  gemm_body<128, 4, EPI_SPLIT_BIAS_RES, 4>(nullptr, nullptr, A, W, C, bias, res, xchg, nullptr, flags, DM, FF);
}
extern "C" __global__ void zz_text_end() {}

constexpr int lds_bytes(int BN, int NBUF, bool side) { return NBUF * (128 * 64 * 2 + 64 * BN * 2) + (side ? 5120 : 0); }

extern "C" void kernel_launch(void* const* d_in, const int* in_sizes, int n_in, void* d_out, int out_size,
                              void* d_ws, size_t ws_size, hipStream_t stream) {
  const u16* x = (const u16*)d_in[0];
  const u16* wqkv = (const u16*)d_in[1];
  const u16* wo = (const u16*)d_in[2];
  const u16* w1 = (const u16*)d_in[4];
  const u16* b1 = (const u16*)d_in[5];
  const u16* w2 = (const u16*)d_in[6];
  const u16* b2 = (const u16*)d_in[7];
  u16* out = (u16*)d_out;
  char* ws = (char*)d_ws;
  u16* xn = (u16*)(ws);
  float* xchg = (float*)(ws);
  u16* h = (u16*)(ws + (8u << 20));
  u16* qkv = (u16*)(ws + (12u << 20));
  u16* o = (u16*)(ws + (20u << 20));
  u16* hh = (u16*)(ws + (12u << 20));
  float* stats = (float*)(ws + (26u << 20));
  float* cspart = (float*)(ws + (26u << 20) + (256u << 10));
  unsigned* flags = (unsigned*)(ws + (26u << 20) + (512u << 10));

  (void)hipFuncSetAttribute((const void*)qkv_attn_kernel, hipFuncAttributeMaxDynamicSharedMemorySize, lds_bytes(192, 3, true));
  (void)hipFuncSetAttribute((const void*)gemm_wo_kernel, hipFuncAttributeMaxDynamicSharedMemorySize, lds_bytes(64, 6, false));
  (void)hipFuncSetAttribute((const void*)gemm_w1_kernel, hipFuncAttributeMaxDynamicSharedMemorySize, lds_bytes(224, 3, true));
  (void)hipFuncSetAttribute((const void*)gemm_w2_kernel, hipFuncAttributeMaxDynamicSharedMemorySize, lds_bytes(128, 4, false));
  prep_kernel<<<S_LEN / 4, 256, 0, stream>>>(x, xn, (const char*)wqkv, (const char*)b1, (const char*)b2, flags);
  qkv_attn_kernel<<<256, 512, lds_bytes(192, 3, true), stream>>>(xn, wqkv, o, (const char*)wo, w1, (float*)qkv, cspart, flags + 512 * 32);
  gemm_wo_kernel<<<256, 512, lds_bytes(64, 6, false), stream>>>(o, wo, h, x, stats);
  gemm_w1_kernel<<<256, 512, lds_bytes(224, 3, true), stream>>>(h, w1, hh, b1, stats, cspart, (const char*)w2);
  gemm_w2_kernel<<<256, 512, lds_bytes(128, 4, false), stream>>>(hh, w2, out, b2, h, xchg, flags);
}
```

```cpp
#include <hip/hip_runtime.h>
#include <hip/hip_bf16.h>
#include <stdint.h>

typedef unsigned short u16;
typedef __attribute__((ext_vector_type(8))) short bf16x8;
typedef __attribute__((ext_vector_type(4))) short s16x4;
typedef __attribute__((ext_vector_type(4))) float f32x4;
typedef __attribute__((ext_vector_type(2))) float f32x2;
typedef __attribute__((ext_vector_type(4))) unsigned u32x4;
typedef __attribute__((ext_vector_type(2))) unsigned u32x2;
typedef __attribute__((ext_vector_type(2))) __bf16 bf16x2_t;
typedef __attribute__((address_space(3))) const char* lds_cptr;

#define STAMP(kid, i)
__device__ __forceinline__ void st16_sc1(void* p, __attribute__((ext_vector_type(4))) unsigned v) { asm volatile("global_store_dwordx4 %0, %1, off sc1\n\ts_nop 1" :: "v"(p), "v"(v) : "memory"); }
#define ST16(p, v) st16_sc1((void*)(p), (v))
constexpr int S_LEN = 2048, DM = 1024, NH = 32, DH = 32, WIN = 256, FF = 3584;
constexpr float LN_EPS = 1e-5f;

__device__ __forceinline__ float bf2f(u16 v) { return __uint_as_float(((unsigned)v) << 16); }
__device__ __forceinline__ float bflo(unsigned v) { return __uint_as_float(v << 16); }
__device__ __forceinline__ float bfhi(unsigned v) { return __uint_as_float(v & 0xffff0000u); }
__device__ __forceinline__ unsigned pk2(float lo, float hi) {
  return __builtin_bit_cast(unsigned, __builtin_convertvector(f32x2{lo, hi}, bf16x2_t));
}
__device__ __forceinline__ void glds16(const void* sbase, unsigned voff, unsigned lds_addr) {
  unsigned sv;
  asm volatile("s_mov_b32 %0, m0\n\ts_mov_b32 m0, %3\n\ts_nop 0\n\tglobal_load_lds_dwordx4 %1, %2\n\ts_mov_b32 m0, %0"
               : "=&s"(sv) : "v"(voff), "s"(sbase), "s"(lds_addr) : "memory");
}
__device__ __forceinline__ void glds16_sc1(const void* sbase, unsigned voff, unsigned lds_addr) {
  unsigned sv;
  asm volatile("s_mov_b32 %0, m0\n\ts_mov_b32 m0, %3\n\ts_nop 0\n\tglobal_load_lds_dwordx4 %1, %2 sc1\n\ts_mov_b32 m0, %0"
               : "=&s"(sv) : "v"(voff), "s"(sbase), "s"(lds_addr) : "memory");
}
__device__ __forceinline__ void touch_lds(const char* p, unsigned lds_scratch) {
  unsigned sv;
  asm volatile("s_mov_b32 %0, m0\n\ts_mov_b32 m0, %2\n\ts_nop 0\n\tglobal_load_lds_dword %1, off\n\ts_mov_b32 m0, %0"
               : "=&s"(sv) : "v"(p), "s"(lds_scratch) : "memory");
}
__device__ __forceinline__ s16x4 vtr(lds_cptr p) {
  return __builtin_bit_cast(s16x4, __builtin_amdgcn_ds_read_tr16_b64_v4i16((__attribute__((address_space(3))) s16x4*)p));
}
template <int CTRL>
__device__ __forceinline__ float dpp_add(float v) {
  return v + __int_as_float(__builtin_amdgcn_update_dpp(0, __float_as_int(v), CTRL, 0xf, 0xf, false));
}
__device__ __forceinline__ float sum8(float v) {
  v = dpp_add<0xB1>(v);
  v = dpp_add<0x4E>(v);
  return dpp_add<0x141>(v);
}
__device__ __forceinline__ float xsum16_32(float m) {
  auto a = __builtin_amdgcn_permlane32_swap(__float_as_uint(m), __float_as_uint(m), false, false);
  m = __uint_as_float(a[0]) + __uint_as_float(a[1]);
  auto b = __builtin_amdgcn_permlane16_swap(__float_as_uint(m), __float_as_uint(m), false, false);
  return __uint_as_float(b[0]) + __uint_as_float(b[1]);
}
__device__ __forceinline__ float xmax16_32(float m) {
  auto a = __builtin_amdgcn_permlane32_swap(__float_as_uint(m), __float_as_uint(m), false, false);
  m = fmaxf(__uint_as_float(a[0]), __uint_as_float(a[1]));
  auto b = __builtin_amdgcn_permlane16_swap(__float_as_uint(m), __float_as_uint(m), false, false);
  return fmaxf(__uint_as_float(b[0]), __uint_as_float(b[1]));
}
__device__ __forceinline__ float wave_sum(float v) {
  v = sum8(v);
  v = dpp_add<0x140>(v);
  return xsum16_32(v);
}

__global__ __launch_bounds__(256) void prep_kernel(const u16* __restrict__ in, u16* __restrict__ out,
                                                   const char* __restrict__ wqkv, const char* __restrict__ b1,
                                                   const char* __restrict__ b2, unsigned* __restrict__ flags) {
  const int lane = threadIdx.x & 63;
  const int row = blockIdx.x * 4 + (threadIdx.x >> 6);
  const u32x4* src = (const u32x4*)(in + (size_t)row * DM);
  u32x4 a = src[lane], b = src[lane + 64];
  const int gi = blockIdx.x * 256 + threadIdx.x;
  unsigned w0 = 0;
  if (gi < 24576) w0 = *(const unsigned*)(wqkv + (size_t)gi * 128);
  else if (gi < 49152) {}
  else if (gi < 49152 + 56) w0 = *(const unsigned*)(b1 + (size_t)(gi - 49152) * 128);
  else if (gi < 49152 + 72) w0 = *(const unsigned*)(b2 + (size_t)(gi - 49152 - 56) * 128);
  if (gi < 768) flags[gi * 32] = 0u;
  unsigned w1c = 0;
  if (threadIdx.x < 64 && blockIdx.x < 128) {
    unsigned long long pc, pe;
    asm volatile("s_getpc_b64 %0" : "=s"(pc));
    asm volatile("s_getpc_b64 s[40:41]\n\ts_add_u32 s40, s40, zz_text_end@rel32@lo+4\n\t"
                 "s_addc_u32 s41, s41, zz_text_end@rel32@hi+12\n\ts_mov_b64 %0, s[40:41]"
                 : "=s"(pe) : : "s40", "s41", "scc");
    const unsigned long long a = (pc & ~127ull) + (unsigned long long)((blockIdx.x >> 3) * 64 + lane) * 128;
    if (a + 4 <= pe) w1c = *(const unsigned*)a;
  }
  float v[16];
#pragma unroll
  for (int i = 0; i < 4; ++i) {
    v[2 * i] = bflo(a[i]);
    v[2 * i + 1] = bfhi(a[i]);
    v[8 + 2 * i] = bflo(b[i]);
    v[8 + 2 * i + 1] = bfhi(b[i]);
  }
  float s = 0.f;
#pragma unroll
  for (int i = 0; i < 16; ++i) s += v[i];
  const float mean = wave_sum(s) * (1.0f / DM);
  float q = 0.f;
#pragma unroll
  for (int i = 0; i < 16; ++i) { v[i] -= mean; q += v[i] * v[i]; }
  const float var = wave_sum(q) * (1.0f / DM);
  const float r = 1.0f / sqrtf(var + LN_EPS);
  u32x4 oa, ob;
#pragma unroll
  for (int i = 0; i < 4; ++i) {
    oa[i] = pk2(v[2 * i] * r, v[2 * i + 1] * r);
    ob[i] = pk2(v[8 + 2 * i] * r, v[8 + 2 * i + 1] * r);
  }
  u32x4* dst = (u32x4*)(out + (size_t)row * DM);
  dst[lane] = oa;
  dst[lane + 64] = ob;
  asm volatile("" ::"v"(w0), "v"(w1c));
}

__device__ __forceinline__ float rowmax17(const f32x4* s) {
  float a = fmaxf(fmaxf(s[0][0], s[0][1]), fmaxf(s[0][2], s[0][3]));
#pragma unroll
  for (int t = 1; t < 17; ++t) {
    a = fmaxf(fmaxf(a, s[t][0]), s[t][1]);
    a = fmaxf(fmaxf(a, s[t][2]), s[t][3]);
  }
  return a;
}

template <bool EARLY>
__device__ __forceinline__ void attn_body(const char* ks, const char* vs, bf16x8 qf, int wid, int lane, int tmin,
                                          char* op) {
  const int fr = lane & 15, g = lane >> 4;
  const float NEG = -1e30f;
  f32x4 s[17];
  const char* kp = ks + (wid * 16 + fr) * 64 + ((g ^ ((4 - ((fr >> 2) & 3)) & 3)) << 4);
#pragma unroll
  for (int t = 0; t < 17; ++t) {
    bf16x8 kf = *(const bf16x8*)(kp + t * 1024);
    s[t] = __builtin_amdgcn_mfma_f32_16x16x32_bf16(kf, qf, f32x4{0.f, 0.f, 0.f, 0.f}, 0, 0, 0);
  }
#pragma unroll
  for (int r = 0; r < 4; ++r) {
    s[0][r] = (4 * g + r > fr) ? s[0][r] : NEG;
    s[16][r] = (4 * g + r <= fr) ? s[16][r] : NEG;
  }
  if (EARLY) {
#pragma unroll
    for (int t = 0; t < 16; ++t)
      if (t < tmin) s[t] = f32x4{NEG, NEG, NEG, NEG};
  }
  const float mx = xmax16_32(rowmax17(s));
  const float c = 0.17677669529663687f * 1.4426950408889634f;
  const f32x2 c2 = {c, c};
  const f32x2 nmc2 = {-mx * c, -mx * c};
  f32x2 sum2 = {0.f, 0.f};
  unsigned pk[18][2];
#pragma unroll
  for (int t = 0; t < 17; ++t) {
#pragma unroll
    for (int hh = 0; hh < 2; ++hh) {
      const f32x2 z = f32x2{s[t][2 * hh], s[t][2 * hh + 1]} * c2 + nmc2;
      const f32x2 p = {__builtin_amdgcn_exp2f(z[0]), __builtin_amdgcn_exp2f(z[1])};
      sum2 += p;
      pk[t][hh] = pk2(p[0], p[1]);
    }
  }
  float sum = sum2[0] + sum2[1];
  pk[17][0] = 0u;
  pk[17][1] = 0u;
  sum = xsum16_32(sum);
  const int q4 = (lane >> 2) & 3, pp = lane & 3;
  const char* vp0 = vs + (wid * 16 + 4 * g + q4) * 64 + ((g & 1) << 5) + pp * 8;
  const char* vp1 = vs + (wid * 16 + 4 * g + q4) * 64 + (((g & 1) ^ 1) << 5) + pp * 8;
  f32x4 o0 = {0.f, 0.f, 0.f, 0.f}, o1 = {0.f, 0.f, 0.f, 0.f};
#pragma unroll
  for (int tp = 0; tp < 9; ++tp) {
    const int t0 = 2 * tp, t1 = (2 * tp + 1 < 17) ? 2 * tp + 1 : 16;
    u32x4 pb = {pk[2 * tp][0], pk[2 * tp][1], pk[2 * tp + 1][0], pk[2 * tp + 1][1]};
    bf16x8 pf = __builtin_bit_cast(bf16x8, pb);
    s16x4 a0 = vtr((lds_cptr)(vp0 + t0 * 1024)), a1 = vtr((lds_cptr)(vp0 + t1 * 1024));
    s16x4 b0 = vtr((lds_cptr)(vp1 + t0 * 1024)), b1 = vtr((lds_cptr)(vp1 + t1 * 1024));
    o0 = __builtin_amdgcn_mfma_f32_16x16x32_bf16(__builtin_shufflevector(a0, a1, 0, 1, 2, 3, 4, 5, 6, 7), pf, o0, 0, 0, 0);
    o1 = __builtin_amdgcn_mfma_f32_16x16x32_bf16(__builtin_shufflevector(b0, b1, 0, 1, 2, 3, 4, 5, 6, 7), pf, o1, 0, 0, 0);
  }
  const float inv = 1.0f / sum;
  u32x2 w0, w1;
  w0[0] = pk2(o0[0] * inv, o0[1] * inv);
  w0[1] = pk2(o0[2] * inv, o0[3] * inv);
  w1[0] = pk2(o1[0] * inv, o1[1] * inv);
  w1[1] = pk2(o1[2] * inv, o1[3] * inv);
  *(u32x2*)(op + 8 * g) = w0;
  *(u32x2*)(op + 32 + 8 * g) = w1;
}

enum { EPI_QKV = 0, EPI_RES_STATS = 1, EPI_LN_GELU = 2, EPI_SPLIT_BIAS_RES = 3 };
constexpr int NLW = 4;

template <int CNT, int NBUF, int NPA, int STAGE>
__device__ __forceinline__ void loader_loop(const char* Ab, const char* Wb, const unsigned* voff, int lw, int nk,
                                            unsigned wstep, unsigned lds0) {
  const char* sp[CNT];
  unsigned st[CNT];
#pragma unroll
  for (int i = 0; i < CNT; ++i) {
    const bool isA = (lw + i * NLW) < NPA;
    sp[i] = isA ? Ab : Wb;
    st[i] = isA ? 128u : wstep;
  }
  auto issue = [&](int buf) {
#pragma unroll
    for (int i = 0; i < CNT; ++i) {
      glds16(sp[i], voff[i], lds0 + buf * STAGE + (lw + i * NLW) * 1024);
      sp[i] += st[i];
    }
  };
  issue(0);
  issue(1);
  asm volatile("s_waitcnt vmcnt(%0)" ::"i"(CNT) : "memory");
  __builtin_amdgcn_s_barrier();
#pragma unroll
  for (int s = 2; s < NBUF; ++s) issue(s);
  int buf = 0;
  for (int t = 0; t < nk; ++t) {
    if (t + NBUF <= nk) {
      asm volatile("s_waitcnt vmcnt(%0)" ::"i"((NBUF - 2) * CNT) : "memory");
    } else {
      asm volatile("s_waitcnt vmcnt(0)" ::: "memory");
    }
    __builtin_amdgcn_s_barrier();
    if (t + NBUF < nk) issue(buf);
    buf = (buf + 1 == NBUF) ? 0 : buf + 1;
  }
}

template <int BN, int NBUF, int EPI, int XA, int TL0 = 0, int TL1 = 0>
__device__ __forceinline__ void gemm_body(const char* __restrict__ tp0, const char* __restrict__ tp1, const u16* __restrict__ A, const u16* __restrict__ W,
                                          u16* __restrict__ C, const u16* __restrict__ bias,
                                          const u16* __restrict__ res, float* __restrict__ aux0,
                                          float* __restrict__ aux1, unsigned* __restrict__ flags,
                                          const int N, const int K) {
  constexpr int BM = 128;
  constexpr bool WIDE = BN > 64;
  constexpr bool SPLIT = (EPI == EPI_SPLIT_BIAS_RES);
  constexpr int NTHR = 512;
  constexpr int NG = BN / 64;
  constexpr bool HALF = (BN % 64) != 0;
  constexpr int NT = WIDE ? BN / 32 : 4;
  constexpr int ABYTES = BM * 128, WBYTES = 64 * BN * 2, STAGE = ABYTES + WBYTES;
  constexpr int NPA = ABYTES / 1024, NP = STAGE / 1024;
  constexpr int CMAX = (NP + NLW - 1) / NLW;
  constexpr int TSCR = NBUF * STAGE + 4096;
  constexpr int SIDE = NBUF * STAGE;
  extern __shared__ __attribute__((aligned(16))) char smem[];

  const int tid = threadIdx.x, lane = tid & 63;
  const int wid = __builtin_amdgcn_readfirstlane(tid >> 6);
  STAMP(EPI, 0);
  const int xc = blockIdx.x & 7, rr = blockIdx.x >> 3;
  int bm, bn, hk = 0;
  if (SPLIT) {
    hk = rr & 1;
    const int tt = rr >> 1;
    bm = (xc & 3) * 4 + (tt & 3);
    bn = (xc >> 2) * 4 + (tt >> 2);
  } else {
    constexpr int XB = 32 / XA, PM = 16 / XA;
    bm = (xc % PM) * XA + (rr % XA);
    bn = (xc / PM) * XB + (rr / XA);
  }
  const int m0 = bm * BM, n0 = bn * BN;
  const int nk = SPLIT ? K / 128 : K / 64;
  const int kbeg = hk * (K / 2);
  const int odd = HALF ? (bn & 1) : 0;
  const int cfull = odd ? 32 : 0, chalf = odd ? 0 : NG * 64;
  const unsigned lds0 = (unsigned)(uintptr_t)(__attribute__((address_space(3))) char*)smem;
  const int fr = lane & 15, g = lane >> 4;

  f32x4 acc[4][NT];
  const int wm = wid & 1, wx = (wid >> 1) & 1;
  int tcol[NT];

  if (wid >= 4) {
    const int lw = wid - 4;
    unsigned voff[CMAX];
#pragma unroll
    for (int i = 0; i < CMAX; ++i) {
      const int idx = lw + i * NLW;
      if (idx < NPA) {
        const int row = idx * 8 + (lane >> 3), ch = (lane & 7) ^ (row & 7);
        voff[i] = (unsigned)(((m0 + row) * K + ch * 8) * 2);
      } else if (idx < NPA + NG * 8) {
        const int w = idx - NPA, G = w >> 3, k = (w & 7) * 8 + (lane >> 3);
        const int ch = (lane & 7) ^ ((k & 2) | ((k >> 1) & 4));
        const int col = (EPI == EPI_QKV) ? G * DM + bn * 64 : n0 + cfull + G * 64;
        voff[i] = (unsigned)((k * N + col + ch * 8) * 2);
      } else {
        const int w = idx - NPA - NG * 8, k = w * 16 + (lane >> 2);
        const int ch = (lane & 3) ^ ((k >> 2) & 2);
        voff[i] = (unsigned)((k * N + n0 + chalf + ch * 8) * 2);
      }
    }
    const unsigned wstep = (unsigned)(64 * N * 2);
    const char* Ab = (const char*)(A + kbeg);
    const char* Wb = (const char*)(W + (size_t)kbeg * N);
    if (lw + (CMAX - 1) * NLW < NP)
      loader_loop<CMAX, NBUF, NPA, STAGE>(Ab, Wb, voff, lw, nk, wstep, lds0);
    else
      loader_loop<CMAX - 1, NBUF, NPA, STAGE>(Ab, Wb, voff, lw, nk, wstep, lds0);
  } else {
    if (EPI == EPI_LN_GELU) {
      if (tid < 128) {
        const f32x4* sp = (const f32x4*)(aux0 + (size_t)(m0 + tid) * 32);
        f32x4 p[8];
#pragma unroll
        for (int i = 0; i < 8; ++i) p[i] = sp[i];
        float mean = 0.f;
#pragma unroll
        for (int i = 0; i < 8; ++i) mean += p[i][0] + p[i][2];
        mean *= (1.0f / 16.0f);
        float m2 = 0.f;
#pragma unroll
        for (int i = 0; i < 8; ++i) {
          const float d0 = p[i][0] - mean, d1 = p[i][2] - mean;
          m2 += p[i][1] + p[i][3] + 64.0f * (d0 * d0 + d1 * d1);
        }
        const float var = m2 * (1.0f / DM);
        const float r1 = 1.0f / sqrtf(var + LN_EPS);
        const float r2 = 1.0f / sqrtf(var * r1 * r1 + LN_EPS);
        *(f32x2*)(smem + SIDE + tid * 8) = f32x2{mean, r1 * r2};
      } else if (tid - 128 < BN / 2) {
        const int c = (tid - 128) * 2;
        f32x2 cs = {0.f, 0.f};
#pragma unroll
        for (int kc = 0; kc < 16; ++kc) cs += *(const f32x2*)(aux1 + (size_t)kc * FF + n0 + c);
        const unsigned bb = *(const unsigned*)(bias + n0 + c);
        *(f32x4*)(smem + SIDE + 1024 + c * 8) = f32x4{cs[0], bflo(bb), cs[1], bfhi(bb)};
      }
    }
    if (EPI == EPI_QKV) {
      const int line = 24576 + (blockIdx.x * 4 + wid) * 64 + lane;
      if (__builtin_amdgcn_readfirstlane(line) < 49152)
        touch_lds((const char*)W + (size_t)(line < 49152 ? line : 49151) * 128, lds0 + TSCR + wid * 256);
    }
    const int q = (lane >> 2) & 3, pp = lane & 3;
    const int wk = WIDE ? 0 : wx;
    int aoff[2];
#pragma unroll
    for (int ks = 0; ks < 2; ++ks) aoff[ks] = (wm * 64 + fr) * 128 + (((ks * 4 + g) ^ (fr & 7)) << 4);
    int tb[NT];
    const int f1 = (q & 2) | ((g & 1) << 2);
#pragma unroll
    for (int j = 0; j < NT; ++j) {
      int G, tp;
      if (!WIDE) { G = 0; tp = j; }
      else if (EPI == EPI_QKV) { G = (j >> 1) ^ 2; G = G == 3 ? 1 : G; tp = wx * 2 + (j & 1); }
      else if (j < 4) { G = wx * (NG - 1); tp = j; }
      else { G = 1; tp = wx * 2 + (j - 4); }
      if (WIDE && HALF && j == NT - 1) {
        tb[j] = ABYTES + NG * 8192 + (8 * g + q) * 64 + (((2 * wx + (pp >> 1)) ^ ((g & 1) << 1)) << 4) + (pp & 1) * 8;
        tcol[j] = chalf + wx * 16;
      } else {
        tb[j] = ABYTES + G * 8192 + (8 * g + q) * 128 + (((2 * tp + (pp >> 1)) ^ f1) << 4) + (pp & 1) * 8;
        tcol[j] = cfull + G * 64 + tp * 16;
      }
    }
#pragma unroll
    for (int i = 0; i < 4; ++i)
#pragma unroll
      for (int j = 0; j < NT; ++j) acc[i][j] = f32x4{0.f, 0.f, 0.f, 0.f};

    bf16x8 xa[4], wa[NT], xb[4], wb[NT];
    auto rd = [&](bf16x8* xf, bf16x8* wf, int buf, int ks) {
      const char* base = smem + buf * STAGE;
#pragma unroll
      for (int j = 0; j < NT; ++j) {
        const int rs = (WIDE && HALF && j == NT - 1) ? 64 : 128;
        s16x4 lo = vtr((lds_cptr)(base + tb[j] + ks * 32 * rs));
        s16x4 hi = vtr((lds_cptr)(base + tb[j] + ks * 32 * rs + 4 * rs));
        wf[j] = __builtin_shufflevector(lo, hi, 0, 1, 2, 3, 4, 5, 6, 7);
      }
#pragma unroll
      for (int i = 0; i < 4; ++i) xf[i] = *(const bf16x8*)(base + aoff[ks] + i * 2048);
    };
    auto mm = [&](const bf16x8* xf, const bf16x8* wf) {
#pragma unroll
      for (int i = 0; i < 4; ++i)
#pragma unroll
        for (int j = 0; j < NT; ++j)
          acc[i][j] = __builtin_amdgcn_mfma_f32_16x16x32_bf16(wf[j], xf[i], acc[i][j], 0, 0, 0);
    };
#define INTERLEAVE() do { _Pragma("unroll") for (int z_ = 0; z_ < 2 * NT + 4; ++z_) { __builtin_amdgcn_sched_group_barrier(0x8, 1, 0); __builtin_amdgcn_sched_group_barrier(0x100, 1, 0); } __builtin_amdgcn_sched_group_barrier(0x8, 4 * NT - (2 * NT + 4), 0); } while (0)
    __builtin_amdgcn_s_waitcnt(0xC07F);
    __builtin_amdgcn_s_barrier();
    STAMP(EPI, 1);
    auto do_touch = [&](int i) {
      const int line = ((blockIdx.x * 4 + wid) * 2 + i) * 64 + lane;
      if (__builtin_amdgcn_readfirstlane(line) < TL0 + TL1) {
        const int lc = line < TL0 + TL1 ? line : TL0 + TL1 - 1;
        const char* p = lc < TL0 ? tp0 + (size_t)lc * 128 : tp1 + (size_t)(lc - TL0) * 128;
        unsigned sv;
        asm volatile("s_mov_b32 %0, m0\n\ts_mov_b32 m0, %2\n\ts_nop 0\n\tglobal_load_lds_dword %1, off\n\ts_mov_b32 m0, %0"
                     : "=&s"(sv) : "v"(p), "s"(lds0 + TSCR + wid * 256) : "memory");
      }
    };
    if (WIDE) {
      rd(xa, wa, 0, 0);
      int b0 = 0;
      for (int t = 0; t < nk; ++t) {
        if (TL0 + TL1 > 0) { if (t == 2) do_touch(0); if (t == 7) do_touch(1); }
        rd(xb, wb, b0, 1);
        b0 = (b0 + 1 == NBUF) ? 0 : b0 + 1;
        mm(xa, wa);
        INTERLEAVE();
        __builtin_amdgcn_sched_barrier(0);
        __builtin_amdgcn_s_waitcnt(0xC07F);
        __builtin_amdgcn_s_barrier();
        rd(xa, wa, b0, 0);
        mm(xb, wb);
        INTERLEAVE();
        __builtin_amdgcn_sched_barrier(0);
      }
    } else {
      rd(xa, wa, 0, wk);
      int b1 = 1;
      for (int t = 0; t < nk; t += 2) {
        __builtin_amdgcn_s_waitcnt(0xC07F);
        __builtin_amdgcn_s_barrier();
        rd(xb, wb, b1, wk);
        b1 = (b1 + 1 == NBUF) ? 0 : b1 + 1;
        mm(xa, wa);
        INTERLEAVE();
        __builtin_amdgcn_sched_barrier(0);
        __builtin_amdgcn_s_waitcnt(0xC07F);
        __builtin_amdgcn_s_barrier();
        rd(xa, wa, b1, wk);
        b1 = (b1 + 1 == NBUF) ? 0 : b1 + 1;
        mm(xb, wb);
        INTERLEAVE();
        __builtin_amdgcn_sched_barrier(0);
      }
    }
    __builtin_amdgcn_s_waitcnt(0xC07F);
    if (TL0 + TL1 > 0) asm volatile("s_waitcnt vmcnt(0)" ::: "memory");
    STAMP(EPI, 2);
    if (SPLIT) {
      const int tile = bm * 8 + bn;
      const auto rsrc = __builtin_amdgcn_make_buffer_rsrc(aux0, 0, 128 * 2 * 2 * 16384, 0x00020000);
      if (wm != hk) {
        const int slot = ((tile * 2 + hk) * 2 + wx);
#pragma unroll
        for (int i = 0; i < 4; ++i)
#pragma unroll
          for (int j = 0; j < NT; ++j)
            __builtin_amdgcn_raw_buffer_store_b128(__builtin_bit_cast(u32x4, acc[i][j]), rsrc,
                                                   slot * 16384 + (i * NT + j) * 1024 + lane * 16, 0, 16);
        asm volatile("s_waitcnt vmcnt(0)" ::: "memory");
        if (lane == 0) __hip_atomic_store(flags + slot * 32, 1u, __ATOMIC_RELAXED, __HIP_MEMORY_SCOPE_AGENT);
      } else {
        const int slot = ((tile * 2 + (hk ^ 1)) * 2 + wx);
        for (int spin = 0; spin < (1 << 24); ++spin) {
          if (__hip_atomic_load(flags + slot * 32, __ATOMIC_RELAXED, __HIP_MEMORY_SCOPE_AGENT) != 0u) break;
          __builtin_amdgcn_s_sleep(2);
        }
        asm volatile("" ::: "memory");
#pragma unroll
        for (int i = 0; i < 4; ++i) {
          u32x4 t[NT];
#pragma unroll
          for (int j = 0; j < NT; ++j)
            t[j] = __builtin_amdgcn_raw_buffer_load_b128(rsrc, slot * 16384 + (i * NT + j) * 1024 + lane * 16, 0, 16);
#pragma unroll
          for (int j = 0; j < NT; ++j) acc[i][j] += __builtin_bit_cast(f32x4, t[j]);
        }
      }
    }
  }
  __syncthreads();
  STAMP(EPI, 3);

  constexpr int CPR = BN / 8;
  if (EPI == EPI_QKV) {
    constexpr int KIMG = 0, VIMG = 49152, QIMG = 98304, RED = 114688, HIMG = 24576;
    u16* kvg = (u16*)aux0;
    u32x4 cw[8];
    const int t2 = tid - 256;
    const int cb = blockIdx.x < 224 ? blockIdx.x : 223;
    const int cg = cb % 14, kc = cb / 14;
    if (wid < 4) {
      auto put = [&](int j) {
        const int grp = tcol[j] >> 6, hd = (tcol[j] >> 5) & 1;
        const int dd = (tcol[j] & 31) + g * 4, chn = dd >> 3, hf = (dd >> 2) & 1;
        const int sw = (grp == 2) ? (((fr >> 2) & 1) << 1) : ((4 - ((fr >> 2) & 3)) & 3);
        const int base = (grp == 0) ? QIMG + hd * 8192 : ((grp == 1) ? KIMG : VIMG) + hd * HIMG + 256 * 64;
#pragma unroll
        for (int i = 0; i < 4; ++i) {
          u32x2 o;
          o[0] = pk2(acc[i][j][0], acc[i][j][1]);
          o[1] = pk2(acc[i][j][2], acc[i][j][3]);
          *(u32x2*)(smem + base + (wm * 64 + i * 16 + fr) * 64 + ((chn ^ sw) << 4) + hf * 8) = o;
        }
      };
#pragma unroll
      for (int j = 0; j < 4; ++j) put(j);
      if (bm < 15) {
#pragma unroll
        for (int kv = 0; kv < 2; ++kv)
#pragma unroll
          for (int pc = 0; pc < 4; ++pc) {
            const int row = wm * 64 + pc * 16 + (lane >> 2), slot = lane & 3;
            const int sw = kv ? (((row >> 2) & 1) << 1) : ((4 - ((row >> 2) & 3)) & 3);
            const u32x4 v = *(const u32x4*)(smem + (kv ? VIMG : KIMG) + wx * HIMG + (256 + row) * 64 + slot * 16);
            ST16(kvg + ((size_t)(kv * NH + bn * 2 + wx) * S_LEN + m0 + row) * 32 + (slot ^ sw) * 8, v);
          }
      }
      put(4);
      put(5);
      if (bm < 15) {
        asm volatile("s_waitcnt vmcnt(0)" ::: "memory");
        if (lane == 0)
          __hip_atomic_fetch_add(flags + (bm * 16 + bn) * 32, 1u, __ATOMIC_RELAXED, __HIP_MEMORY_SCOPE_AGENT);
      }
    } else {
      const u16* cp = res + (size_t)(kc * 64 + (t2 >> 5) * 8) * FF + cg * 256 + (t2 & 31) * 8;
#pragma unroll
      for (int i = 0; i < 8; ++i) cw[i] = *(const u32x4*)(cp + (size_t)i * FF);
      const int lw = wid - 4, pr = lw >> 1, kv = lw & 1;
      const int pb = bm - 2 + pr;
      const unsigned dst = lds0 + (kv ? VIMG : KIMG) + pr * 128 * 64;
      if (pb >= 0) {
        const unsigned* fp = flags + (pb * 16 + bn) * 32;
        for (int spin = 0; spin < (1 << 24); ++spin) {
          if (__hip_atomic_load(fp, __ATOMIC_RELAXED, __HIP_MEMORY_SCOPE_AGENT) >= 4u) break;
          __builtin_amdgcn_s_sleep(2);
        }
        asm volatile("" ::: "memory");
        const int kl = lane >> 2, slot = lane & 3;
        const int sw = kv ? (((kl >> 2) & 1) << 1) : ((4 - ((kl >> 2) & 3)) & 3);
        const unsigned vo = (unsigned)((kl * 32 + (slot ^ sw) * 8) * 2);
#pragma unroll
        for (int hd = 0; hd < 2; ++hd) {
          const u16* src = kvg + ((size_t)(kv * NH + bn * 2 + hd) * S_LEN + pb * 128) * 32;
#pragma unroll
          for (int pc = 0; pc < 8; ++pc) glds16_sc1(src + pc * 512, vo, dst + hd * HIMG + pc * 1024);
        }
      } else {
#pragma unroll
        for (int hd = 0; hd < 2; ++hd)
#pragma unroll
          for (int pc = 0; pc < 8; ++pc)
            *(u32x4*)(smem + (kv ? VIMG : KIMG) + pr * 128 * 64 + hd * HIMG + pc * 1024 + lane * 16) = u32x4{0u, 0u, 0u, 0u};
      }
      {
        float* red = (float*)(smem + RED);
        float sacc[8];
#pragma unroll
        for (int e = 0; e < 8; ++e) sacc[e] = 0.f;
#pragma unroll
        for (int i = 0; i < 8; ++i)
#pragma unroll
          for (int e = 0; e < 4; ++e) { sacc[2 * e] += bflo(cw[i][e]); sacc[2 * e + 1] += bfhi(cw[i][e]); }
        *(f32x4*)(red + (t2 >> 5) * 256 + (t2 & 31) * 8) = f32x4{sacc[0], sacc[1], sacc[2], sacc[3]};
        *(f32x4*)(red + (t2 >> 5) * 256 + (t2 & 31) * 8 + 4) = f32x4{sacc[4], sacc[5], sacc[6], sacc[7]};
      }
      asm volatile("s_waitcnt vmcnt(0)" ::: "memory");
    }
    STAMP(EPI, 5);
    __syncthreads();
    STAMP(EPI, 6);
    if (wid < 4) {
      const int li = (rr * 4 + wid) * 64 + lane;
      const char* p = tp1 + (size_t)(li >> 3) * 2048 + (xc >> 2) * 1024 + (li & 7) * 128;
      unsigned sv;
      asm volatile("s_mov_b32 %0, m0\n\ts_mov_b32 m0, %2\n\ts_nop 0\n\tglobal_load_lds_dword %1, off\n\ts_mov_b32 m0, %0"
                   : "=&s"(sv) : "v"(p), "s"(lds0 + TSCR + wid * 256) : "memory");
    }
    if (blockIdx.x < 224 && tid < 256) {
      const float* red = (const float*)(smem + RED);
      float t = 0.f;
#pragma unroll
      for (int i = 0; i < 8; ++i) t += red[i * 256 + tid];
      aux1[(size_t)kc * FF + cg * 256 + tid] = t;
    }
    const int qw = m0 + wid * 16;
    const int tmin = (qw < WIN) ? (WIN - qw) >> 4 : 0;
    const int qo = (wid * 16 + fr) * 64 + ((g ^ ((4 - ((fr >> 2) & 3)) & 3)) << 4);
    const bf16x8 qf0 = *(const bf16x8*)(smem + QIMG + qo), qf1 = *(const bf16x8*)(smem + QIMG + 8192 + qo);
    char* ost = smem + QIMG + wid * 1024;
#pragma unroll 1
    for (int hd = 0; hd < 2; ++hd) {
      const bf16x8 qf = hd ? qf1 : qf0;
      char* op = ost + hd * 8192 + fr * 64;
      if (bm < 2) attn_body<true>(smem + KIMG + hd * HIMG, smem + VIMG + hd * HIMG, qf, wid, lane, tmin, op);
      else attn_body<false>(smem + KIMG + hd * HIMG, smem + VIMG + hd * HIMG, qf, wid, lane, 0, op);
    }
#pragma unroll
    for (int i = 0; i < 2; ++i) {
      const int row = i * 8 + (lane >> 3), ch = lane & 7;
      const u32x4 v = *(const u32x4*)(ost + (ch >> 2) * 8192 + row * 64 + (ch & 3) * 16);
      ST16(C + (size_t)(qw + row) * DM + bn * 64 + ch * 8, v);
    }
    STAMP(EPI, 7);
  } else if (EPI == EPI_LN_GELU) {
    constexpr int RS = BN * 4 + 16;
    if (wid < 4) {
#pragma unroll
      for (int j = 0; j < NT; ++j)
#pragma unroll
        for (int i = 0; i < 4; ++i)
          *(f32x4*)(smem + (wm * 64 + i * 16 + fr) * RS + (tcol[j] + g * 4) * 4) = acc[i][j];
    }
    __syncthreads();
#pragma unroll
    for (int c = tid; c < BM * CPR; c += NTHR) {
      const int row = c / CPR, ch = c % CPR;
      const f32x4 a0 = *(const f32x4*)(smem + row * RS + ch * 32);
      const f32x4 a1 = *(const f32x4*)(smem + row * RS + ch * 32 + 16);
      const f32x2 rs = *(const f32x2*)(smem + SIDE + row * 8);
      float v[8];
#pragma unroll
      for (int r = 0; r < 4; ++r) {
        const f32x4 cb = *(const f32x4*)(smem + SIDE + 1024 + ch * 64 + r * 16);
        const float x0 = (r < 2) ? a0[2 * r] : a1[2 * r - 4];
        const float x1 = (r < 2) ? a0[2 * r + 1] : a1[2 * r - 3];
        v[2 * r] = (x0 - rs[0] * cb[0]) * rs[1] + cb[1];
        v[2 * r + 1] = (x1 - rs[0] * cb[2]) * rs[1] + cb[3];
      }
      u32x4 o;
#pragma unroll
      for (int r = 0; r < 4; ++r) {
        const f32x2 x = {v[2 * r], v[2 * r + 1]};
        const f32x2 x2 = x * x;
        const f32x2 w = x2 * f32x2{-0.10294324f, -0.10294324f} + f32x2{-2.302208198f, -2.302208198f};
        const f32x2 z = x * w;
        f32x2 e = {__builtin_amdgcn_exp2f(z[0]), __builtin_amdgcn_exp2f(z[1])};
        e = e + f32x2{1.0f, 1.0f};
        const f32x2 y = x * f32x2{__builtin_amdgcn_rcpf(e[0]), __builtin_amdgcn_rcpf(e[1])};
        o[r] = pk2(y[0], y[1]);
      }
      ST16(C + (size_t)(m0 + row) * N + n0 + ch * 8, o);
    }
  } else if (EPI == EPI_RES_STATS) {
    constexpr int RS = BN * 4 + 16;
    constexpr int PART = BM * RS;
    if (wid < 4) {
#pragma unroll
      for (int i = 0; i < 4; ++i)
#pragma unroll
        for (int j = 0; j < NT; ++j)
          *(f32x4*)(smem + wx * PART + (wm * 64 + i * 16 + fr) * RS + (j * 16 + g * 4) * 4) = acc[i][j];
    }
    __syncthreads();
#pragma unroll
    for (int c = tid; c < BM * CPR; c += NTHR) {
      const int row = c / CPR, ch = c % CPR;
      const f32x4 a0 = *(const f32x4*)(smem + row * RS + ch * 32);
      const f32x4 a1 = *(const f32x4*)(smem + row * RS + ch * 32 + 16);
      const f32x4 b0 = *(const f32x4*)(smem + PART + row * RS + ch * 32);
      const f32x4 b1 = *(const f32x4*)(smem + PART + row * RS + ch * 32 + 16);
      const u32x4 rv = *(const u32x4*)(res + (size_t)(m0 + row) * N + n0 + ch * 8);
      u32x4 o;
      o[0] = pk2(a0[0] + b0[0] + bflo(rv[0]), a0[1] + b0[1] + bfhi(rv[0]));
      o[1] = pk2(a0[2] + b0[2] + bflo(rv[1]), a0[3] + b0[3] + bfhi(rv[1]));
      o[2] = pk2(a1[0] + b1[0] + bflo(rv[2]), a1[1] + b1[1] + bfhi(rv[2]));
      o[3] = pk2(a1[2] + b1[2] + bflo(rv[3]), a1[3] + b1[3] + bfhi(rv[3]));
      ST16(C + (size_t)(m0 + row) * N + n0 + ch * 8, o);
      float s = 0.f;
#pragma unroll
      for (int r = 0; r < 4; ++r) s += bflo(o[r]) + bfhi(o[r]);
      const float mean = sum8(s) * (1.0f / 64.0f);
      float m2 = 0.f;
#pragma unroll
      for (int r = 0; r < 4; ++r) {
        const float d0 = bflo(o[r]) - mean, d1 = bfhi(o[r]) - mean;
        m2 += d0 * d0 + d1 * d1;
      }
      m2 = sum8(m2);
      if (ch == 0) *(f32x2*)(aux0 + ((size_t)(m0 + row) * 16 + bn) * 2) = f32x2{mean, m2};
    }
  } else {
    constexpr int RS = BN * 4 + 16;
    if (wid < 4 && wm == hk) {
#pragma unroll
      for (int i = 0; i < 4; ++i)
#pragma unroll
        for (int j = 0; j < NT; ++j)
          *(f32x4*)(smem + (i * 16 + fr) * RS + (tcol[j] + g * 4) * 4) = acc[i][j];
    }
    __syncthreads();
#pragma unroll
    for (int c = tid; c < 64 * CPR; c += NTHR) {
      const int row = c / CPR, ch = c % CPR;
      const f32x4 a0 = *(const f32x4*)(smem + row * RS + ch * 32);
      const f32x4 a1 = *(const f32x4*)(smem + row * RS + ch * 32 + 16);
      const u32x4 bb = *(const u32x4*)(bias + n0 + ch * 8);
      const size_t go = (size_t)(m0 + hk * 64 + row) * N + n0 + ch * 8;
      const u32x4 rv = *(const u32x4*)(res + go);
      u32x4 o;
      o[0] = pk2(a0[0] + bflo(bb[0]) + bflo(rv[0]), a0[1] + bfhi(bb[0]) + bfhi(rv[0]));
      o[1] = pk2(a0[2] + bflo(bb[1]) + bflo(rv[1]), a0[3] + bfhi(bb[1]) + bfhi(rv[1]));
      o[2] = pk2(a1[0] + bflo(bb[2]) + bflo(rv[2]), a1[1] + bfhi(bb[2]) + bfhi(rv[2]));
      o[3] = pk2(a1[2] + bflo(bb[3]) + bflo(rv[3]), a1[3] + bfhi(bb[3]) + bfhi(rv[3]));
      ST16(C + go, o);
    }
  }
  asm volatile("s_waitcnt vmcnt(0)" ::: "memory");
  STAMP(EPI, 4);
}

__global__ __launch_bounds__(512, 1) void qkv_attn_kernel(const u16* __restrict__ A, const u16* __restrict__ W,
                                                         u16* __restrict__ o, const char* __restrict__ wo,
                                                         const u16* __restrict__ w1, float* __restrict__ kvg,
                                                         float* __restrict__ cspart, unsigned* __restrict__ flags) {
  gemm_body<192, 3, EPI_QKV, 8, 57344, 0>((const char*)w1, wo, A, W, o, nullptr, w1, kvg, cspart, flags, 3 * DM, DM);
}
__global__ __launch_bounds__(512, 1) void gemm_wo_kernel(const u16* __restrict__ A, const u16* __restrict__ W,
                                                        u16* __restrict__ C, const u16* __restrict__ res,
                                                        float* __restrict__ stats) {
  gemm_body<64, 6, EPI_RES_STATS, 4>(nullptr, nullptr, A, W, C, nullptr, res, stats, nullptr, nullptr, DM, DM);
}
__global__ __launch_bounds__(512, 1) void gemm_w1_kernel(const u16* __restrict__ A, const u16* __restrict__ W,
                                                        u16* __restrict__ C, const u16* __restrict__ bias,
                                                        float* __restrict__ stats, float* __restrict__ cspart,
                                                        const char* __restrict__ t0) {
  gemm_body<224, 3, EPI_LN_GELU, 8, 57344, 0>(t0, t0, A, W, C, bias, nullptr, stats, cspart, nullptr, FF, DM);
}
__global__ __launch_bounds__(512, 1) void gemm_w2_kernel(const u16* __restrict__ A, const u16* __restrict__ W,
                                                        u16* __restrict__ C, const u16* __restrict__ bias,
                                                        const u16* __restrict__ res, float* __restrict__ xchg,
                                                        unsigned* __restrict__ flags) {
  gemm_body<128, 4, EPI_SPLIT_BIAS_RES, 4>(nullptr, nullptr, A, W, C, bias, res, xchg, nullptr, flags, DM, FF);
}
extern "C" __global__ void zz_text_end() {}

constexpr int lds_bytes(int BN, int NBUF, bool side) { return NBUF * (128 * 64 * 2 + 64 * BN * 2) + (side ? 5120 : 0); }

extern "C" void kernel_launch(void* const* d_in, const int* in_sizes, int n_in, void* d_out, int out_size,
                              void* d_ws, size_t ws_size, hipStream_t stream) {
  const u16* x = (const u16*)d_in[0];
  const u16* wqkv = (const u16*)d_in[1];
  const u16* wo = (const u16*)d_in[2];
  const u16* w1 = (const u16*)d_in[4];
  const u16* b1 = (const u16*)d_in[5];
  const u16* w2 = (const u16*)d_in[6];
  const u16* b2 = (const u16*)d_in[7];
  u16* out = (u16*)d_out;
  char* ws = (char*)d_ws;
  u16* xn = (u16*)(ws);
  float* xchg = (float*)(ws);
  u16* h = (u16*)(ws + (8u << 20));
  u16* qkv = (u16*)(ws + (12u << 20));
  u16* o = (u16*)(ws + (20u << 20));
  u16* hh = (u16*)(ws + (12u << 20));
  float* stats = (float*)(ws + (26u << 20));
  float* cspart = (float*)(ws + (26u << 20) + (256u << 10));
  unsigned* flags = (unsigned*)(ws + (26u << 20) + (512u << 10));

  (void)hipFuncSetAttribute((const void*)qkv_attn_kernel, hipFuncAttributeMaxDynamicSharedMemorySize, lds_bytes(192, 3, true));
  (void)hipFuncSetAttribute((const void*)gemm_wo_kernel, hipFuncAttributeMaxDynamicSharedMemorySize, lds_bytes(64, 6, false));
  (void)hipFuncSetAttribute((const void*)gemm_w1_kernel, hipFuncAttributeMaxDynamicSharedMemorySize, lds_bytes(224, 3, true));
  (void)hipFuncSetAttribute((const void*)gemm_w2_kernel, hipFuncAttributeMaxDynamicSharedMemorySize, lds_bytes(128, 4, false));
  prep_kernel<<<S_LEN / 4, 256, 0, stream>>>(x, xn, (const char*)wqkv, (const char*)b1, (const char*)b2, flags);
  qkv_attn_kernel<<<256, 512, lds_bytes(192, 3, true), stream>>>(xn, wqkv, o, (const char*)wo, w1, (float*)qkv, cspart, flags + 512 * 32);
  gemm_wo_kernel<<<256, 512, lds_bytes(64, 6, false), stream>>>(o, wo, h, x, stats);
  gemm_w1_kernel<<<256, 512, lds_bytes(224, 3, true), stream>>>(h, w1, hh, b1, stats, cspart, (const char*)w2);
  gemm_w2_kernel<<<256, 512, lds_bytes(128, 4, false), stream>>>(hh, w2, out, b2, h, xchg, flags);
}
```

```cpp
#include <hip/hip_runtime.h>
#include <hip/hip_bf16.h>
#include <stdint.h>

typedef unsigned short u16;
typedef __attribute__((ext_vector_type(8))) short bf16x8;
typedef __attribute__((ext_vector_type(4))) short s16x4;
typedef __attribute__((ext_vector_type(4))) float f32x4;
typedef __attribute__((ext_vector_type(2))) float f32x2;
typedef __attribute__((ext_vector_type(4))) unsigned u32x4;
typedef __attribute__((ext_vector_type(2))) unsigned u32x2;
typedef __attribute__((ext_vector_type(2))) __bf16 bf16x2_t;
typedef __attribute__((address_space(3))) const char* lds_cptr;

__device__ __forceinline__ void st16_sc1(void* p, u32x4 v) {
  asm volatile("global_store_dwordx4 %0, %1, off sc1\n\ts_nop 1" ::"v"(p), "v"(v) : "memory");
}
#define ST16(p, v) st16_sc1((void*)(p), (v))

constexpr int S_LEN = 2048, DM = 1024, NH = 32, DH = 32, WIN = 256, FF = 3584;
constexpr float LN_EPS = 1e-5f;

__device__ __forceinline__ float bf2f(u16 v) { return __uint_as_float(((unsigned)v) << 16); }
__device__ __forceinline__ float bflo(unsigned v) { return __uint_as_float(v << 16); }
__device__ __forceinline__ float bfhi(unsigned v) { return __uint_as_float(v & 0xffff0000u); }
__device__ __forceinline__ unsigned pk2(float lo, float hi) {
  return __builtin_bit_cast(unsigned, __builtin_convertvector(f32x2{lo, hi}, bf16x2_t));
}
__device__ __forceinline__ void glds16(const void* sbase, unsigned voff, unsigned lds_addr) {
  unsigned sv;
  asm volatile("s_mov_b32 %0, m0\n\ts_mov_b32 m0, %3\n\ts_nop 0\n\tglobal_load_lds_dwordx4 %1, %2\n\ts_mov_b32 m0, %0"
               : "=&s"(sv) : "v"(voff), "s"(sbase), "s"(lds_addr) : "memory");
}
__device__ __forceinline__ void glds16_sc1(const void* sbase, unsigned voff, unsigned lds_addr) {
  unsigned sv;
  asm volatile("s_mov_b32 %0, m0\n\ts_mov_b32 m0, %3\n\ts_nop 0\n\tglobal_load_lds_dwordx4 %1, %2 sc1\n\ts_mov_b32 m0, %0"
               : "=&s"(sv) : "v"(voff), "s"(sbase), "s"(lds_addr) : "memory");
}
__device__ __forceinline__ void touch_lds(const char* p, unsigned lds_scratch) {
  unsigned sv;
  asm volatile("s_mov_b32 %0, m0\n\ts_mov_b32 m0, %2\n\ts_nop 0\n\tglobal_load_lds_dword %1, off\n\ts_mov_b32 m0, %0"
               : "=&s"(sv) : "v"(p), "s"(lds_scratch) : "memory");
}
__device__ __forceinline__ s16x4 vtr(lds_cptr p) {
  return __builtin_bit_cast(s16x4, __builtin_amdgcn_ds_read_tr16_b64_v4i16((__attribute__((address_space(3))) s16x4*)p));
}
template <int CTRL>
__device__ __forceinline__ float dpp_add(float v) {
  return v + __int_as_float(__builtin_amdgcn_update_dpp(0, __float_as_int(v), CTRL, 0xf, 0xf, false));
}
__device__ __forceinline__ float sum8(float v) {
  v = dpp_add<0xB1>(v);
  v = dpp_add<0x4E>(v);
  return dpp_add<0x141>(v);
}
__device__ __forceinline__ float xsum16_32(float m) {
  auto a = __builtin_amdgcn_permlane32_swap(__float_as_uint(m), __float_as_uint(m), false, false);
  m = __uint_as_float(a[0]) + __uint_as_float(a[1]);
  auto b = __builtin_amdgcn_permlane16_swap(__float_as_uint(m), __float_as_uint(m), false, false);
  return __uint_as_float(b[0]) + __uint_as_float(b[1]);
}
__device__ __forceinline__ float xmax16_32(float m) {
  auto a = __builtin_amdgcn_permlane32_swap(__float_as_uint(m), __float_as_uint(m), false, false);
  m = fmaxf(__uint_as_float(a[0]), __uint_as_float(a[1]));
  auto b = __builtin_amdgcn_permlane16_swap(__float_as_uint(m), __float_as_uint(m), false, false);
  return fmaxf(__uint_as_float(b[0]), __uint_as_float(b[1]));
}
__device__ __forceinline__ float wave_sum(float v) {
  v = sum8(v);
  v = dpp_add<0x140>(v);
  return xsum16_32(v);
}

__global__ __launch_bounds__(256) void prep_kernel(const u16* __restrict__ in, u16* __restrict__ out,
                                                   const char* __restrict__ wqkv, const char* __restrict__ b1,
                                                   const char* __restrict__ b2, unsigned* __restrict__ flags) {
  const int lane = threadIdx.x & 63;
  const int row = blockIdx.x * 4 + (threadIdx.x >> 6);
  const u32x4* src = (const u32x4*)(in + (size_t)row * DM);
  u32x4 a, b;
  asm volatile("global_load_dwordx4 %0, %2, off\n\tglobal_load_dwordx4 %1, %2, off offset:1024"
               : "=&v"(a), "=&v"(b) : "v"(src + lane) : "memory");
  __shared__ unsigned scratch[512];
  const unsigned scr = (unsigned)(uintptr_t)(__attribute__((address_space(3))) unsigned*)scratch +
                       __builtin_amdgcn_readfirstlane(threadIdx.x >> 6) * 512;
  const int gi = blockIdx.x * 256 + threadIdx.x;
  {
    const char* p = gi < 49152 ? wqkv + (size_t)gi * 128
                  : gi < 49152 + 56 ? b1 + (size_t)(gi - 49152) * 128
                  : gi < 49152 + 72 ? b2 + (size_t)(gi - 49152 - 56) * 128 : (const char*)src;
    touch_lds(p, scr);
  }
  {
    unsigned long long pc, pe;
    asm volatile("s_getpc_b64 %0" : "=s"(pc));
    asm volatile("s_getpc_b64 s[40:41]\n\ts_add_u32 s40, s40, zz_text_end@rel32@lo+4\n\t"
                 "s_addc_u32 s41, s41, zz_text_end@rel32@hi+12\n\ts_mov_b64 %0, s[40:41]"
                 : "=s"(pe) : : "s40", "s41", "scc");
    unsigned long long ca = (pc & ~127ull) + (unsigned long long)((blockIdx.x >> 3) * 64 + lane) * 128;
    ca = (threadIdx.x < 64 && blockIdx.x < 128 && ca + 4 <= pe) ? ca : (unsigned long long)src;
    touch_lds((const char*)ca, scr + 256);
  }
  asm volatile("s_waitcnt vmcnt(2)" : "+v"(a), "+v"(b)::"memory");
  float v[16];
#pragma unroll
  for (int i = 0; i < 4; ++i) {
    v[2 * i] = bflo(a[i]);
    v[2 * i + 1] = bfhi(a[i]);
    v[8 + 2 * i] = bflo(b[i]);
    v[8 + 2 * i + 1] = bfhi(b[i]);
  }
  float s = 0.f;
#pragma unroll
  for (int i = 0; i < 16; ++i) s += v[i];
  const float mean = wave_sum(s) * (1.0f / DM);
  float q = 0.f;
#pragma unroll
  for (int i = 0; i < 16; ++i) { v[i] -= mean; q += v[i] * v[i]; }
  const float var = wave_sum(q) * (1.0f / DM);
  const float r = 1.0f / sqrtf(var + LN_EPS);
  u32x4 oa, ob;
#pragma unroll
  for (int i = 0; i < 4; ++i) {
    oa[i] = pk2(v[2 * i] * r, v[2 * i + 1] * r);
    ob[i] = pk2(v[8 + 2 * i] * r, v[8 + 2 * i + 1] * r);
  }
  u32x4* dst = (u32x4*)(out + (size_t)row * DM);
  ST16(dst + lane, oa);
  ST16(dst + lane + 64, ob);
  if (gi < 768) flags[gi * 32] = 0u;
  asm volatile("s_waitcnt vmcnt(0)" ::: "memory");
}

__device__ __forceinline__ float rowmax17(const f32x4* s) {
  float a = fmaxf(fmaxf(s[0][0], s[0][1]), fmaxf(s[0][2], s[0][3]));
#pragma unroll
  for (int t = 1; t < 17; ++t) {
    a = fmaxf(fmaxf(a, s[t][0]), s[t][1]);
    a = fmaxf(fmaxf(a, s[t][2]), s[t][3]);
  }
  return a;
}

template <bool EARLY>
__device__ __forceinline__ void attn_body(const char* ks, const char* vs, bf16x8 qf, int wid, int lane, int tmin,
                                          char* op) {
  const int fr = lane & 15, g = lane >> 4;
  const float NEG = -1e30f;
  f32x4 s[17];
  const char* kp = ks + (wid * 16 + fr) * 64 + ((g ^ ((4 - ((fr >> 2) & 3)) & 3)) << 4);
#pragma unroll
  for (int t = 0; t < 17; ++t) {
    bf16x8 kf = *(const bf16x8*)(kp + t * 1024);
    s[t] = __builtin_amdgcn_mfma_f32_16x16x32_bf16(kf, qf, f32x4{0.f, 0.f, 0.f, 0.f}, 0, 0, 0);
  }
#pragma unroll
  for (int r = 0; r < 4; ++r) {
    s[0][r] = (4 * g + r > fr) ? s[0][r] : NEG;
    s[16][r] = (4 * g + r <= fr) ? s[16][r] : NEG;
  }
  if (EARLY) {
#pragma unroll
    for (int t = 0; t < 16; ++t)
      if (t < tmin) s[t] = f32x4{NEG, NEG, NEG, NEG};
  }
  const float mx = xmax16_32(rowmax17(s));
  const float c = 0.17677669529663687f * 1.4426950408889634f;
  const f32x2 c2 = {c, c};
  const f32x2 nmc2 = {-mx * c, -mx * c};
  f32x2 sum2 = {0.f, 0.f};
  unsigned pk[18][2];
#pragma unroll
  for (int t = 0; t < 17; ++t) {
#pragma unroll
    for (int hh = 0; hh < 2; ++hh) {
      const f32x2 z = f32x2{s[t][2 * hh], s[t][2 * hh + 1]} * c2 + nmc2;
      const f32x2 p = {__builtin_amdgcn_exp2f(z[0]), __builtin_amdgcn_exp2f(z[1])};
      sum2 += p;
      pk[t][hh] = pk2(p[0], p[1]);
    }
  }
  float sum = sum2[0] + sum2[1];
  pk[17][0] = 0u;
  pk[17][1] = 0u;
  sum = xsum16_32(sum);
  const int q4 = (lane >> 2) & 3, pp = lane & 3;
  const char* vp0 = vs + (wid * 16 + 4 * g + q4) * 64 + ((g & 1) << 5) + pp * 8;
  const char* vp1 = vs + (wid * 16 + 4 * g + q4) * 64 + (((g & 1) ^ 1) << 5) + pp * 8;
  f32x4 o0 = {0.f, 0.f, 0.f, 0.f}, o1 = {0.f, 0.f, 0.f, 0.f};
#pragma unroll
  for (int tp = 0; tp < 9; ++tp) {
    const int t0 = 2 * tp, t1 = (2 * tp + 1 < 17) ? 2 * tp + 1 : 16;
    u32x4 pb = {pk[2 * tp][0], pk[2 * tp][1], pk[2 * tp + 1][0], pk[2 * tp + 1][1]};
    bf16x8 pf = __builtin_bit_cast(bf16x8, pb);
    s16x4 a0 = vtr((lds_cptr)(vp0 + t0 * 1024)), a1 = vtr((lds_cptr)(vp0 + t1 * 1024));
    s16x4 b0 = vtr((lds_cptr)(vp1 + t0 * 1024)), b1 = vtr((lds_cptr)(vp1 + t1 * 1024));
    o0 = __builtin_amdgcn_mfma_f32_16x16x32_bf16(__builtin_shufflevector(a0, a1, 0, 1, 2, 3, 4, 5, 6, 7), pf, o0, 0, 0, 0);
    o1 = __builtin_amdgcn_mfma_f32_16x16x32_bf16(__builtin_shufflevector(b0, b1, 0, 1, 2, 3, 4, 5, 6, 7), pf, o1, 0, 0, 0);
  }
  const float inv = 1.0f / sum;
  u32x2 w0, w1;
  w0[0] = pk2(o0[0] * inv, o0[1] * inv);
  w0[1] = pk2(o0[2] * inv, o0[3] * inv);
  w1[0] = pk2(o1[0] * inv, o1[1] * inv);
  w1[1] = pk2(o1[2] * inv, o1[3] * inv);
  *(u32x2*)(op + 8 * g) = w0;
  *(u32x2*)(op + 32 + 8 * g) = w1;
}


enum { EPI_QKV = 0, EPI_RES_STATS = 1, EPI_LN_GELU = 2, EPI_SPLIT_BIAS_RES = 3 };
constexpr int NLW = 4;

template <int CNT, int NBUF, int NPA, int STAGE>
__device__ __forceinline__ void loader_loop(const char* Ab, const char* Wb, const unsigned* voff, int lw, int nk,
                                            unsigned wstep, unsigned lds0) {
  const char* sp[CNT];
  unsigned st[CNT];
#pragma unroll
  for (int i = 0; i < CNT; ++i) {
    const bool isA = (lw + i * NLW) < NPA;
    sp[i] = isA ? Ab : Wb;
    st[i] = isA ? 128u : wstep;
  }
  auto issue = [&](int buf) {
#pragma unroll
    for (int i = 0; i < CNT; ++i) {
      glds16(sp[i], voff[i], lds0 + buf * STAGE + (lw + i * NLW) * 1024);
      sp[i] += st[i];
    }
  };
  issue(0);
  issue(1);
  asm volatile("s_waitcnt vmcnt(%0)" ::"i"(CNT) : "memory");
  __builtin_amdgcn_s_barrier();
#pragma unroll
  for (int s = 2; s < NBUF; ++s) issue(s);
  int buf = 0;
  for (int t = 0; t < nk; ++t) {
    if (t + NBUF <= nk) {
      asm volatile("s_waitcnt vmcnt(%0)" ::"i"((NBUF - 2) * CNT) : "memory");
    } else {
      asm volatile("s_waitcnt vmcnt(0)" ::: "memory");
    }
    __builtin_amdgcn_s_barrier();
    if (t + NBUF < nk) issue(buf);
    buf = (buf + 1 == NBUF) ? 0 : buf + 1;
  }
}

template <int BN, int NBUF, int EPI, int XA, int TL0 = 0, int TL1 = 0>
__device__ __forceinline__ void gemm_body(const char* __restrict__ tp0, const char* __restrict__ tp1, const u16* __restrict__ A, const u16* __restrict__ W,
                                          u16* __restrict__ C, const u16* __restrict__ bias,
                                          const u16* __restrict__ res, float* __restrict__ aux0,
                                          float* __restrict__ aux1, unsigned* __restrict__ flags,
                                          const int N, const int K) {
  constexpr int BM = 128;
  constexpr bool WIDE = BN > 64;
  constexpr bool SPLIT = (EPI == EPI_SPLIT_BIAS_RES);
  constexpr int NTHR = 512;
  constexpr int NG = BN / 64;
  constexpr bool HALF = (BN % 64) != 0;
  constexpr int NT = WIDE ? BN / 32 : 4;
  constexpr int ABYTES = BM * 128, WBYTES = 64 * BN * 2, STAGE = ABYTES + WBYTES;
  constexpr int NPA = ABYTES / 1024, NP = STAGE / 1024;
  constexpr int CMAX = (NP + NLW - 1) / NLW;
  constexpr int TSCR = NBUF * STAGE + 4096;
  constexpr int SIDE = NBUF * STAGE;
  extern __shared__ __attribute__((aligned(16))) char smem[];

  const int tid = threadIdx.x, lane = tid & 63;
  const int wid = __builtin_amdgcn_readfirstlane(tid >> 6);
  const int xc = blockIdx.x & 7, rr = blockIdx.x >> 3;
  int bm, bn, hk = 0;
  if (SPLIT) {
    hk = rr & 1;
    const int tt = rr >> 1;
    bm = (xc & 3) * 4 + (tt & 3);
    bn = (xc >> 2) * 4 + (tt >> 2);
  } else {
    constexpr int XB = 32 / XA, PM = 16 / XA;
    bm = (xc % PM) * XA + (rr % XA);
    bn = (xc / PM) * XB + (rr / XA);
  }
  const int m0 = bm * BM, n0 = bn * BN;
  const int nk = SPLIT ? K / 128 : K / 64;
  const int kbeg = hk * (K / 2);
  const int odd = HALF ? (bn & 1) : 0;
  const int cfull = odd ? 32 : 0, chalf = odd ? 0 : NG * 64;
  const unsigned lds0 = (unsigned)(uintptr_t)(__attribute__((address_space(3))) char*)smem;
  const int fr = lane & 15, g = lane >> 4;

  f32x4 acc[4][NT];
  const int wm = wid & 1, wx = (wid >> 1) & 1;
  int tcol[NT];

  if (wid >= 4) {
    const int lw = wid - 4;
    unsigned voff[CMAX];
#pragma unroll
    for (int i = 0; i < CMAX; ++i) {
      const int idx = lw + i * NLW;
      if (idx < NPA) {
        const int row = idx * 8 + (lane >> 3), ch = (lane & 7) ^ (row & 7);
        voff[i] = (unsigned)(((m0 + row) * K + ch * 8) * 2);
      } else if (idx < NPA + NG * 8) {
        const int w = idx - NPA, G = w >> 3, k = (w & 7) * 8 + (lane >> 3);
        const int ch = (lane & 7) ^ ((k & 2) | ((k >> 1) & 4));
        const int col = (EPI == EPI_QKV) ? G * DM + bn * 64 : n0 + cfull + G * 64;
        voff[i] = (unsigned)((k * N + col + ch * 8) * 2);
      } else {
        const int w = idx - NPA - NG * 8, k = w * 16 + (lane >> 2);
        const int ch = (lane & 3) ^ ((k >> 2) & 2);
        voff[i] = (unsigned)((k * N + n0 + chalf + ch * 8) * 2);
      }
    }
    const unsigned wstep = (unsigned)(64 * N * 2);
    const char* Ab = (const char*)(A + kbeg);
    const char* Wb = (const char*)(W + (size_t)kbeg * N);
    if (lw + (CMAX - 1) * NLW < NP)
      loader_loop<CMAX, NBUF, NPA, STAGE>(Ab, Wb, voff, lw, nk, wstep, lds0);
    else
      loader_loop<CMAX - 1, NBUF, NPA, STAGE>(Ab, Wb, voff, lw, nk, wstep, lds0);
  } else {
    if (EPI == EPI_LN_GELU) {
      if (tid < 128) {
        const f32x4* sp = (const f32x4*)(aux0 + (size_t)(m0 + tid) * 32);
        f32x4 p[8];
#pragma unroll
        for (int i = 0; i < 8; ++i) p[i] = sp[i];
        float mean = 0.f;
#pragma unroll
        for (int i = 0; i < 8; ++i) mean += p[i][0] + p[i][2];
        mean *= (1.0f / 16.0f);
        float m2 = 0.f;
#pragma unroll
        for (int i = 0; i < 8; ++i) {
          const float d0 = p[i][0] - mean, d1 = p[i][2] - mean;
          m2 += p[i][1] + p[i][3] + 64.0f * (d0 * d0 + d1 * d1);
        }
        const float var = m2 * (1.0f / DM);
        const float r1 = 1.0f / sqrtf(var + LN_EPS);
        const float r2 = 1.0f / sqrtf(var * r1 * r1 + LN_EPS);
        *(f32x2*)(smem + SIDE + tid * 8) = f32x2{mean, r1 * r2};
      } else if (tid - 128 < BN / 2) {
        const int c = (tid - 128) * 2;
        f32x2 cs = {0.f, 0.f};
#pragma unroll
        for (int kc = 0; kc < 16; ++kc) cs += *(const f32x2*)(aux1 + (size_t)kc * FF + n0 + c);
        const unsigned bb = *(const unsigned*)(bias + n0 + c);
        *(f32x4*)(smem + SIDE + 1024 + c * 8) = f32x4{cs[0], bflo(bb), cs[1], bfhi(bb)};
      }
    }
    const int q = (lane >> 2) & 3, pp = lane & 3;
    const int wk = WIDE ? 0 : wx;
    int aoff[2];
#pragma unroll
    for (int ks = 0; ks < 2; ++ks) aoff[ks] = (wm * 64 + fr) * 128 + (((ks * 4 + g) ^ (fr & 7)) << 4);
    int tb[NT];
    const int f1 = (q & 2) | ((g & 1) << 2);
#pragma unroll
    for (int j = 0; j < NT; ++j) {
      int G, tp;
      if (!WIDE) { G = 0; tp = j; }
      else if (EPI == EPI_QKV) { G = (j >> 1) ^ 2; G = G == 3 ? 1 : G; tp = wx * 2 + (j & 1); }
      else if (j < 4) { G = wx * (NG - 1); tp = j; }
      else { G = 1; tp = wx * 2 + (j - 4); }
      if (WIDE && HALF && j == NT - 1) {
        tb[j] = ABYTES + NG * 8192 + (8 * g + q) * 64 + (((2 * wx + (pp >> 1)) ^ ((g & 1) << 1)) << 4) + (pp & 1) * 8;
        tcol[j] = chalf + wx * 16;
      } else {
        tb[j] = ABYTES + G * 8192 + (8 * g + q) * 128 + (((2 * tp + (pp >> 1)) ^ f1) << 4) + (pp & 1) * 8;
        tcol[j] = cfull + G * 64 + tp * 16;
      }
    }
#pragma unroll
    for (int i = 0; i < 4; ++i)
#pragma unroll
      for (int j = 0; j < NT; ++j) acc[i][j] = f32x4{0.f, 0.f, 0.f, 0.f};

    bf16x8 xa[4], wa[NT], xb[4], wb[NT];
    auto rd = [&](bf16x8* xf, bf16x8* wf, int buf, int ks) {
      const char* base = smem + buf * STAGE;
#pragma unroll
      for (int j = 0; j < NT; ++j) {
        const int rs = (WIDE && HALF && j == NT - 1) ? 64 : 128;
        s16x4 lo = vtr((lds_cptr)(base + tb[j] + ks * 32 * rs));
        s16x4 hi = vtr((lds_cptr)(base + tb[j] + ks * 32 * rs + 4 * rs));
        wf[j] = __builtin_shufflevector(lo, hi, 0, 1, 2, 3, 4, 5, 6, 7);
      }
#pragma unroll
      for (int i = 0; i < 4; ++i) xf[i] = *(const bf16x8*)(base + aoff[ks] + i * 2048);
    };
    auto mm = [&](const bf16x8* xf, const bf16x8* wf) {
#pragma unroll
      for (int i = 0; i < 4; ++i)
#pragma unroll
        for (int j = 0; j < NT; ++j)
          acc[i][j] = __builtin_amdgcn_mfma_f32_16x16x32_bf16(wf[j], xf[i], acc[i][j], 0, 0, 0);
    };
#define INTERLEAVE() do { _Pragma("unroll") for (int z_ = 0; z_ < 2 * NT + 4; ++z_) { __builtin_amdgcn_sched_group_barrier(0x8, 1, 0); __builtin_amdgcn_sched_group_barrier(0x100, 1, 0); } __builtin_amdgcn_sched_group_barrier(0x8, 4 * NT - (2 * NT + 4), 0); } while (0)
    __builtin_amdgcn_s_waitcnt(0xC07F);
    __builtin_amdgcn_s_barrier();
    auto do_touch = [&](int i) {
      const int line = ((blockIdx.x * 4 + wid) * 2 + i) * 64 + lane;
      if (__builtin_amdgcn_readfirstlane(line) < TL0 + TL1) {
        const int lc = line < TL0 + TL1 ? line : TL0 + TL1 - 1;
        const char* p = lc < TL0 ? tp0 + (size_t)lc * 128 : tp1 + (size_t)(lc - TL0) * 128;
        unsigned sv;
        asm volatile("s_mov_b32 %0, m0\n\ts_mov_b32 m0, %2\n\ts_nop 0\n\tglobal_load_lds_dword %1, off\n\ts_mov_b32 m0, %0"
                     : "=&s"(sv) : "v"(p), "s"(lds0 + TSCR + wid * 256) : "memory");
      }
    };
    if (WIDE) {
      rd(xa, wa, 0, 0);
      int b0 = 0;
      for (int t = 0; t < nk; ++t) {
        if (TL0 + TL1 > 0) { if (t == 2) do_touch(0); if (t == 7) do_touch(1); }
        rd(xb, wb, b0, 1);
        b0 = (b0 + 1 == NBUF) ? 0 : b0 + 1;
        mm(xa, wa);
        INTERLEAVE();
        __builtin_amdgcn_sched_barrier(0);
        __builtin_amdgcn_s_waitcnt(0xC07F);
        __builtin_amdgcn_s_barrier();
        rd(xa, wa, b0, 0);
        mm(xb, wb);
        INTERLEAVE();
        __builtin_amdgcn_sched_barrier(0);
      }
    } else {
      rd(xa, wa, 0, wk);
      int b1 = 1;
      for (int t = 0; t < nk; t += 2) {
        __builtin_amdgcn_s_waitcnt(0xC07F);
        __builtin_amdgcn_s_barrier();
        rd(xb, wb, b1, wk);
        b1 = (b1 + 1 == NBUF) ? 0 : b1 + 1;
        mm(xa, wa);
        INTERLEAVE();
        __builtin_amdgcn_sched_barrier(0);
        __builtin_amdgcn_s_waitcnt(0xC07F);
        __builtin_amdgcn_s_barrier();
        rd(xa, wa, b1, wk);
        b1 = (b1 + 1 == NBUF) ? 0 : b1 + 1;
        mm(xb, wb);
        INTERLEAVE();
        __builtin_amdgcn_sched_barrier(0);
      }
    }
    __builtin_amdgcn_s_waitcnt(0xC07F);
    if (TL0 + TL1 > 0) asm volatile("s_waitcnt vmcnt(0)" ::: "memory");
    if (SPLIT) {
      const int tile = bm * 8 + bn;
      const auto rsrc = __builtin_amdgcn_make_buffer_rsrc(aux0, 0, 128 * 2 * 2 * 16384, 0x00020000);
      if (wm != hk) {
        const int slot = ((tile * 2 + hk) * 2 + wx);
#pragma unroll
        for (int i = 0; i < 4; ++i)
#pragma unroll
          for (int j = 0; j < NT; ++j)
            __builtin_amdgcn_raw_buffer_store_b128(__builtin_bit_cast(u32x4, acc[i][j]), rsrc,
                                                   slot * 16384 + (i * NT + j) * 1024 + lane * 16, 0, 16);
        asm volatile("s_waitcnt vmcnt(0)" ::: "memory");
        if (lane == 0) __hip_atomic_store(flags + slot * 32, 1u, __ATOMIC_RELAXED, __HIP_MEMORY_SCOPE_AGENT);
      } else {
        const int slot = ((tile * 2 + (hk ^ 1)) * 2 + wx);
        for (int spin = 0; spin < (1 << 24); ++spin) {
          if (__hip_atomic_load(flags + slot * 32, __ATOMIC_RELAXED, __HIP_MEMORY_SCOPE_AGENT) != 0u) break;
          __builtin_amdgcn_s_sleep(2);
        }
        asm volatile("" ::: "memory");
#pragma unroll
        for (int i = 0; i < 4; ++i) {
          u32x4 t[NT];
#pragma unroll
          for (int j = 0; j < NT; ++j)
            t[j] = __builtin_amdgcn_raw_buffer_load_b128(rsrc, slot * 16384 + (i * NT + j) * 1024 + lane * 16, 0, 16);
#pragma unroll
          for (int j = 0; j < NT; ++j) acc[i][j] += __builtin_bit_cast(f32x4, t[j]);
        }
      }
    }
  }
  __syncthreads();

  constexpr int CPR = BN / 8;
  if (EPI == EPI_QKV) {
    constexpr int KIMG = 0, VIMG = 49152, QIMG = 98304, RED = 114688, HIMG = 24576;
    u16* kvg = (u16*)aux0;
    u32x4 cw[8];
    const int t2 = tid - 256;
    const int cb = blockIdx.x < 224 ? blockIdx.x : 223;
    const int cg = cb % 14, kc = cb / 14;
    if (wid < 4) {
      auto put = [&](int j) {
        const int grp = tcol[j] >> 6, hd = (tcol[j] >> 5) & 1;
        const int dd = (tcol[j] & 31) + g * 4, chn = dd >> 3, hf = (dd >> 2) & 1;
        const int sw = (grp == 2) ? (((fr >> 2) & 1) << 1) : ((4 - ((fr >> 2) & 3)) & 3);
        const int base = (grp == 0) ? QIMG + hd * 8192 : ((grp == 1) ? KIMG : VIMG) + hd * HIMG + 256 * 64;
#pragma unroll
        for (int i = 0; i < 4; ++i) {
          u32x2 o;
          o[0] = pk2(acc[i][j][0], acc[i][j][1]);
          o[1] = pk2(acc[i][j][2], acc[i][j][3]);
          *(u32x2*)(smem + base + (wm * 64 + i * 16 + fr) * 64 + ((chn ^ sw) << 4) + hf * 8) = o;
        }
      };
#pragma unroll
      for (int j = 0; j < 4; ++j) put(j);
      if (bm < 15) {
#pragma unroll
        for (int kv = 0; kv < 2; ++kv)
#pragma unroll
          for (int pc = 0; pc < 4; ++pc) {
            const int row = wm * 64 + pc * 16 + (lane >> 2), slot = lane & 3;
            const int sw = kv ? (((row >> 2) & 1) << 1) : ((4 - ((row >> 2) & 3)) & 3);
            const u32x4 v = *(const u32x4*)(smem + (kv ? VIMG : KIMG) + wx * HIMG + (256 + row) * 64 + slot * 16);
            ST16(kvg + ((size_t)(kv * NH + bn * 2 + wx) * S_LEN + m0 + row) * 32 + (slot ^ sw) * 8, v);
          }
      }
      put(4);
      put(5);
      if (bm < 15) {
        asm volatile("s_waitcnt vmcnt(0)" ::: "memory");
        if (lane == 0)
          __hip_atomic_fetch_add(flags + (bm * 16 + bn) * 32, 1u, __ATOMIC_RELAXED, __HIP_MEMORY_SCOPE_AGENT);
      }
    } else {
      const u16* cp = res + (size_t)(kc * 64 + (t2 >> 5) * 8) * FF + cg * 256 + (t2 & 31) * 8;
#pragma unroll
      for (int i = 0; i < 8; ++i) cw[i] = *(const u32x4*)(cp + (size_t)i * FF);
      const int lw = wid - 4, pr = lw >> 1, kv = lw & 1;
      const int pb = bm - 2 + pr;
      const unsigned dst = lds0 + (kv ? VIMG : KIMG) + pr * 128 * 64;
      if (pb >= 0) {
        const unsigned* fp = flags + (pb * 16 + bn) * 32;
        for (int spin = 0; spin < (1 << 24); ++spin) {
          if (__hip_atomic_load(fp, __ATOMIC_RELAXED, __HIP_MEMORY_SCOPE_AGENT) >= 4u) break;
          __builtin_amdgcn_s_sleep(2);
        }
        asm volatile("" ::: "memory");
        const int kl = lane >> 2, slot = lane & 3;
        const int sw = kv ? (((kl >> 2) & 1) << 1) : ((4 - ((kl >> 2) & 3)) & 3);
        const unsigned vo = (unsigned)((kl * 32 + (slot ^ sw) * 8) * 2);
#pragma unroll
        for (int hd = 0; hd < 2; ++hd) {
          const u16* src = kvg + ((size_t)(kv * NH + bn * 2 + hd) * S_LEN + pb * 128) * 32;
#pragma unroll
          for (int pc = 0; pc < 8; ++pc) glds16_sc1(src + pc * 512, vo, dst + hd * HIMG + pc * 1024);
        }
      } else {
#pragma unroll
        for (int hd = 0; hd < 2; ++hd)
#pragma unroll
          for (int pc = 0; pc < 8; ++pc)
            *(u32x4*)(smem + (kv ? VIMG : KIMG) + pr * 128 * 64 + hd * HIMG + pc * 1024 + lane * 16) = u32x4{0u, 0u, 0u, 0u};
      }
      {
        float* red = (float*)(smem + RED);
        float sacc[8];
#pragma unroll
        for (int e = 0; e < 8; ++e) sacc[e] = 0.f;
#pragma unroll
        for (int i = 0; i < 8; ++i)
#pragma unroll
          for (int e = 0; e < 4; ++e) { sacc[2 * e] += bflo(cw[i][e]); sacc[2 * e + 1] += bfhi(cw[i][e]); }
        *(f32x4*)(red + (t2 >> 5) * 256 + (t2 & 31) * 8) = f32x4{sacc[0], sacc[1], sacc[2], sacc[3]};
        *(f32x4*)(red + (t2 >> 5) * 256 + (t2 & 31) * 8 + 4) = f32x4{sacc[4], sacc[5], sacc[6], sacc[7]};
      }
      asm volatile("s_waitcnt vmcnt(0)" ::: "memory");
    }
    __syncthreads();
    if (wid < 4) {
      const int li = (rr * 4 + wid) * 64 + lane;
      const char* p = tp1 + (size_t)(li >> 3) * 2048 + (xc >> 2) * 1024 + (li & 7) * 128;
      unsigned sv;
      asm volatile("s_mov_b32 %0, m0\n\ts_mov_b32 m0, %2\n\ts_nop 0\n\tglobal_load_lds_dword %1, off\n\ts_mov_b32 m0, %0"
                   : "=&s"(sv) : "v"(p), "s"(lds0 + TSCR + wid * 256) : "memory");
    }
    if (blockIdx.x < 224 && tid < 256) {
      const float* red = (const float*)(smem + RED);
      float t = 0.f;
#pragma unroll
      for (int i = 0; i < 8; ++i) t += red[i * 256 + tid];
      aux1[(size_t)kc * FF + cg * 256 + tid] = t;
    }
    const int qw = m0 + wid * 16;
    const int tmin = (qw < WIN) ? (WIN - qw) >> 4 : 0;
    const int qo = (wid * 16 + fr) * 64 + ((g ^ ((4 - ((fr >> 2) & 3)) & 3)) << 4);
    const bf16x8 qf0 = *(const bf16x8*)(smem + QIMG + qo), qf1 = *(const bf16x8*)(smem + QIMG + 8192 + qo);
    char* ost = smem + QIMG + wid * 1024;
#pragma unroll 1
    for (int hd = 0; hd < 2; ++hd) {
      const bf16x8 qf = hd ? qf1 : qf0;
      char* op = ost + hd * 8192 + fr * 64;
      if (bm < 2) attn_body<true>(smem + KIMG + hd * HIMG, smem + VIMG + hd * HIMG, qf, wid, lane, tmin, op);
      else attn_body<false>(smem + KIMG + hd * HIMG, smem + VIMG + hd * HIMG, qf, wid, lane, 0, op);
    }
#pragma unroll
    for (int i = 0; i < 2; ++i) {
      const int row = i * 8 + (lane >> 3), ch = lane & 7;
      const u32x4 v = *(const u32x4*)(ost + (ch >> 2) * 8192 + row * 64 + (ch & 3) * 16);
      ST16(C + (size_t)(qw + row) * DM + bn * 64 + ch * 8, v);
    }
  } else if (EPI == EPI_LN_GELU) {
    constexpr int RS = BN * 4 + 16;
    if (wid < 4) {
#pragma unroll
      for (int j = 0; j < NT; ++j)
#pragma unroll
        for (int i = 0; i < 4; ++i)
          *(f32x4*)(smem + (wm * 64 + i * 16 + fr) * RS + (tcol[j] + g * 4) * 4) = acc[i][j];
    }
    __syncthreads();
#pragma unroll
    for (int c = tid; c < BM * CPR; c += NTHR) {
      const int row = c / CPR, ch = c % CPR;
      const f32x4 a0 = *(const f32x4*)(smem + row * RS + ch * 32);
      const f32x4 a1 = *(const f32x4*)(smem + row * RS + ch * 32 + 16);
      const f32x2 rs = *(const f32x2*)(smem + SIDE + row * 8);
      float v[8];
#pragma unroll
      for (int r = 0; r < 4; ++r) {
        const f32x4 cb = *(const f32x4*)(smem + SIDE + 1024 + ch * 64 + r * 16);
        const float x0 = (r < 2) ? a0[2 * r] : a1[2 * r - 4];
        const float x1 = (r < 2) ? a0[2 * r + 1] : a1[2 * r - 3];
        v[2 * r] = (x0 - rs[0] * cb[0]) * rs[1] + cb[1];
        v[2 * r + 1] = (x1 - rs[0] * cb[2]) * rs[1] + cb[3];
      }
      u32x4 o;
#pragma unroll
      for (int r = 0; r < 4; ++r) {
        const f32x2 x = {v[2 * r], v[2 * r + 1]};
        const f32x2 x2 = x * x;
        const f32x2 w = x2 * f32x2{-0.10294324f, -0.10294324f} + f32x2{-2.302208198f, -2.302208198f};
        const f32x2 z = x * w;
        f32x2 e = {__builtin_amdgcn_exp2f(z[0]), __builtin_amdgcn_exp2f(z[1])};
        e = e + f32x2{1.0f, 1.0f};
        const f32x2 y = x * f32x2{__builtin_amdgcn_rcpf(e[0]), __builtin_amdgcn_rcpf(e[1])};
        o[r] = pk2(y[0], y[1]);
      }
      ST16(C + (size_t)(m0 + row) * N + n0 + ch * 8, o);
    }
  } else if (EPI == EPI_RES_STATS) {
    constexpr int RS = BN * 4 + 16;
    constexpr int PART = BM * RS;
    if (wid < 4) {
#pragma unroll
      for (int i = 0; i < 4; ++i)
#pragma unroll
        for (int j = 0; j < NT; ++j)
          *(f32x4*)(smem + wx * PART + (wm * 64 + i * 16 + fr) * RS + (j * 16 + g * 4) * 4) = acc[i][j];
    }
    __syncthreads();
#pragma unroll
    for (int c = tid; c < BM * CPR; c += NTHR) {
      const int row = c / CPR, ch = c % CPR;
      const f32x4 a0 = *(const f32x4*)(smem + row * RS + ch * 32);
      const f32x4 a1 = *(const f32x4*)(smem + row * RS + ch * 32 + 16);
      const f32x4 b0 = *(const f32x4*)(smem + PART + row * RS + ch * 32);
      const f32x4 b1 = *(const f32x4*)(smem + PART + row * RS + ch * 32 + 16);
      const u32x4 rv = *(const u32x4*)(res + (size_t)(m0 + row) * N + n0 + ch * 8);
      u32x4 o;
      o[0] = pk2(a0[0] + b0[0] + bflo(rv[0]), a0[1] + b0[1] + bfhi(rv[0]));
      o[1] = pk2(a0[2] + b0[2] + bflo(rv[1]), a0[3] + b0[3] + bfhi(rv[1]));
      o[2] = pk2(a1[0] + b1[0] + bflo(rv[2]), a1[1] + b1[1] + bfhi(rv[2]));
      o[3] = pk2(a1[2] + b1[2] + bflo(rv[3]), a1[3] + b1[3] + bfhi(rv[3]));
      ST16(C + (size_t)(m0 + row) * N + n0 + ch * 8, o);
      float s = 0.f;
#pragma unroll
      for (int r = 0; r < 4; ++r) s += bflo(o[r]) + bfhi(o[r]);
      const float mean = sum8(s) * (1.0f / 64.0f);
      float m2 = 0.f;
#pragma unroll
      for (int r = 0; r < 4; ++r) {
        const float d0 = bflo(o[r]) - mean, d1 = bfhi(o[r]) - mean;
        m2 += d0 * d0 + d1 * d1;
      }
      m2 = sum8(m2);
      if (ch == 0) *(f32x2*)(aux0 + ((size_t)(m0 + row) * 16 + bn) * 2) = f32x2{mean, m2};
    }
  } else {
    constexpr int RS = BN * 4 + 16;
    if (wid < 4 && wm == hk) {
#pragma unroll
      for (int i = 0; i < 4; ++i)
#pragma unroll
        for (int j = 0; j < NT; ++j)
          *(f32x4*)(smem + (i * 16 + fr) * RS + (tcol[j] + g * 4) * 4) = acc[i][j];
    }
    __syncthreads();
#pragma unroll
    for (int c = tid; c < 64 * CPR; c += NTHR) {
      const int row = c / CPR, ch = c % CPR;
      const f32x4 a0 = *(const f32x4*)(smem + row * RS + ch * 32);
      const f32x4 a1 = *(const f32x4*)(smem + row * RS + ch * 32 + 16);
      const u32x4 bb = *(const u32x4*)(bias + n0 + ch * 8);
      const size_t go = (size_t)(m0 + hk * 64 + row) * N + n0 + ch * 8;
      const u32x4 rv = *(const u32x4*)(res + go);
      u32x4 o;
      o[0] = pk2(a0[0] + bflo(bb[0]) + bflo(rv[0]), a0[1] + bfhi(bb[0]) + bfhi(rv[0]));
      o[1] = pk2(a0[2] + bflo(bb[1]) + bflo(rv[1]), a0[3] + bfhi(bb[1]) + bfhi(rv[1]));
      o[2] = pk2(a1[0] + bflo(bb[2]) + bflo(rv[2]), a1[1] + bfhi(bb[2]) + bfhi(rv[2]));
      o[3] = pk2(a1[2] + bflo(bb[3]) + bflo(rv[3]), a1[3] + bfhi(bb[3]) + bfhi(rv[3]));
      ST16(C + go, o);
    }
  }
  asm volatile("s_waitcnt vmcnt(0)" ::: "memory");
}

__global__ __launch_bounds__(512, 1) void qkv_attn_kernel(const u16* __restrict__ A, const u16* __restrict__ W,
                                                         u16* __restrict__ o, const char* __restrict__ wo,
                                                         const u16* __restrict__ w1, float* __restrict__ kvg,
                                                         float* __restrict__ cspart, unsigned* __restrict__ flags) {
  gemm_body<192, 3, EPI_QKV, 8, 57344, 0>((const char*)w1, wo, A, W, o, nullptr, w1, kvg, cspart, flags, 3 * DM, DM);
}
__global__ __launch_bounds__(512, 1) void gemm_wo_kernel(const u16* __restrict__ A, const u16* __restrict__ W,
                                                        u16* __restrict__ C, const u16* __restrict__ res,
                                                        float* __restrict__ stats) {
  gemm_body<64, 6, EPI_RES_STATS, 4>(nullptr, nullptr, A, W, C, nullptr, res, stats, nullptr, nullptr, DM, DM);
}
__global__ __launch_bounds__(512, 1) void gemm_w1_kernel(const u16* __restrict__ A, const u16* __restrict__ W,
                                                        u16* __restrict__ C, const u16* __restrict__ bias,
                                                        float* __restrict__ stats, float* __restrict__ cspart,
                                                        const char* __restrict__ t0) {
  gemm_body<224, 3, EPI_LN_GELU, 8, 57344, 0>(t0, t0, A, W, C, bias, nullptr, stats, cspart, nullptr, FF, DM);
}
__global__ __launch_bounds__(512, 1) void gemm_w2_kernel(const u16* __restrict__ A, const u16* __restrict__ W,
                                                        u16* __restrict__ C, const u16* __restrict__ bias,
                                                        const u16* __restrict__ res, float* __restrict__ xchg,
                                                        unsigned* __restrict__ flags) {
  gemm_body<128, 4, EPI_SPLIT_BIAS_RES, 4>(nullptr, nullptr, A, W, C, bias, res, xchg, nullptr, flags, DM, FF);
}
extern "C" __global__ void zz_text_end() {}

constexpr int lds_bytes(int BN, int NBUF, bool side) { return NBUF * (128 * 64 * 2 + 64 * BN * 2) + (side ? 5120 : 0); }

extern "C" void kernel_launch(void* const* d_in, const int* in_sizes, int n_in, void* d_out, int out_size,
                              void* d_ws, size_t ws_size, hipStream_t stream) {
  const u16* x = (const u16*)d_in[0];
  const u16* wqkv = (const u16*)d_in[1];
  const u16* wo = (const u16*)d_in[2];
  const u16* w1 = (const u16*)d_in[4];
  const u16* b1 = (const u16*)d_in[5];
  const u16* w2 = (const u16*)d_in[6];
  const u16* b2 = (const u16*)d_in[7];
  u16* out = (u16*)d_out;
  char* ws = (char*)d_ws;
  u16* xn = (u16*)(ws);
  float* xchg = (float*)(ws);
  u16* h = (u16*)(ws + (8u << 20));
  u16* qkv = (u16*)(ws + (12u << 20));
  u16* o = (u16*)(ws + (20u << 20));
  u16* hh = (u16*)(ws + (12u << 20));
  float* stats = (float*)(ws + (26u << 20));
  float* cspart = (float*)(ws + (26u << 20) + (256u << 10));
  unsigned* flags = (unsigned*)(ws + (26u << 20) + (512u << 10));

  (void)hipFuncSetAttribute((const void*)qkv_attn_kernel, hipFuncAttributeMaxDynamicSharedMemorySize, lds_bytes(192, 3, true));
  (void)hipFuncSetAttribute((const void*)gemm_wo_kernel, hipFuncAttributeMaxDynamicSharedMemorySize, lds_bytes(64, 6, false));
  (void)hipFuncSetAttribute((const void*)gemm_w1_kernel, hipFuncAttributeMaxDynamicSharedMemorySize, lds_bytes(224, 3, true));
  (void)hipFuncSetAttribute((const void*)gemm_w2_kernel, hipFuncAttributeMaxDynamicSharedMemorySize, lds_bytes(128, 4, false));
  prep_kernel<<<S_LEN / 4, 256, 0, stream>>>(x, xn, (const char*)wqkv, (const char*)b1, (const char*)b2, flags);
  qkv_attn_kernel<<<256, 512, lds_bytes(192, 3, true), stream>>>(xn, wqkv, o, (const char*)wo, w1, (float*)qkv, cspart, flags + 512 * 32);
  gemm_wo_kernel<<<256, 512, lds_bytes(64, 6, false), stream>>>(o, wo, h, x, stats);
  gemm_w1_kernel<<<256, 512, lds_bytes(224, 3, true), stream>>>(h, w1, hh, b1, stats, cspart, (const char*)w2);
  gemm_w2_kernel<<<256, 512, lds_bytes(128, 4, false), stream>>>(hh, w2, out, b2, h, xchg, flags);
}
```

```cpp
#include <hip/hip_runtime.h>
#include <hip/hip_bf16.h>
#include <stdint.h>

typedef unsigned short u16;
typedef __attribute__((ext_vector_type(8))) short bf16x8;
typedef __attribute__((ext_vector_type(4))) short s16x4;
typedef __attribute__((ext_vector_type(4))) float f32x4;
typedef __attribute__((ext_vector_type(2))) float f32x2;
typedef __attribute__((ext_vector_type(4))) unsigned u32x4;
typedef __attribute__((ext_vector_type(2))) unsigned u32x2;
typedef __attribute__((ext_vector_type(2))) __bf16 bf16x2_t;
typedef __attribute__((address_space(3))) const char* lds_cptr;

__device__ __forceinline__ void st16_sc1(void* p, u32x4 v) {
  asm volatile("global_store_dwordx4 %0, %1, off sc1\n\ts_nop 1" ::"v"(p), "v"(v) : "memory");
}
#define ST16(p, v) st16_sc1((void*)(p), (v))

constexpr int S_LEN = 2048, DM = 1024, NH = 32, DH = 32, WIN = 256, FF = 3584;
constexpr float LN_EPS = 1e-5f;

__device__ __forceinline__ float bf2f(u16 v) { return __uint_as_float(((unsigned)v) << 16); }
__device__ __forceinline__ float bflo(unsigned v) { return __uint_as_float(v << 16); }
__device__ __forceinline__ float bfhi(unsigned v) { return __uint_as_float(v & 0xffff0000u); }
__device__ __forceinline__ unsigned pk2(float lo, float hi) {
  return __builtin_bit_cast(unsigned, __builtin_convertvector(f32x2{lo, hi}, bf16x2_t));
}
__device__ __forceinline__ void glds16(const void* sbase, unsigned voff, unsigned lds_addr) {
  unsigned sv;
  asm volatile("s_mov_b32 %0, m0\n\ts_mov_b32 m0, %3\n\ts_nop 0\n\tglobal_load_lds_dwordx4 %1, %2\n\ts_mov_b32 m0, %0"
               : "=&s"(sv) : "v"(voff), "s"(sbase), "s"(lds_addr) : "memory");
}
__device__ __forceinline__ void glds16_sc1(const void* sbase, unsigned voff, unsigned lds_addr) {
  unsigned sv;
  asm volatile("s_mov_b32 %0, m0\n\ts_mov_b32 m0, %3\n\ts_nop 0\n\tglobal_load_lds_dwordx4 %1, %2 sc1\n\ts_mov_b32 m0, %0"
               : "=&s"(sv) : "v"(voff), "s"(sbase), "s"(lds_addr) : "memory");
}
__device__ __forceinline__ void touch_lds(const char* p, unsigned lds_scratch) {
  unsigned sv;
  asm volatile("s_mov_b32 %0, m0\n\ts_mov_b32 m0, %2\n\ts_nop 0\n\tglobal_load_lds_dword %1, off\n\ts_mov_b32 m0, %0"
               : "=&s"(sv) : "v"(p), "s"(lds_scratch) : "memory");
}
__device__ __forceinline__ s16x4 vtr(lds_cptr p) {
  return __builtin_bit_cast(s16x4, __builtin_amdgcn_ds_read_tr16_b64_v4i16((__attribute__((address_space(3))) s16x4*)p));
}
template <int CTRL>
__device__ __forceinline__ float dpp_add(float v) {
  return v + __int_as_float(__builtin_amdgcn_update_dpp(0, __float_as_int(v), CTRL, 0xf, 0xf, false));
}
__device__ __forceinline__ float sum8(float v) {
  v = dpp_add<0xB1>(v);
  v = dpp_add<0x4E>(v);
  return dpp_add<0x141>(v);
}
__device__ __forceinline__ float xsum16_32(float m) {
  auto a = __builtin_amdgcn_permlane32_swap(__float_as_uint(m), __float_as_uint(m), false, false);
  m = __uint_as_float(a[0]) + __uint_as_float(a[1]);
  auto b = __builtin_amdgcn_permlane16_swap(__float_as_uint(m), __float_as_uint(m), false, false);
  return __uint_as_float(b[0]) + __uint_as_float(b[1]);
}
__device__ __forceinline__ float xmax16_32(float m) {
  auto a = __builtin_amdgcn_permlane32_swap(__float_as_uint(m), __float_as_uint(m), false, false);
  m = fmaxf(__uint_as_float(a[0]), __uint_as_float(a[1]));
  auto b = __builtin_amdgcn_permlane16_swap(__float_as_uint(m), __float_as_uint(m), false, false);
  return fmaxf(__uint_as_float(b[0]), __uint_as_float(b[1]));
}
__device__ __forceinline__ float wave_sum(float v) {
  v = sum8(v);
  v = dpp_add<0x140>(v);
  return xsum16_32(v);
}

__global__ __launch_bounds__(256) void prep_kernel(const u16* __restrict__ in, u16* __restrict__ out,
                                                   const char* __restrict__ wqkv, const char* __restrict__ b1,
                                                   const char* __restrict__ b2, unsigned* __restrict__ flags) {
  const int lane = threadIdx.x & 63;
  const int row = blockIdx.x * 4 + (threadIdx.x >> 6);
  const u32x4* src = (const u32x4*)(in + (size_t)row * DM);
  u32x4 a = src[lane], b = src[lane + 64];
  const int gi = blockIdx.x * 256 + threadIdx.x;
  unsigned w0 = 0;
  if (gi < 49152) w0 = *(const unsigned*)(wqkv + (size_t)gi * 128);
  else if (gi < 49152 + 56) w0 = *(const unsigned*)(b1 + (size_t)(gi - 49152) * 128);
  else if (gi < 49152 + 72) w0 = *(const unsigned*)(b2 + (size_t)(gi - 49152 - 56) * 128);
  if (gi < 768) flags[gi * 32] = 0u;
  unsigned w1c = 0;
  if (threadIdx.x < 64 && blockIdx.x < 128) {
    unsigned long long pc, pe;
    asm volatile("s_getpc_b64 %0" : "=s"(pc));
    asm volatile("s_getpc_b64 s[40:41]\n\ts_add_u32 s40, s40, zz_text_end@rel32@lo+4\n\t"
                 "s_addc_u32 s41, s41, zz_text_end@rel32@hi+12\n\ts_mov_b64 %0, s[40:41]"
                 : "=s"(pe) : : "s40", "s41", "scc");
    const unsigned long long a = (pc & ~127ull) + (unsigned long long)((blockIdx.x >> 3) * 64 + lane) * 128;
    if (a + 4 <= pe) w1c = *(const unsigned*)a;
  }
  float v[16];
#pragma unroll
  for (int i = 0; i < 4; ++i) {
    v[2 * i] = bflo(a[i]);
    v[2 * i + 1] = bfhi(a[i]);
    v[8 + 2 * i] = bflo(b[i]);
    v[8 + 2 * i + 1] = bfhi(b[i]);
  }
  float s = 0.f;
#pragma unroll
  for (int i = 0; i < 16; ++i) s += v[i];
  const float mean = wave_sum(s) * (1.0f / DM);
  float q = 0.f;
#pragma unroll
  for (int i = 0; i < 16; ++i) { v[i] -= mean; q += v[i] * v[i]; }
  const float var = wave_sum(q) * (1.0f / DM);
  const float r = 1.0f / sqrtf(var + LN_EPS);
  u32x4 oa, ob;
#pragma unroll
  for (int i = 0; i < 4; ++i) {
    oa[i] = pk2(v[2 * i] * r, v[2 * i + 1] * r);
    ob[i] = pk2(v[8 + 2 * i] * r, v[8 + 2 * i + 1] * r);
  }
  u32x4* dst = (u32x4*)(out + (size_t)row * DM);
  dst[lane] = oa;
  dst[lane + 64] = ob;
  asm volatile("" ::"v"(w0), "v"(w1c));
}

__device__ __forceinline__ float rowmax17(const f32x4* s) {
  float a = fmaxf(fmaxf(s[0][0], s[0][1]), fmaxf(s[0][2], s[0][3]));
#pragma unroll
  for (int t = 1; t < 17; ++t) {
    a = fmaxf(fmaxf(a, s[t][0]), s[t][1]);
    a = fmaxf(fmaxf(a, s[t][2]), s[t][3]);
  }
  return a;
}

template <bool EARLY>
__device__ __forceinline__ void attn_body(const char* ks, const char* vs, bf16x8 qf, int wid, int lane, int tmin,
                                          char* op) {
  const int fr = lane & 15, g = lane >> 4;
  const float NEG = -1e30f;
  f32x4 s[17];
  const char* kp = ks + (wid * 16 + fr) * 64 + ((g ^ ((4 - ((fr >> 2) & 3)) & 3)) << 4);
#pragma unroll
  for (int t = 0; t < 17; ++t) {
    bf16x8 kf = *(const bf16x8*)(kp + t * 1024);
    s[t] = __builtin_amdgcn_mfma_f32_16x16x32_bf16(kf, qf, f32x4{0.f, 0.f, 0.f, 0.f}, 0, 0, 0);
  }
#pragma unroll
  for (int r = 0; r < 4; ++r) {
    s[0][r] = (4 * g + r > fr) ? s[0][r] : NEG;
    s[16][r] = (4 * g + r <= fr) ? s[16][r] : NEG;
  }
  if (EARLY) {
#pragma unroll
    for (int t = 0; t < 16; ++t)
      if (t < tmin) s[t] = f32x4{NEG, NEG, NEG, NEG};
  }
  const float mx = xmax16_32(rowmax17(s));
  const float c = 0.17677669529663687f * 1.4426950408889634f;
  const f32x2 c2 = {c, c};
  const f32x2 nmc2 = {-mx * c, -mx * c};
  f32x2 sum2 = {0.f, 0.f};
  unsigned pk[18][2];
#pragma unroll
  for (int t = 0; t < 17; ++t) {
#pragma unroll
    for (int hh = 0; hh < 2; ++hh) {
      const f32x2 z = f32x2{s[t][2 * hh], s[t][2 * hh + 1]} * c2 + nmc2;
      const f32x2 p = {__builtin_amdgcn_exp2f(z[0]), __builtin_amdgcn_exp2f(z[1])};
      sum2 += p;
      pk[t][hh] = pk2(p[0], p[1]);
    }
  }
  float sum = sum2[0] + sum2[1];
  pk[17][0] = 0u;
  pk[17][1] = 0u;
  sum = xsum16_32(sum);
  const int q4 = (lane >> 2) & 3, pp = lane & 3;
  const char* vp0 = vs + (wid * 16 + 4 * g + q4) * 64 + ((g & 1) << 5) + pp * 8;
  const char* vp1 = vs + (wid * 16 + 4 * g + q4) * 64 + (((g & 1) ^ 1) << 5) + pp * 8;
  f32x4 o0 = {0.f, 0.f, 0.f, 0.f}, o1 = {0.f, 0.f, 0.f, 0.f};
#pragma unroll
  for (int tp = 0; tp < 9; ++tp) {
    const int t0 = 2 * tp, t1 = (2 * tp + 1 < 17) ? 2 * tp + 1 : 16;
    u32x4 pb = {pk[2 * tp][0], pk[2 * tp][1], pk[2 * tp + 1][0], pk[2 * tp + 1][1]};
    bf16x8 pf = __builtin_bit_cast(bf16x8, pb);
    s16x4 a0 = vtr((lds_cptr)(vp0 + t0 * 1024)), a1 = vtr((lds_cptr)(vp0 + t1 * 1024));
    s16x4 b0 = vtr((lds_cptr)(vp1 + t0 * 1024)), b1 = vtr((lds_cptr)(vp1 + t1 * 1024));
    o0 = __builtin_amdgcn_mfma_f32_16x16x32_bf16(__builtin_shufflevector(a0, a1, 0, 1, 2, 3, 4, 5, 6, 7), pf, o0, 0, 0, 0);
    o1 = __builtin_amdgcn_mfma_f32_16x16x32_bf16(__builtin_shufflevector(b0, b1, 0, 1, 2, 3, 4, 5, 6, 7), pf, o1, 0, 0, 0);
  }
  const float inv = 1.0f / sum;
  u32x2 w0, w1;
  w0[0] = pk2(o0[0] * inv, o0[1] * inv);
  w0[1] = pk2(o0[2] * inv, o0[3] * inv);
  w1[0] = pk2(o1[0] * inv, o1[1] * inv);
  w1[1] = pk2(o1[2] * inv, o1[3] * inv);
  *(u32x2*)(op + 8 * g) = w0;
  *(u32x2*)(op + 32 + 8 * g) = w1;
}


enum { EPI_QKV = 0, EPI_RES_STATS = 1, EPI_LN_GELU = 2, EPI_SPLIT_BIAS_RES = 3 };
constexpr int NLW = 4;

template <int CNT, int NBUF, int NPA, int STAGE>
__device__ __forceinline__ void loader_loop(const char* Ab, const char* Wb, const unsigned* voff, int lw, int nk,
                                            unsigned wstep, unsigned lds0) {
  const char* sp[CNT];
  unsigned st[CNT];
#pragma unroll
  for (int i = 0; i < CNT; ++i) {
    const bool isA = (lw + i * NLW) < NPA;
    sp[i] = isA ? Ab : Wb;
    st[i] = isA ? 128u : wstep;
  }
  auto issue = [&](int buf) {
#pragma unroll
    for (int i = 0; i < CNT; ++i) {
      glds16(sp[i], voff[i], lds0 + buf * STAGE + (lw + i * NLW) * 1024);
      sp[i] += st[i];
    }
  };
  issue(0);
  issue(1);
  asm volatile("s_waitcnt vmcnt(%0)" ::"i"(CNT) : "memory");
  __builtin_amdgcn_s_barrier();
#pragma unroll
  for (int s = 2; s < NBUF; ++s) issue(s);
  int buf = 0;
  for (int t = 0; t < nk; ++t) {
    if (t + NBUF <= nk) {
      asm volatile("s_waitcnt vmcnt(%0)" ::"i"((NBUF - 2) * CNT) : "memory");
    } else {
      asm volatile("s_waitcnt vmcnt(0)" ::: "memory");
    }
    __builtin_amdgcn_s_barrier();
    if (t + NBUF < nk) issue(buf);
    buf = (buf + 1 == NBUF) ? 0 : buf + 1;
  }
}

template <int BN, int NBUF, int EPI, int XA, int TL0 = 0, int TL1 = 0>
__device__ __forceinline__ void gemm_body(const char* __restrict__ tp0, const char* __restrict__ tp1, const u16* __restrict__ A, const u16* __restrict__ W,
                                          u16* __restrict__ C, const u16* __restrict__ bias,
                                          const u16* __restrict__ res, float* __restrict__ aux0,
                                          float* __restrict__ aux1, unsigned* __restrict__ flags,
                                          const int N, const int K) {
  constexpr int BM = 128;
  constexpr bool WIDE = BN > 64;
  constexpr bool SPLIT = (EPI == EPI_SPLIT_BIAS_RES);
  constexpr int NTHR = 512;
  constexpr int NG = BN / 64;
  constexpr bool HALF = (BN % 64) != 0;
  constexpr int NT = WIDE ? BN / 32 : 4;
  constexpr int ABYTES = BM * 128, WBYTES = 64 * BN * 2, STAGE = ABYTES + WBYTES;
  constexpr int NPA = ABYTES / 1024, NP = STAGE / 1024;
  constexpr int CMAX = (NP + NLW - 1) / NLW;
  constexpr int TSCR = NBUF * STAGE + 4096;
  constexpr int SIDE = NBUF * STAGE;
  extern __shared__ __attribute__((aligned(16))) char smem[];

  const int tid = threadIdx.x, lane = tid & 63;
  const int wid = __builtin_amdgcn_readfirstlane(tid >> 6);
  const int xc = blockIdx.x & 7, rr = blockIdx.x >> 3;
  int bm, bn, hk = 0;
  if (SPLIT) {
    hk = rr & 1;
    const int tt = rr >> 1;
    bm = (xc & 3) * 4 + (tt & 3);
    bn = (xc >> 2) * 4 + (tt >> 2);
  } else {
    constexpr int XB = 32 / XA, PM = 16 / XA;
    bm = (xc % PM) * XA + (rr % XA);
    bn = (xc / PM) * XB + (rr / XA);
  }
  const int m0 = bm * BM, n0 = bn * BN;
  const int nk = SPLIT ? K / 128 : K / 64;
  const int kbeg = hk * (K / 2);
  const int odd = HALF ? (bn & 1) : 0;
  const int cfull = odd ? 32 : 0, chalf = odd ? 0 : NG * 64;
  const unsigned lds0 = (unsigned)(uintptr_t)(__attribute__((address_space(3))) char*)smem;
  const int fr = lane & 15, g = lane >> 4;

  f32x4 acc[4][NT];
  const int wm = wid & 1, wx = (wid >> 1) & 1;
  int tcol[NT];

  if (wid >= 4) {
    const int lw = wid - 4;
    unsigned voff[CMAX];
#pragma unroll
    for (int i = 0; i < CMAX; ++i) {
      const int idx = lw + i * NLW;
      if (idx < NPA) {
        const int row = idx * 8 + (lane >> 3), ch = (lane & 7) ^ (row & 7);
        voff[i] = (unsigned)(((m0 + row) * K + ch * 8) * 2);
      } else if (idx < NPA + NG * 8) {
        const int w = idx - NPA, G = w >> 3, k = (w & 7) * 8 + (lane >> 3);
        const int ch = (lane & 7) ^ ((k & 2) | ((k >> 1) & 4));
        const int col = (EPI == EPI_QKV) ? G * DM + bn * 64 : n0 + cfull + G * 64;
        voff[i] = (unsigned)((k * N + col + ch * 8) * 2);
      } else {
        const int w = idx - NPA - NG * 8, k = w * 16 + (lane >> 2);
        const int ch = (lane & 3) ^ ((k >> 2) & 2);
        voff[i] = (unsigned)((k * N + n0 + chalf + ch * 8) * 2);
      }
    }
    const unsigned wstep = (unsigned)(64 * N * 2);
    const char* Ab = (const char*)(A + kbeg);
    const char* Wb = (const char*)(W + (size_t)kbeg * N);
    if (lw + (CMAX - 1) * NLW < NP)
      loader_loop<CMAX, NBUF, NPA, STAGE>(Ab, Wb, voff, lw, nk, wstep, lds0);
    else
      loader_loop<CMAX - 1, NBUF, NPA, STAGE>(Ab, Wb, voff, lw, nk, wstep, lds0);
  } else {
    if (EPI == EPI_LN_GELU) {
      if (tid < 128) {
        const f32x4* sp = (const f32x4*)(aux0 + (size_t)(m0 + tid) * 32);
        f32x4 p[8];
#pragma unroll
        for (int i = 0; i < 8; ++i) p[i] = sp[i];
        float mean = 0.f;
#pragma unroll
        for (int i = 0; i < 8; ++i) mean += p[i][0] + p[i][2];
        mean *= (1.0f / 16.0f);
        float m2 = 0.f;
#pragma unroll
        for (int i = 0; i < 8; ++i) {
          const float d0 = p[i][0] - mean, d1 = p[i][2] - mean;
          m2 += p[i][1] + p[i][3] + 64.0f * (d0 * d0 + d1 * d1);
        }
        const float var = m2 * (1.0f / DM);
        const float r1 = 1.0f / sqrtf(var + LN_EPS);
        const float r2 = 1.0f / sqrtf(var * r1 * r1 + LN_EPS);
        *(f32x2*)(smem + SIDE + tid * 8) = f32x2{mean, r1 * r2};
      } else if (tid - 128 < BN / 2) {
        const int c = (tid - 128) * 2;
        f32x2 cs = {0.f, 0.f};
#pragma unroll
        for (int kc = 0; kc < 16; ++kc) cs += *(const f32x2*)(aux1 + (size_t)kc * FF + n0 + c);
        const unsigned bb = *(const unsigned*)(bias + n0 + c);
        *(f32x4*)(smem + SIDE + 1024 + c * 8) = f32x4{cs[0], bflo(bb), cs[1], bfhi(bb)};
      }
    }
    const int q = (lane >> 2) & 3, pp = lane & 3;
    const int wk = WIDE ? 0 : wx;
    int aoff[2];
#pragma unroll
    for (int ks = 0; ks < 2; ++ks) aoff[ks] = (wm * 64 + fr) * 128 + (((ks * 4 + g) ^ (fr & 7)) << 4);
    int tb[NT];
    const int f1 = (q & 2) | ((g & 1) << 2);
#pragma unroll
    for (int j = 0; j < NT; ++j) {
      int G, tp;
      if (!WIDE) { G = 0; tp = j; }
      else if (EPI == EPI_QKV) { G = (j >> 1) ^ 2; G = G == 3 ? 1 : G; tp = wx * 2 + (j & 1); }
      else if (j < 4) { G = wx * (NG - 1); tp = j; }
      else { G = 1; tp = wx * 2 + (j - 4); }
      if (WIDE && HALF && j == NT - 1) {
        tb[j] = ABYTES + NG * 8192 + (8 * g + q) * 64 + (((2 * wx + (pp >> 1)) ^ ((g & 1) << 1)) << 4) + (pp & 1) * 8;
        tcol[j] = chalf + wx * 16;
      } else {
        tb[j] = ABYTES + G * 8192 + (8 * g + q) * 128 + (((2 * tp + (pp >> 1)) ^ f1) << 4) + (pp & 1) * 8;
        tcol[j] = cfull + G * 64 + tp * 16;
      }
    }
#pragma unroll
    for (int i = 0; i < 4; ++i)
#pragma unroll
      for (int j = 0; j < NT; ++j) acc[i][j] = f32x4{0.f, 0.f, 0.f, 0.f};

    bf16x8 xa[4], wa[NT], xb[4], wb[NT];
    auto rd = [&](bf16x8* xf, bf16x8* wf, int buf, int ks) {
      const char* base = smem + buf * STAGE;
#pragma unroll
      for (int j = 0; j < NT; ++j) {
        const int rs = (WIDE && HALF && j == NT - 1) ? 64 : 128;
        s16x4 lo = vtr((lds_cptr)(base + tb[j] + ks * 32 * rs));
        s16x4 hi = vtr((lds_cptr)(base + tb[j] + ks * 32 * rs + 4 * rs));
        wf[j] = __builtin_shufflevector(lo, hi, 0, 1, 2, 3, 4, 5, 6, 7);
      }
#pragma unroll
      for (int i = 0; i < 4; ++i) xf[i] = *(const bf16x8*)(base + aoff[ks] + i * 2048);
    };
    auto mm = [&](const bf16x8* xf, const bf16x8* wf) {
#pragma unroll
      for (int i = 0; i < 4; ++i)
#pragma unroll
        for (int j = 0; j < NT; ++j)
          acc[i][j] = __builtin_amdgcn_mfma_f32_16x16x32_bf16(wf[j], xf[i], acc[i][j], 0, 0, 0);
    };
#define INTERLEAVE() do { _Pragma("unroll") for (int z_ = 0; z_ < 2 * NT + 4; ++z_) { __builtin_amdgcn_sched_group_barrier(0x8, 1, 0); __builtin_amdgcn_sched_group_barrier(0x100, 1, 0); } __builtin_amdgcn_sched_group_barrier(0x8, 4 * NT - (2 * NT + 4), 0); } while (0)
    __builtin_amdgcn_s_waitcnt(0xC07F);
    __builtin_amdgcn_s_barrier();
    auto do_touch = [&](int i) {
      const int line = ((blockIdx.x * 4 + wid) * 2 + i) * 64 + lane;
      if (__builtin_amdgcn_readfirstlane(line) < TL0 + TL1) {
        const int lc = line < TL0 + TL1 ? line : TL0 + TL1 - 1;
        const char* p = lc < TL0 ? tp0 + (size_t)lc * 128 : tp1 + (size_t)(lc - TL0) * 128;
        unsigned sv;
        asm volatile("s_mov_b32 %0, m0\n\ts_mov_b32 m0, %2\n\ts_nop 0\n\tglobal_load_lds_dword %1, off\n\ts_mov_b32 m0, %0"
                     : "=&s"(sv) : "v"(p), "s"(lds0 + TSCR + wid * 256) : "memory");
      }
    };
    if (WIDE) {
      rd(xa, wa, 0, 0);
      int b0 = 0;
      for (int t = 0; t < nk; ++t) {
        if (TL0 + TL1 > 0) { if (t == 2) do_touch(0); if (t == 7) do_touch(1); }
        rd(xb, wb, b0, 1);
        b0 = (b0 + 1 == NBUF) ? 0 : b0 + 1;
        mm(xa, wa);
        INTERLEAVE();
        __builtin_amdgcn_sched_barrier(0);
        __builtin_amdgcn_s_waitcnt(0xC07F);
        __builtin_amdgcn_s_barrier();
        rd(xa, wa, b0, 0);
        mm(xb, wb);
        INTERLEAVE();
        __builtin_amdgcn_sched_barrier(0);
      }
    } else {
      rd(xa, wa, 0, wk);
      int b1 = 1;
      for (int t = 0; t < nk; t += 2) {
        __builtin_amdgcn_s_waitcnt(0xC07F);
        __builtin_amdgcn_s_barrier();
        rd(xb, wb, b1, wk);
        b1 = (b1 + 1 == NBUF) ? 0 : b1 + 1;
        mm(xa, wa);
        INTERLEAVE();
        __builtin_amdgcn_sched_barrier(0);
        __builtin_amdgcn_s_waitcnt(0xC07F);
        __builtin_amdgcn_s_barrier();
        rd(xa, wa, b1, wk);
        b1 = (b1 + 1 == NBUF) ? 0 : b1 + 1;
        mm(xb, wb);
        INTERLEAVE();
        __builtin_amdgcn_sched_barrier(0);
      }
    }
    __builtin_amdgcn_s_waitcnt(0xC07F);
    if (TL0 + TL1 > 0) asm volatile("s_waitcnt vmcnt(0)" ::: "memory");
    if (SPLIT) {
      const int tile = bm * 8 + bn;
      const auto rsrc = __builtin_amdgcn_make_buffer_rsrc(aux0, 0, 128 * 2 * 2 * 16384, 0x00020000);
      if (wm != hk) {
        const int slot = ((tile * 2 + hk) * 2 + wx);
#pragma unroll
        for (int i = 0; i < 4; ++i)
#pragma unroll
          for (int j = 0; j < NT; ++j)
            __builtin_amdgcn_raw_buffer_store_b128(__builtin_bit_cast(u32x4, acc[i][j]), rsrc,
                                                   slot * 16384 + (i * NT + j) * 1024 + lane * 16, 0, 16);
        asm volatile("s_waitcnt vmcnt(0)" ::: "memory");
        if (lane == 0) __hip_atomic_store(flags + slot * 32, 1u, __ATOMIC_RELAXED, __HIP_MEMORY_SCOPE_AGENT);
      } else {
        const int slot = ((tile * 2 + (hk ^ 1)) * 2 + wx);
        for (int spin = 0; spin < (1 << 24); ++spin) {
          if (__hip_atomic_load(flags + slot * 32, __ATOMIC_RELAXED, __HIP_MEMORY_SCOPE_AGENT) != 0u) break;
          __builtin_amdgcn_s_sleep(2);
        }
        asm volatile("" ::: "memory");
#pragma unroll
        for (int i = 0; i < 4; ++i) {
          u32x4 t[NT];
#pragma unroll
          for (int j = 0; j < NT; ++j)
            t[j] = __builtin_amdgcn_raw_buffer_load_b128(rsrc, slot * 16384 + (i * NT + j) * 1024 + lane * 16, 0, 16);
#pragma unroll
          for (int j = 0; j < NT; ++j) acc[i][j] += __builtin_bit_cast(f32x4, t[j]);
        }
      }
    }
  }
  __syncthreads();

  constexpr int CPR = BN / 8;
  if (EPI == EPI_QKV) {
    constexpr int KIMG = 0, VIMG = 49152, QIMG = 98304, RED = 114688, HIMG = 24576;
    u16* kvg = (u16*)aux0;
    u32x4 cw[8];
    const int t2 = tid - 256;
    const int cb = blockIdx.x < 224 ? blockIdx.x : 223;
    const int cg = cb % 14, kc = cb / 14;
    if (wid < 4) {
      auto put = [&](int j) {
        const int grp = tcol[j] >> 6, hd = (tcol[j] >> 5) & 1;
        const int dd = (tcol[j] & 31) + g * 4, chn = dd >> 3, hf = (dd >> 2) & 1;
        const int sw = (grp == 2) ? (((fr >> 2) & 1) << 1) : ((4 - ((fr >> 2) & 3)) & 3);
        const int base = (grp == 0) ? QIMG + hd * 8192 : ((grp == 1) ? KIMG : VIMG) + hd * HIMG + 256 * 64;
#pragma unroll
        for (int i = 0; i < 4; ++i) {
          u32x2 o;
          o[0] = pk2(acc[i][j][0], acc[i][j][1]);
          o[1] = pk2(acc[i][j][2], acc[i][j][3]);
          *(u32x2*)(smem + base + (wm * 64 + i * 16 + fr) * 64 + ((chn ^ sw) << 4) + hf * 8) = o;
        }
      };
#pragma unroll
      for (int j = 0; j < 4; ++j) put(j);
      if (bm < 15) {
#pragma unroll
        for (int kv = 0; kv < 2; ++kv)
#pragma unroll
          for (int pc = 0; pc < 4; ++pc) {
            const int row = wm * 64 + pc * 16 + (lane >> 2), slot = lane & 3;
            const int sw = kv ? (((row >> 2) & 1) << 1) : ((4 - ((row >> 2) & 3)) & 3);
            const u32x4 v = *(const u32x4*)(smem + (kv ? VIMG : KIMG) + wx * HIMG + (256 + row) * 64 + slot * 16);
            ST16(kvg + ((size_t)(kv * NH + bn * 2 + wx) * S_LEN + m0 + row) * 32 + (slot ^ sw) * 8, v);
          }
      }
      put(4);
      put(5);
      if (bm < 15) {
        asm volatile("s_waitcnt vmcnt(0)" ::: "memory");
        if (lane == 0)
          __hip_atomic_fetch_add(flags + (bm * 16 + bn) * 32, 1u, __ATOMIC_RELAXED, __HIP_MEMORY_SCOPE_AGENT);
      }
    } else {
      const u16* cp = res + (size_t)(kc * 64 + (t2 >> 5) * 8) * FF + cg * 256 + (t2 & 31) * 8;
#pragma unroll
      for (int i = 0; i < 8; ++i) cw[i] = *(const u32x4*)(cp + (size_t)i * FF);
      const int lw = wid - 4, pr = lw >> 1, kv = lw & 1;
      const int pb = bm - 2 + pr;
      const unsigned dst = lds0 + (kv ? VIMG : KIMG) + pr * 128 * 64;
      if (pb >= 0) {
        const unsigned* fp = flags + (pb * 16 + bn) * 32;
        for (int spin = 0; spin < (1 << 24); ++spin) {
          if (__hip_atomic_load(fp, __ATOMIC_RELAXED, __HIP_MEMORY_SCOPE_AGENT) >= 4u) break;
          __builtin_amdgcn_s_sleep(2);
        }
        asm volatile("" ::: "memory");
        const int kl = lane >> 2, slot = lane & 3;
        const int sw = kv ? (((kl >> 2) & 1) << 1) : ((4 - ((kl >> 2) & 3)) & 3);
        const unsigned vo = (unsigned)((kl * 32 + (slot ^ sw) * 8) * 2);
#pragma unroll
        for (int hd = 0; hd < 2; ++hd) {
          const u16* src = kvg + ((size_t)(kv * NH + bn * 2 + hd) * S_LEN + pb * 128) * 32;
#pragma unroll
          for (int pc = 0; pc < 8; ++pc) glds16_sc1(src + pc * 512, vo, dst + hd * HIMG + pc * 1024);
        }
      } else {
#pragma unroll
        for (int hd = 0; hd < 2; ++hd)
#pragma unroll
          for (int pc = 0; pc < 8; ++pc)
            *(u32x4*)(smem + (kv ? VIMG : KIMG) + pr * 128 * 64 + hd * HIMG + pc * 1024 + lane * 16) = u32x4{0u, 0u, 0u, 0u};
      }
      {
        float* red = (float*)(smem + RED);
        float sacc[8];
#pragma unroll
        for (int e = 0; e < 8; ++e) sacc[e] = 0.f;
#pragma unroll
        for (int i = 0; i < 8; ++i)
#pragma unroll
          for (int e = 0; e < 4; ++e) { sacc[2 * e] += bflo(cw[i][e]); sacc[2 * e + 1] += bfhi(cw[i][e]); }
        *(f32x4*)(red + (t2 >> 5) * 256 + (t2 & 31) * 8) = f32x4{sacc[0], sacc[1], sacc[2], sacc[3]};
        *(f32x4*)(red + (t2 >> 5) * 256 + (t2 & 31) * 8 + 4) = f32x4{sacc[4], sacc[5], sacc[6], sacc[7]};
      }
      asm volatile("s_waitcnt vmcnt(0)" ::: "memory");
    }
    __syncthreads();
    if (wid < 4) {
      const int li = (rr * 4 + wid) * 64 + lane;
      const char* p = tp1 + (size_t)(li >> 3) * 2048 + (xc >> 2) * 1024 + (li & 7) * 128;
      unsigned sv;
      asm volatile("s_mov_b32 %0, m0\n\ts_mov_b32 m0, %2\n\ts_nop 0\n\tglobal_load_lds_dword %1, off\n\ts_mov_b32 m0, %0"
                   : "=&s"(sv) : "v"(p), "s"(lds0 + TSCR + wid * 256) : "memory");
    }
    if (blockIdx.x < 224 && tid < 256) {
      const float* red = (const float*)(smem + RED);
      float t = 0.f;
#pragma unroll
      for (int i = 0; i < 8; ++i) t += red[i * 256 + tid];
      aux1[(size_t)kc * FF + cg * 256 + tid] = t;
    }
    const int qw = m0 + wid * 16;
    const int tmin = (qw < WIN) ? (WIN - qw) >> 4 : 0;
    const int qo = (wid * 16 + fr) * 64 + ((g ^ ((4 - ((fr >> 2) & 3)) & 3)) << 4);
    const bf16x8 qf0 = *(const bf16x8*)(smem + QIMG + qo), qf1 = *(const bf16x8*)(smem + QIMG + 8192 + qo);
    char* ost = smem + QIMG + wid * 1024;
#pragma unroll 1
    for (int hd = 0; hd < 2; ++hd) {
      const bf16x8 qf = hd ? qf1 : qf0;
      char* op = ost + hd * 8192 + fr * 64;
      if (bm < 2) attn_body<true>(smem + KIMG + hd * HIMG, smem + VIMG + hd * HIMG, qf, wid, lane, tmin, op);
      else attn_body<false>(smem + KIMG + hd * HIMG, smem + VIMG + hd * HIMG, qf, wid, lane, 0, op);
    }
#pragma unroll
    for (int i = 0; i < 2; ++i) {
      const int row = i * 8 + (lane >> 3), ch = lane & 7;
      const u32x4 v = *(const u32x4*)(ost + (ch >> 2) * 8192 + row * 64 + (ch & 3) * 16);
      ST16(C + (size_t)(qw + row) * DM + bn * 64 + ch * 8, v);
    }
  } else if (EPI == EPI_LN_GELU) {
    constexpr int RS = BN * 4 + 16;
    if (wid < 4) {
#pragma unroll
      for (int j = 0; j < NT; ++j)
#pragma unroll
        for (int i = 0; i < 4; ++i)
          *(f32x4*)(smem + (wm * 64 + i * 16 + fr) * RS + (tcol[j] + g * 4) * 4) = acc[i][j];
    }
    __syncthreads();
#pragma unroll
    for (int c = tid; c < BM * CPR; c += NTHR) {
      const int row = c / CPR, ch = c % CPR;
      const f32x4 a0 = *(const f32x4*)(smem + row * RS + ch * 32);
      const f32x4 a1 = *(const f32x4*)(smem + row * RS + ch * 32 + 16);
      const f32x2 rs = *(const f32x2*)(smem + SIDE + row * 8);
      float v[8];
#pragma unroll
      for (int r = 0; r < 4; ++r) {
        const f32x4 cb = *(const f32x4*)(smem + SIDE + 1024 + ch * 64 + r * 16);
        const float x0 = (r < 2) ? a0[2 * r] : a1[2 * r - 4];
        const float x1 = (r < 2) ? a0[2 * r + 1] : a1[2 * r - 3];
        v[2 * r] = (x0 - rs[0] * cb[0]) * rs[1] + cb[1];
        v[2 * r + 1] = (x1 - rs[0] * cb[2]) * rs[1] + cb[3];
      }
      u32x4 o;
#pragma unroll
      for (int r = 0; r < 4; ++r) {
        const f32x2 x = {v[2 * r], v[2 * r + 1]};
        const f32x2 x2 = x * x;
        const f32x2 w = x2 * f32x2{-0.10294324f, -0.10294324f} + f32x2{-2.302208198f, -2.302208198f};
        const f32x2 z = x * w;
        f32x2 e = {__builtin_amdgcn_exp2f(z[0]), __builtin_amdgcn_exp2f(z[1])};
        e = e + f32x2{1.0f, 1.0f};
        const f32x2 y = x * f32x2{__builtin_amdgcn_rcpf(e[0]), __builtin_amdgcn_rcpf(e[1])};
        o[r] = pk2(y[0], y[1]);
      }
      ST16(C + (size_t)(m0 + row) * N + n0 + ch * 8, o);
    }
  } else if (EPI == EPI_RES_STATS) {
    constexpr int RS = BN * 4 + 16;
    constexpr int PART = BM * RS;
    if (wid < 4) {
#pragma unroll
      for (int i = 0; i < 4; ++i)
#pragma unroll
        for (int j = 0; j < NT; ++j)
          *(f32x4*)(smem + wx * PART + (wm * 64 + i * 16 + fr) * RS + (j * 16 + g * 4) * 4) = acc[i][j];
    }
    __syncthreads();
#pragma unroll
    for (int c = tid; c < BM * CPR; c += NTHR) {
      const int row = c / CPR, ch = c % CPR;
      const f32x4 a0 = *(const f32x4*)(smem + row * RS + ch * 32);
      const f32x4 a1 = *(const f32x4*)(smem + row * RS + ch * 32 + 16);
      const f32x4 b0 = *(const f32x4*)(smem + PART + row * RS + ch * 32);
      const f32x4 b1 = *(const f32x4*)(smem + PART + row * RS + ch * 32 + 16);
      const u32x4 rv = *(const u32x4*)(res + (size_t)(m0 + row) * N + n0 + ch * 8);
      u32x4 o;
      o[0] = pk2(a0[0] + b0[0] + bflo(rv[0]), a0[1] + b0[1] + bfhi(rv[0]));
      o[1] = pk2(a0[2] + b0[2] + bflo(rv[1]), a0[3] + b0[3] + bfhi(rv[1]));
      o[2] = pk2(a1[0] + b1[0] + bflo(rv[2]), a1[1] + b1[1] + bfhi(rv[2]));
      o[3] = pk2(a1[2] + b1[2] + bflo(rv[3]), a1[3] + b1[3] + bfhi(rv[3]));
      ST16(C + (size_t)(m0 + row) * N + n0 + ch * 8, o);
      float s = 0.f;
#pragma unroll
      for (int r = 0; r < 4; ++r) s += bflo(o[r]) + bfhi(o[r]);
      const float mean = sum8(s) * (1.0f / 64.0f);
      float m2 = 0.f;
#pragma unroll
      for (int r = 0; r < 4; ++r) {
        const float d0 = bflo(o[r]) - mean, d1 = bfhi(o[r]) - mean;
        m2 += d0 * d0 + d1 * d1;
      }
      m2 = sum8(m2);
      if (ch == 0) *(f32x2*)(aux0 + ((size_t)(m0 + row) * 16 + bn) * 2) = f32x2{mean, m2};
    }
  } else {
    constexpr int RS = BN * 4 + 16;
    if (wid < 4 && wm == hk) {
#pragma unroll
      for (int i = 0; i < 4; ++i)
#pragma unroll
        for (int j = 0; j < NT; ++j)
          *(f32x4*)(smem + (i * 16 + fr) * RS + (tcol[j] + g * 4) * 4) = acc[i][j];
    }
    __syncthreads();
#pragma unroll
    for (int c = tid; c < 64 * CPR; c += NTHR) {
      const int row = c / CPR, ch = c % CPR;
      const f32x4 a0 = *(const f32x4*)(smem + row * RS + ch * 32);
      const f32x4 a1 = *(const f32x4*)(smem + row * RS + ch * 32 + 16);
      const u32x4 bb = *(const u32x4*)(bias + n0 + ch * 8);
      const size_t go = (size_t)(m0 + hk * 64 + row) * N + n0 + ch * 8;
      const u32x4 rv = *(const u32x4*)(res + go);
      u32x4 o;
      o[0] = pk2(a0[0] + bflo(bb[0]) + bflo(rv[0]), a0[1] + bfhi(bb[0]) + bfhi(rv[0]));
      o[1] = pk2(a0[2] + bflo(bb[1]) + bflo(rv[1]), a0[3] + bfhi(bb[1]) + bfhi(rv[1]));
      o[2] = pk2(a1[0] + bflo(bb[2]) + bflo(rv[2]), a1[1] + bfhi(bb[2]) + bfhi(rv[2]));
      o[3] = pk2(a1[2] + bflo(bb[3]) + bflo(rv[3]), a1[3] + bfhi(bb[3]) + bfhi(rv[3]));
      ST16(C + go, o);
    }
  }
  asm volatile("s_waitcnt vmcnt(0)" ::: "memory");
}

__global__ __launch_bounds__(512, 1) void qkv_attn_kernel(const u16* __restrict__ A, const u16* __restrict__ W,
                                                         u16* __restrict__ o, const char* __restrict__ wo,
                                                         const u16* __restrict__ w1, float* __restrict__ kvg,
                                                         float* __restrict__ cspart, unsigned* __restrict__ flags) {
  gemm_body<192, 3, EPI_QKV, 8, 57344, 0>((const char*)w1, wo, A, W, o, nullptr, w1, kvg, cspart, flags, 3 * DM, DM);
}
__global__ __launch_bounds__(512, 1) void gemm_wo_kernel(const u16* __restrict__ A, const u16* __restrict__ W,
                                                        u16* __restrict__ C, const u16* __restrict__ res,
                                                        float* __restrict__ stats) {
  gemm_body<64, 4, EPI_RES_STATS, 4>(nullptr, nullptr, A, W, C, nullptr, res, stats, nullptr, nullptr, DM, DM);
}
__global__ __launch_bounds__(512, 1) void gemm_w1_kernel(const u16* __restrict__ A, const u16* __restrict__ W,
                                                        u16* __restrict__ C, const u16* __restrict__ bias,
                                                        float* __restrict__ stats, float* __restrict__ cspart,
                                                        const char* __restrict__ t0) {
  gemm_body<224, 3, EPI_LN_GELU, 8, 57344, 0>(t0, t0, A, W, C, bias, nullptr, stats, cspart, nullptr, FF, DM);
}
__global__ __launch_bounds__(512, 1) void gemm_w2_kernel(const u16* __restrict__ A, const u16* __restrict__ W,
                                                        u16* __restrict__ C, const u16* __restrict__ bias,
                                                        const u16* __restrict__ res, float* __restrict__ xchg,
                                                        unsigned* __restrict__ flags) {
  gemm_body<128, 3, EPI_SPLIT_BIAS_RES, 4>(nullptr, nullptr, A, W, C, bias, res, xchg, nullptr, flags, DM, FF);
}
extern "C" __global__ void zz_text_end() {}

constexpr int lds_bytes(int BN, int NBUF, bool side) { return NBUF * (128 * 64 * 2 + 64 * BN * 2) + (side ? 5120 : 0); }

extern "C" void kernel_launch(void* const* d_in, const int* in_sizes, int n_in, void* d_out, int out_size,
                              void* d_ws, size_t ws_size, hipStream_t stream) {
  const u16* x = (const u16*)d_in[0];
  const u16* wqkv = (const u16*)d_in[1];
  const u16* wo = (const u16*)d_in[2];
  const u16* w1 = (const u16*)d_in[4];
  const u16* b1 = (const u16*)d_in[5];
  const u16* w2 = (const u16*)d_in[6];
  const u16* b2 = (const u16*)d_in[7];
  u16* out = (u16*)d_out;
  char* ws = (char*)d_ws;
  u16* xn = (u16*)(ws);
  float* xchg = (float*)(ws);
  u16* h = (u16*)(ws + (8u << 20));
  u16* qkv = (u16*)(ws + (12u << 20));
  u16* o = (u16*)(ws + (20u << 20));
  u16* hh = (u16*)(ws + (12u << 20));
  float* stats = (float*)(ws + (26u << 20));
  float* cspart = (float*)(ws + (26u << 20) + (256u << 10));
  unsigned* flags = (unsigned*)(ws + (26u << 20) + (512u << 10));

  (void)hipFuncSetAttribute((const void*)qkv_attn_kernel, hipFuncAttributeMaxDynamicSharedMemorySize, lds_bytes(192, 3, true));
  (void)hipFuncSetAttribute((const void*)gemm_wo_kernel, hipFuncAttributeMaxDynamicSharedMemorySize, lds_bytes(64, 4, false));
  (void)hipFuncSetAttribute((const void*)gemm_w1_kernel, hipFuncAttributeMaxDynamicSharedMemorySize, lds_bytes(224, 3, true));
  (void)hipFuncSetAttribute((const void*)gemm_w2_kernel, hipFuncAttributeMaxDynamicSharedMemorySize, lds_bytes(128, 3, false));
  prep_kernel<<<S_LEN / 4, 256, 0, stream>>>(x, xn, (const char*)wqkv, (const char*)b1, (const char*)b2, flags);
  qkv_attn_kernel<<<256, 512, lds_bytes(192, 3, true), stream>>>(xn, wqkv, o, (const char*)wo, w1, (float*)qkv, cspart, flags + 512 * 32);
  gemm_wo_kernel<<<256, 512, lds_bytes(64, 4, false), stream>>>(o, wo, h, x, stats);
  gemm_w1_kernel<<<256, 512, lds_bytes(224, 3, true), stream>>>(h, w1, hh, b1, stats, cspart, (const char*)w2);
  gemm_w2_kernel<<<256, 512, lds_bytes(128, 3, false), stream>>>(hh, w2, out, b2, h, xchg, flags);
}
```

```cpp
#include <hip/hip_runtime.h>
#include <hip/hip_bf16.h>
#include <stdint.h>

typedef unsigned short u16;
typedef __attribute__((ext_vector_type(8))) short bf16x8;
typedef __attribute__((ext_vector_type(4))) short s16x4;
typedef __attribute__((ext_vector_type(4))) float f32x4;
typedef __attribute__((ext_vector_type(2))) float f32x2;
typedef __attribute__((ext_vector_type(4))) unsigned u32x4;
typedef __attribute__((ext_vector_type(2))) unsigned u32x2;
typedef __attribute__((ext_vector_type(2))) __bf16 bf16x2_t;
typedef __attribute__((address_space(3))) const char* lds_cptr;

__device__ __forceinline__ void st16_sc1(void* p, u32x4 v) {
  asm volatile("global_store_dwordx4 %0, %1, off sc1\n\ts_nop 1" ::"v"(p), "v"(v) : "memory");
}
#define ST16(p, v) st16_sc1((void*)(p), (v))

constexpr int S_LEN = 2048, DM = 1024, NH = 32, DH = 32, WIN = 256, FF = 3584;
constexpr float LN_EPS = 1e-5f;

__device__ __forceinline__ float bf2f(u16 v) { return __uint_as_float(((unsigned)v) << 16); }
__device__ __forceinline__ float bflo(unsigned v) { return __uint_as_float(v << 16); }
__device__ __forceinline__ float bfhi(unsigned v) { return __uint_as_float(v & 0xffff0000u); }
__device__ __forceinline__ unsigned pk2(float lo, float hi) {
  return __builtin_bit_cast(unsigned, __builtin_convertvector(f32x2{lo, hi}, bf16x2_t));
}
__device__ __forceinline__ void glds16(const void* sbase, unsigned voff, unsigned lds_addr) {
  unsigned sv;
  asm volatile("s_mov_b32 %0, m0\n\ts_mov_b32 m0, %3\n\ts_nop 0\n\tglobal_load_lds_dwordx4 %1, %2\n\ts_mov_b32 m0, %0"
               : "=&s"(sv) : "v"(voff), "s"(sbase), "s"(lds_addr) : "memory");
}
__device__ __forceinline__ void glds16_sc1(const void* sbase, unsigned voff, unsigned lds_addr) {
  unsigned sv;
  asm volatile("s_mov_b32 %0, m0\n\ts_mov_b32 m0, %3\n\ts_nop 0\n\tglobal_load_lds_dwordx4 %1, %2 sc1\n\ts_mov_b32 m0, %0"
               : "=&s"(sv) : "v"(voff), "s"(sbase), "s"(lds_addr) : "memory");
}
__device__ __forceinline__ void touch_lds(const char* p, unsigned lds_scratch) {
  unsigned sv;
  asm volatile("s_mov_b32 %0, m0\n\ts_mov_b32 m0, %2\n\ts_nop 0\n\tglobal_load_lds_dword %1, off\n\ts_mov_b32 m0, %0"
               : "=&s"(sv) : "v"(p), "s"(lds_scratch) : "memory");
}
__device__ __forceinline__ s16x4 vtr(lds_cptr p) {
  return __builtin_bit_cast(s16x4, __builtin_amdgcn_ds_read_tr16_b64_v4i16((__attribute__((address_space(3))) s16x4*)p));
}
template <int CTRL>
__device__ __forceinline__ float dpp_add(float v) {
  return v + __int_as_float(__builtin_amdgcn_update_dpp(0, __float_as_int(v), CTRL, 0xf, 0xf, false));
}
__device__ __forceinline__ float sum8(float v) {
  v = dpp_add<0xB1>(v);
  v = dpp_add<0x4E>(v);
  return dpp_add<0x141>(v);
}
__device__ __forceinline__ float xsum16_32(float m) {
  auto a = __builtin_amdgcn_permlane32_swap(__float_as_uint(m), __float_as_uint(m), false, false);
  m = __uint_as_float(a[0]) + __uint_as_float(a[1]);
  auto b = __builtin_amdgcn_permlane16_swap(__float_as_uint(m), __float_as_uint(m), false, false);
  return __uint_as_float(b[0]) + __uint_as_float(b[1]);
}
__device__ __forceinline__ float xmax16_32(float m) {
  auto a = __builtin_amdgcn_permlane32_swap(__float_as_uint(m), __float_as_uint(m), false, false);
  m = fmaxf(__uint_as_float(a[0]), __uint_as_float(a[1]));
  auto b = __builtin_amdgcn_permlane16_swap(__float_as_uint(m), __float_as_uint(m), false, false);
  return fmaxf(__uint_as_float(b[0]), __uint_as_float(b[1]));
}
__device__ __forceinline__ float wave_sum(float v) {
  v = sum8(v);
  v = dpp_add<0x140>(v);
  return xsum16_32(v);
}

__global__ __launch_bounds__(256) void prep_kernel(const u16* __restrict__ in, u16* __restrict__ out,
                                                   const char* __restrict__ wqkv, const char* __restrict__ b1,
                                                   const char* __restrict__ b2, unsigned* __restrict__ flags) {
  const int lane = threadIdx.x & 63;
  const int row = blockIdx.x * 4 + (threadIdx.x >> 6);
  const u32x4* src = (const u32x4*)(in + (size_t)row * DM);
  u32x4 a = src[lane], b = src[lane + 64];
  const int gi = blockIdx.x * 256 + threadIdx.x;
  unsigned w0 = 0;
  if (gi < 49152) w0 = *(const unsigned*)(wqkv + (size_t)gi * 128);
  else if (gi < 49152 + 56) w0 = *(const unsigned*)(b1 + (size_t)(gi - 49152) * 128);
  else if (gi < 49152 + 72) w0 = *(const unsigned*)(b2 + (size_t)(gi - 49152 - 56) * 128);
  if (gi < 768) flags[gi * 32] = 0u;
  unsigned w1c = 0;
  if (threadIdx.x < 64 && blockIdx.x < 128) {
    unsigned long long pc, pe;
    asm volatile("s_getpc_b64 %0" : "=s"(pc));
    asm volatile("s_getpc_b64 s[40:41]\n\ts_add_u32 s40, s40, zz_text_end@rel32@lo+4\n\t"
                 "s_addc_u32 s41, s41, zz_text_end@rel32@hi+12\n\ts_mov_b64 %0, s[40:41]"
                 : "=s"(pe) : : "s40", "s41", "scc");
    const unsigned long long a = (pc & ~127ull) + (unsigned long long)((blockIdx.x >> 3) * 64 + lane) * 128;
    if (a + 4 <= pe) w1c = *(const unsigned*)a;
  }
  float v[16];
#pragma unroll
  for (int i = 0; i < 4; ++i) {
    v[2 * i] = bflo(a[i]);
    v[2 * i + 1] = bfhi(a[i]);
    v[8 + 2 * i] = bflo(b[i]);
    v[8 + 2 * i + 1] = bfhi(b[i]);
  }
  float s = 0.f;
#pragma unroll
  for (int i = 0; i < 16; ++i) s += v[i];
  const float mean = wave_sum(s) * (1.0f / DM);
  float q = 0.f;
#pragma unroll
  for (int i = 0; i < 16; ++i) { v[i] -= mean; q += v[i] * v[i]; }
  const float var = wave_sum(q) * (1.0f / DM);
  const float r = 1.0f / sqrtf(var + LN_EPS);
  u32x4 oa, ob;
#pragma unroll
  for (int i = 0; i < 4; ++i) {
    oa[i] = pk2(v[2 * i] * r, v[2 * i + 1] * r);
    ob[i] = pk2(v[8 + 2 * i] * r, v[8 + 2 * i + 1] * r);
  }
  u32x4* dst = (u32x4*)(out + (size_t)row * DM);
  dst[lane] = oa;
  dst[lane + 64] = ob;
  asm volatile("" ::"v"(w0), "v"(w1c));
}

__device__ __forceinline__ float rowmax17(const f32x4* s) {
  float a = fmaxf(fmaxf(s[0][0], s[0][1]), fmaxf(s[0][2], s[0][3]));
#pragma unroll
  for (int t = 1; t < 17; ++t) {
    a = fmaxf(fmaxf(a, s[t][0]), s[t][1]);
    a = fmaxf(fmaxf(a, s[t][2]), s[t][3]);
  }
  return a;
}

template <bool EARLY>
__device__ __forceinline__ void attn_body(const char* ks, const char* vs, bf16x8 qf, int wid, int lane, int tmin,
                                          char* op) {
  const int fr = lane & 15, g = lane >> 4;
  const float NEG = -1e30f;
  f32x4 s[17];
  const char* kp = ks + (wid * 16 + fr) * 64 + ((g ^ ((4 - ((fr >> 2) & 3)) & 3)) << 4);
#pragma unroll
  for (int t = 0; t < 17; ++t) {
    bf16x8 kf = *(const bf16x8*)(kp + t * 1024);
    s[t] = __builtin_amdgcn_mfma_f32_16x16x32_bf16(kf, qf, f32x4{0.f, 0.f, 0.f, 0.f}, 0, 0, 0);
  }
#pragma unroll
  for (int r = 0; r < 4; ++r) {
    s[0][r] = (4 * g + r > fr) ? s[0][r] : NEG;
    s[16][r] = (4 * g + r <= fr) ? s[16][r] : NEG;
  }
  if (EARLY) {
#pragma unroll
    for (int t = 0; t < 16; ++t)
      if (t < tmin) s[t] = f32x4{NEG, NEG, NEG, NEG};
  }
  const float mx = xmax16_32(rowmax17(s));
  const float c = 0.17677669529663687f * 1.4426950408889634f;
  const f32x2 c2 = {c, c};
  const f32x2 nmc2 = {-mx * c, -mx * c};
  f32x2 sum2 = {0.f, 0.f};
  unsigned pk[18][2];
#pragma unroll
  for (int t = 0; t < 17; ++t) {
#pragma unroll
    for (int hh = 0; hh < 2; ++hh) {
      const f32x2 z = f32x2{s[t][2 * hh], s[t][2 * hh + 1]} * c2 + nmc2;
      const f32x2 p = {__builtin_amdgcn_exp2f(z[0]), __builtin_amdgcn_exp2f(z[1])};
      sum2 += p;
      pk[t][hh] = pk2(p[0], p[1]);
    }
  }
  float sum = sum2[0] + sum2[1];
  pk[17][0] = 0u;
  pk[17][1] = 0u;
  sum = xsum16_32(sum);
  const int q4 = (lane >> 2) & 3, pp = lane & 3;
  const char* vp0 = vs + (wid * 16 + 4 * g + q4) * 64 + ((g & 1) << 5) + pp * 8;
  const char* vp1 = vs + (wid * 16 + 4 * g + q4) * 64 + (((g & 1) ^ 1) << 5) + pp * 8;
  f32x4 o0 = {0.f, 0.f, 0.f, 0.f}, o1 = {0.f, 0.f, 0.f, 0.f};
#pragma unroll
  for (int tp = 0; tp < 9; ++tp) {
    const int t0 = 2 * tp, t1 = (2 * tp + 1 < 17) ? 2 * tp + 1 : 16;
    u32x4 pb = {pk[2 * tp][0], pk[2 * tp][1], pk[2 * tp + 1][0], pk[2 * tp + 1][1]};
    bf16x8 pf = __builtin_bit_cast(bf16x8, pb);
    s16x4 a0 = vtr((lds_cptr)(vp0 + t0 * 1024)), a1 = vtr((lds_cptr)(vp0 + t1 * 1024));
    s16x4 b0 = vtr((lds_cptr)(vp1 + t0 * 1024)), b1 = vtr((lds_cptr)(vp1 + t1 * 1024));
    o0 = __builtin_amdgcn_mfma_f32_16x16x32_bf16(__builtin_shufflevector(a0, a1, 0, 1, 2, 3, 4, 5, 6, 7), pf, o0, 0, 0, 0);
    o1 = __builtin_amdgcn_mfma_f32_16x16x32_bf16(__builtin_shufflevector(b0, b1, 0, 1, 2, 3, 4, 5, 6, 7), pf, o1, 0, 0, 0);
  }
  const float inv = 1.0f / sum;
  u32x2 w0, w1;
  w0[0] = pk2(o0[0] * inv, o0[1] * inv);
  w0[1] = pk2(o0[2] * inv, o0[3] * inv);
  w1[0] = pk2(o1[0] * inv, o1[1] * inv);
  w1[1] = pk2(o1[2] * inv, o1[3] * inv);
  *(u32x2*)(op + 8 * g) = w0;
  *(u32x2*)(op + 32 + 8 * g) = w1;
}


enum { EPI_QKV = 0, EPI_RES_STATS = 1, EPI_LN_GELU = 2, EPI_SPLIT_BIAS_RES = 3 };
constexpr int NLW = 4;

template <int CNT, int NBUF, int NPA, int STAGE>
__device__ __forceinline__ void loader_loop(const char* Ab, const char* Wb, const unsigned* voff, int lw, int nk,
                                            unsigned wstep, unsigned lds0) {
  const char* sp[CNT];
  unsigned st[CNT];
#pragma unroll
  for (int i = 0; i < CNT; ++i) {
    const bool isA = (lw + i * NLW) < NPA;
    sp[i] = isA ? Ab : Wb;
    st[i] = isA ? 128u : wstep;
  }
  auto issue = [&](int buf) {
#pragma unroll
    for (int i = 0; i < CNT; ++i) {
      glds16(sp[i], voff[i], lds0 + buf * STAGE + (lw + i * NLW) * 1024);
      sp[i] += st[i];
    }
  };
  issue(0);
  issue(1);
  asm volatile("s_waitcnt vmcnt(%0)" ::"i"(CNT) : "memory");
  __builtin_amdgcn_s_barrier();
#pragma unroll
  for (int s = 2; s < NBUF; ++s) issue(s);
  int buf = 0;
  for (int t = 0; t < nk; ++t) {
    if (t + NBUF <= nk) {
      asm volatile("s_waitcnt vmcnt(%0)" ::"i"((NBUF - 2) * CNT) : "memory");
    } else {
      asm volatile("s_waitcnt vmcnt(0)" ::: "memory");
    }
    __builtin_amdgcn_s_barrier();
    if (t + NBUF < nk) issue(buf);
    buf = (buf + 1 == NBUF) ? 0 : buf + 1;
  }
}

template <int BN, int NBUF, int EPI, int XA, int TL0 = 0, int TL1 = 0>
__device__ __forceinline__ void gemm_body(const char* __restrict__ tp0, const char* __restrict__ tp1, const u16* __restrict__ A, const u16* __restrict__ W,
                                          u16* __restrict__ C, const u16* __restrict__ bias,
                                          const u16* __restrict__ res, float* __restrict__ aux0,
                                          float* __restrict__ aux1, unsigned* __restrict__ flags,
                                          const int N, const int K) {
  constexpr int BM = 128;
  constexpr bool WIDE = BN > 64;
  constexpr bool SPLIT = (EPI == EPI_SPLIT_BIAS_RES);
  constexpr int NTHR = 512;
  constexpr int NG = BN / 64;
  constexpr bool HALF = (BN % 64) != 0;
  constexpr int NT = WIDE ? BN / 32 : 4;
  constexpr int ABYTES = BM * 128, WBYTES = 64 * BN * 2, STAGE = ABYTES + WBYTES;
  constexpr int NPA = ABYTES / 1024, NP = STAGE / 1024;
  constexpr int CMAX = (NP + NLW - 1) / NLW;
  constexpr int TSCR = NBUF * STAGE + 4096;
  constexpr int SIDE = NBUF * STAGE;
  extern __shared__ __attribute__((aligned(16))) char smem[];

  const int tid = threadIdx.x, lane = tid & 63;
  const int wid = __builtin_amdgcn_readfirstlane(tid >> 6);
  const int xc = blockIdx.x & 7, rr = blockIdx.x >> 3;
  int bm, bn, hk = 0;
  if (SPLIT) {
    hk = rr & 1;
    const int tt = rr >> 1;
    bm = (xc & 3) * 4 + (tt & 3);
    bn = (xc >> 2) * 4 + (tt >> 2);
  } else {
    constexpr int XB = 32 / XA, PM = 16 / XA;
    bm = (xc % PM) * XA + (rr % XA);
    bn = (xc / PM) * XB + (rr / XA);
  }
  const int m0 = bm * BM, n0 = bn * BN;
  const int nk = SPLIT ? K / 128 : K / 64;
  const int kbeg = hk * (K / 2);
  const int odd = HALF ? (bn & 1) : 0;
  const int cfull = odd ? 32 : 0, chalf = odd ? 0 : NG * 64;
  const unsigned lds0 = (unsigned)(uintptr_t)(__attribute__((address_space(3))) char*)smem;
  const int fr = lane & 15, g = lane >> 4;

  f32x4 acc[4][NT];
  const int wm = wid & 1, wx = (wid >> 1) & 1;
  int tcol[NT];

  if (wid >= 4) {
    const int lw = wid - 4;
    unsigned voff[CMAX];
#pragma unroll
    for (int i = 0; i < CMAX; ++i) {
      const int idx = lw + i * NLW;
      if (idx < NPA) {
        const int row = idx * 8 + (lane >> 3), ch = (lane & 7) ^ (row & 7);
        voff[i] = (unsigned)(((m0 + row) * K + ch * 8) * 2);
      } else if (idx < NPA + NG * 8) {
        const int w = idx - NPA, G = w >> 3, k = (w & 7) * 8 + (lane >> 3);
        const int ch = (lane & 7) ^ ((k & 2) | ((k >> 1) & 4));
        const int col = (EPI == EPI_QKV) ? G * DM + bn * 64 : n0 + cfull + G * 64;
        voff[i] = (unsigned)((k * N + col + ch * 8) * 2);
      } else {
        const int w = idx - NPA - NG * 8, k = w * 16 + (lane >> 2);
        const int ch = (lane & 3) ^ ((k >> 2) & 2);
        voff[i] = (unsigned)((k * N + n0 + chalf + ch * 8) * 2);
      }
    }
    const unsigned wstep = (unsigned)(64 * N * 2);
    const char* Ab = (const char*)(A + kbeg);
    const char* Wb = (const char*)(W + (size_t)kbeg * N);
    if (lw + (CMAX - 1) * NLW < NP)
      loader_loop<CMAX, NBUF, NPA, STAGE>(Ab, Wb, voff, lw, nk, wstep, lds0);
    else
      loader_loop<CMAX - 1, NBUF, NPA, STAGE>(Ab, Wb, voff, lw, nk, wstep, lds0);
  } else {
    if (EPI == EPI_LN_GELU) {
      if (tid < 128) {
        const f32x4* sp = (const f32x4*)(aux0 + (size_t)(m0 + tid) * 32);
        f32x4 p[8];
#pragma unroll
        for (int i = 0; i < 8; ++i) p[i] = sp[i];
        float mean = 0.f;
#pragma unroll
        for (int i = 0; i < 8; ++i) mean += p[i][0] + p[i][2];
        mean *= (1.0f / 16.0f);
        float m2 = 0.f;
#pragma unroll
        for (int i = 0; i < 8; ++i) {
          const float d0 = p[i][0] - mean, d1 = p[i][2] - mean;
          m2 += p[i][1] + p[i][3] + 64.0f * (d0 * d0 + d1 * d1);
        }
        const float var = m2 * (1.0f / DM);
        const float r1 = 1.0f / sqrtf(var + LN_EPS);
        const float r2 = 1.0f / sqrtf(var * r1 * r1 + LN_EPS);
        *(f32x2*)(smem + SIDE + tid * 8) = f32x2{mean, r1 * r2};
      } else if (tid - 128 < BN / 2) {
        const int c = (tid - 128) * 2;
        f32x2 cs = {0.f, 0.f};
#pragma unroll
        for (int kc = 0; kc < 16; ++kc) cs += *(const f32x2*)(aux1 + (size_t)kc * FF + n0 + c);
        const unsigned bb = *(const unsigned*)(bias + n0 + c);
        *(f32x4*)(smem + SIDE + 1024 + c * 8) = f32x4{cs[0], bflo(bb), cs[1], bfhi(bb)};
      }
    }
    const int q = (lane >> 2) & 3, pp = lane & 3;
    const int wk = WIDE ? 0 : wx;
    int aoff[2];
#pragma unroll
    for (int ks = 0; ks < 2; ++ks) aoff[ks] = (wm * 64 + fr) * 128 + (((ks * 4 + g) ^ (fr & 7)) << 4);
    int tb[NT];
    const int f1 = (q & 2) | ((g & 1) << 2);
#pragma unroll
    for (int j = 0; j < NT; ++j) {
      int G, tp;
      if (!WIDE) { G = 0; tp = j; }
      else if (EPI == EPI_QKV) { G = (j >> 1) ^ 2; G = G == 3 ? 1 : G; tp = wx * 2 + (j & 1); }
      else if (j < 4) { G = wx * (NG - 1); tp = j; }
      else { G = 1; tp = wx * 2 + (j - 4); }
      if (WIDE && HALF && j == NT - 1) {
        tb[j] = ABYTES + NG * 8192 + (8 * g + q) * 64 + (((2 * wx + (pp >> 1)) ^ ((g & 1) << 1)) << 4) + (pp & 1) * 8;
        tcol[j] = chalf + wx * 16;
      } else {
        tb[j] = ABYTES + G * 8192 + (8 * g + q) * 128 + (((2 * tp + (pp >> 1)) ^ f1) << 4) + (pp & 1) * 8;
        tcol[j] = cfull + G * 64 + tp * 16;
      }
    }
#pragma unroll
    for (int i = 0; i < 4; ++i)
#pragma unroll
      for (int j = 0; j < NT; ++j) acc[i][j] = f32x4{0.f, 0.f, 0.f, 0.f};

    bf16x8 xa[4], wa[NT], xb[4], wb[NT];
    auto rd = [&](bf16x8* xf, bf16x8* wf, int buf, int ks) {
      const char* base = smem + buf * STAGE;
#pragma unroll
      for (int j = 0; j < NT; ++j) {
        const int rs = (WIDE && HALF && j == NT - 1) ? 64 : 128;
        s16x4 lo = vtr((lds_cptr)(base + tb[j] + ks * 32 * rs));
        s16x4 hi = vtr((lds_cptr)(base + tb[j] + ks * 32 * rs + 4 * rs));
        wf[j] = __builtin_shufflevector(lo, hi, 0, 1, 2, 3, 4, 5, 6, 7);
      }
#pragma unroll
      for (int i = 0; i < 4; ++i) xf[i] = *(const bf16x8*)(base + aoff[ks] + i * 2048);
    };
    auto mm = [&](const bf16x8* xf, const bf16x8* wf) {
#pragma unroll
      for (int i = 0; i < 4; ++i)
#pragma unroll
        for (int j = 0; j < NT; ++j)
          acc[i][j] = __builtin_amdgcn_mfma_f32_16x16x32_bf16(wf[j], xf[i], acc[i][j], 0, 0, 0);
    };
#define INTERLEAVE() do { _Pragma("unroll") for (int z_ = 0; z_ < 2 * NT + 4; ++z_) { __builtin_amdgcn_sched_group_barrier(0x8, 1, 0); __builtin_amdgcn_sched_group_barrier(0x100, 1, 0); } __builtin_amdgcn_sched_group_barrier(0x8, 4 * NT - (2 * NT + 4), 0); } while (0)
    __builtin_amdgcn_s_waitcnt(0xC07F);
    __builtin_amdgcn_s_barrier();
    auto do_touch = [&](int i) {
      const int line = ((blockIdx.x * 4 + wid) * 2 + i) * 64 + lane;
      if (__builtin_amdgcn_readfirstlane(line) < TL0 + TL1) {
        const int lc = line < TL0 + TL1 ? line : TL0 + TL1 - 1;
        const char* p = lc < TL0 ? tp0 + (size_t)lc * 128 : tp1 + (size_t)(lc - TL0) * 128;
        unsigned sv;
        asm volatile("s_mov_b32 %0, m0\n\ts_mov_b32 m0, %2\n\ts_nop 0\n\tglobal_load_lds_dword %1, off\n\ts_mov_b32 m0, %0"
                     : "=&s"(sv) : "v"(p), "s"(lds0 + TSCR + wid * 256) : "memory");
      }
    };
    if (WIDE) {
      rd(xa, wa, 0, 0);
      int b0 = 0;
      for (int t = 0; t < nk; ++t) {
        if (TL0 + TL1 > 0) { if (t == 2) do_touch(0); if (t == 7) do_touch(1); }
        rd(xb, wb, b0, 1);
        b0 = (b0 + 1 == NBUF) ? 0 : b0 + 1;
        mm(xa, wa);
        INTERLEAVE();
        __builtin_amdgcn_sched_barrier(0);
        __builtin_amdgcn_s_waitcnt(0xC07F);
        __builtin_amdgcn_s_barrier();
        rd(xa, wa, b0, 0);
        mm(xb, wb);
        INTERLEAVE();
        __builtin_amdgcn_sched_barrier(0);
      }
    } else {
      rd(xa, wa, 0, wk);
      int b1 = 1;
      for (int t = 0; t < nk; t += 2) {
        __builtin_amdgcn_s_waitcnt(0xC07F);
        __builtin_amdgcn_s_barrier();
        rd(xb, wb, b1, wk);
        b1 = (b1 + 1 == NBUF) ? 0 : b1 + 1;
        mm(xa, wa);
        INTERLEAVE();
        __builtin_amdgcn_sched_barrier(0);
        __builtin_amdgcn_s_waitcnt(0xC07F);
        __builtin_amdgcn_s_barrier();
        rd(xa, wa, b1, wk);
        b1 = (b1 + 1 == NBUF) ? 0 : b1 + 1;
        mm(xb, wb);
        INTERLEAVE();
        __builtin_amdgcn_sched_barrier(0);
      }
    }
    __builtin_amdgcn_s_waitcnt(0xC07F);
    if (TL0 + TL1 > 0) asm volatile("s_waitcnt vmcnt(0)" ::: "memory");
    if (SPLIT) {
      const int tile = bm * 8 + bn;
      const auto rsrc = __builtin_amdgcn_make_buffer_rsrc(aux0, 0, 128 * 2 * 2 * 16384, 0x00020000);
      if (wm != hk) {
        const int slot = ((tile * 2 + hk) * 2 + wx);
#pragma unroll
        for (int i = 0; i < 4; ++i)
#pragma unroll
          for (int j = 0; j < NT; ++j)
            __builtin_amdgcn_raw_buffer_store_b128(__builtin_bit_cast(u32x4, acc[i][j]), rsrc,
                                                   slot * 16384 + (i * NT + j) * 1024 + lane * 16, 0, 16);
        asm volatile("s_waitcnt vmcnt(0)" ::: "memory");
        if (lane == 0) __hip_atomic_store(flags + slot * 32, 1u, __ATOMIC_RELAXED, __HIP_MEMORY_SCOPE_AGENT);
      } else {
        const int slot = ((tile * 2 + (hk ^ 1)) * 2 + wx);
        for (int spin = 0; spin < (1 << 24); ++spin) {
          if (__hip_atomic_load(flags + slot * 32, __ATOMIC_RELAXED, __HIP_MEMORY_SCOPE_AGENT) != 0u) break;
          __builtin_amdgcn_s_sleep(2);
        }
        asm volatile("" ::: "memory");
#pragma unroll
        for (int i = 0; i < 4; ++i) {
          u32x4 t[NT];
#pragma unroll
          for (int j = 0; j < NT; ++j)
            t[j] = __builtin_amdgcn_raw_buffer_load_b128(rsrc, slot * 16384 + (i * NT + j) * 1024 + lane * 16, 0, 16);
#pragma unroll
          for (int j = 0; j < NT; ++j) acc[i][j] += __builtin_bit_cast(f32x4, t[j]);
        }
      }
    }
  }
  __syncthreads();

  constexpr int CPR = BN / 8;
  if (EPI == EPI_QKV) {
    constexpr int KIMG = 0, VIMG = 49152, QIMG = 98304, RED = 114688, HIMG = 24576;
    u16* kvg = (u16*)aux0;
    u32x4 cw[8];
    const int t2 = tid - 256;
    const int cb = blockIdx.x < 224 ? blockIdx.x : 223;
    const int cg = cb % 14, kc = cb / 14;
    if (wid < 4) {
      auto put = [&](int j) {
        const int grp = tcol[j] >> 6, hd = (tcol[j] >> 5) & 1;
        const int dd = (tcol[j] & 31) + g * 4, chn = dd >> 3, hf = (dd >> 2) & 1;
        const int sw = (grp == 2) ? (((fr >> 2) & 1) << 1) : ((4 - ((fr >> 2) & 3)) & 3);
        const int base = (grp == 0) ? QIMG + hd * 8192 : ((grp == 1) ? KIMG : VIMG) + hd * HIMG + 256 * 64;
#pragma unroll
        for (int i = 0; i < 4; ++i) {
          u32x2 o;
          o[0] = pk2(acc[i][j][0], acc[i][j][1]);
          o[1] = pk2(acc[i][j][2], acc[i][j][3]);
          *(u32x2*)(smem + base + (wm * 64 + i * 16 + fr) * 64 + ((chn ^ sw) << 4) + hf * 8) = o;
        }
      };
#pragma unroll
      for (int j = 0; j < 4; ++j) put(j);
      if (bm < 15) {
#pragma unroll
        for (int kv = 0; kv < 2; ++kv)
#pragma unroll
          for (int pc = 0; pc < 4; ++pc) {
            const int row = wm * 64 + pc * 16 + (lane >> 2), slot = lane & 3;
            const int sw = kv ? (((row >> 2) & 1) << 1) : ((4 - ((row >> 2) & 3)) & 3);
            const u32x4 v = *(const u32x4*)(smem + (kv ? VIMG : KIMG) + wx * HIMG + (256 + row) * 64 + slot * 16);
            ST16(kvg + ((size_t)(kv * NH + bn * 2 + wx) * S_LEN + m0 + row) * 32 + (slot ^ sw) * 8, v);
          }
      }
      put(4);
      put(5);
      if (bm < 15) {
        asm volatile("s_waitcnt vmcnt(0)" ::: "memory");
        if (lane == 0)
          __hip_atomic_fetch_add(flags + (bm * 16 + bn) * 32, 1u, __ATOMIC_RELAXED, __HIP_MEMORY_SCOPE_AGENT);
      }
    } else {
      const u16* cp = res + (size_t)(kc * 64 + (t2 >> 5) * 8) * FF + cg * 256 + (t2 & 31) * 8;
#pragma unroll
      for (int i = 0; i < 8; ++i) cw[i] = *(const u32x4*)(cp + (size_t)i * FF);
      const int lw = wid - 4, pr = lw >> 1, kv = lw & 1;
      const int pb = bm - 2 + pr;
      const unsigned dst = lds0 + (kv ? VIMG : KIMG) + pr * 128 * 64;
      if (pb >= 0) {
        const unsigned* fp = flags + (pb * 16 + bn) * 32;
        for (int spin = 0; spin < (1 << 24); ++spin) {
          if (__hip_atomic_load(fp, __ATOMIC_RELAXED, __HIP_MEMORY_SCOPE_AGENT) >= 4u) break;
          __builtin_amdgcn_s_sleep(2);
        }
        asm volatile("" ::: "memory");
        const int kl = lane >> 2, slot = lane & 3;
        const int sw = kv ? (((kl >> 2) & 1) << 1) : ((4 - ((kl >> 2) & 3)) & 3);
        const unsigned vo = (unsigned)((kl * 32 + (slot ^ sw) * 8) * 2);
#pragma unroll
        for (int hd = 0; hd < 2; ++hd) {
          const u16* src = kvg + ((size_t)(kv * NH + bn * 2 + hd) * S_LEN + pb * 128) * 32;
#pragma unroll
          for (int pc = 0; pc < 8; ++pc) glds16_sc1(src + pc * 512, vo, dst + hd * HIMG + pc * 1024);
        }
      } else {
#pragma unroll
        for (int hd = 0; hd < 2; ++hd)
#pragma unroll
          for (int pc = 0; pc < 8; ++pc)
            *(u32x4*)(smem + (kv ? VIMG : KIMG) + pr * 128 * 64 + hd * HIMG + pc * 1024 + lane * 16) = u32x4{0u, 0u, 0u, 0u};
      }
      {
        float* red = (float*)(smem + RED);
        float sacc[8];
#pragma unroll
        for (int e = 0; e < 8; ++e) sacc[e] = 0.f;
#pragma unroll
        for (int i = 0; i < 8; ++i)
#pragma unroll
          for (int e = 0; e < 4; ++e) { sacc[2 * e] += bflo(cw[i][e]); sacc[2 * e + 1] += bfhi(cw[i][e]); }
        *(f32x4*)(red + (t2 >> 5) * 256 + (t2 & 31) * 8) = f32x4{sacc[0], sacc[1], sacc[2], sacc[3]};
        *(f32x4*)(red + (t2 >> 5) * 256 + (t2 & 31) * 8 + 4) = f32x4{sacc[4], sacc[5], sacc[6], sacc[7]};
      }
      asm volatile("s_waitcnt vmcnt(0)" ::: "memory");
    }
    __syncthreads();
    if (wid < 4) {
      const int li = (rr * 4 + wid) * 64 + lane;
      const char* p = tp1 + (size_t)(li >> 3) * 2048 + (xc >> 2) * 1024 + (li & 7) * 128;
      unsigned sv;
      asm volatile("s_mov_b32 %0, m0\n\ts_mov_b32 m0, %2\n\ts_nop 0\n\tglobal_load_lds_dword %1, off\n\ts_mov_b32 m0, %0"
                   : "=&s"(sv) : "v"(p), "s"(lds0 + TSCR + wid * 256) : "memory");
    }
    if (blockIdx.x < 224 && tid < 256) {
      const float* red = (const float*)(smem + RED);
      float t = 0.f;
#pragma unroll
      for (int i = 0; i < 8; ++i) t += red[i * 256 + tid];
      aux1[(size_t)kc * FF + cg * 256 + tid] = t;
    }
    const int qw = m0 + wid * 16;
    const int tmin = (qw < WIN) ? (WIN - qw) >> 4 : 0;
    const int qo = (wid * 16 + fr) * 64 + ((g ^ ((4 - ((fr >> 2) & 3)) & 3)) << 4);
    const bf16x8 qf0 = *(const bf16x8*)(smem + QIMG + qo), qf1 = *(const bf16x8*)(smem + QIMG + 8192 + qo);
    char* ost = smem + QIMG + wid * 1024;
#pragma unroll 1
    for (int hd = 0; hd < 2; ++hd) {
      const bf16x8 qf = hd ? qf1 : qf0;
      char* op = ost + hd * 8192 + fr * 64;
      if (bm < 2) attn_body<true>(smem + KIMG + hd * HIMG, smem + VIMG + hd * HIMG, qf, wid, lane, tmin, op);
      else attn_body<false>(smem + KIMG + hd * HIMG, smem + VIMG + hd * HIMG, qf, wid, lane, 0, op);
    }
#pragma unroll
    for (int i = 0; i < 2; ++i) {
      const int row = i * 8 + (lane >> 3), ch = lane & 7;
      const u32x4 v = *(const u32x4*)(ost + (ch >> 2) * 8192 + row * 64 + (ch & 3) * 16);
      ST16(C + (size_t)(qw + row) * DM + bn * 64 + ch * 8, v);
    }
  } else if (EPI == EPI_LN_GELU) {
    constexpr int RS = BN * 4 + 16;
    if (wid < 4) {
#pragma unroll
      for (int j = 0; j < NT; ++j)
#pragma unroll
        for (int i = 0; i < 4; ++i)
          *(f32x4*)(smem + (wm * 64 + i * 16 + fr) * RS + (tcol[j] + g * 4) * 4) = acc[i][j];
    }
    __syncthreads();
#pragma unroll
    for (int c = tid; c < BM * CPR; c += NTHR) {
      const int row = c / CPR, ch = c % CPR;
      const f32x4 a0 = *(const f32x4*)(smem + row * RS + ch * 32);
      const f32x4 a1 = *(const f32x4*)(smem + row * RS + ch * 32 + 16);
      const f32x2 rs = *(const f32x2*)(smem + SIDE + row * 8);
      float v[8];
#pragma unroll
      for (int r = 0; r < 4; ++r) {
        const f32x4 cb = *(const f32x4*)(smem + SIDE + 1024 + ch * 64 + r * 16);
        const float x0 = (r < 2) ? a0[2 * r] : a1[2 * r - 4];
        const float x1 = (r < 2) ? a0[2 * r + 1] : a1[2 * r - 3];
        v[2 * r] = (x0 - rs[0] * cb[0]) * rs[1] + cb[1];
        v[2 * r + 1] = (x1 - rs[0] * cb[2]) * rs[1] + cb[3];
      }
      u32x4 o;
#pragma unroll
      for (int r = 0; r < 4; ++r) {
        const f32x2 x = {v[2 * r], v[2 * r + 1]};
        const f32x2 x2 = x * x;
        const f32x2 w = x2 * f32x2{-0.10294324f, -0.10294324f} + f32x2{-2.302208198f, -2.302208198f};
        const f32x2 z = x * w;
        f32x2 e = {__builtin_amdgcn_exp2f(z[0]), __builtin_amdgcn_exp2f(z[1])};
        e = e + f32x2{1.0f, 1.0f};
        const f32x2 y = x * f32x2{__builtin_amdgcn_rcpf(e[0]), __builtin_amdgcn_rcpf(e[1])};
        o[r] = pk2(y[0], y[1]);
      }
      ST16(C + (size_t)(m0 + row) * N + n0 + ch * 8, o);
    }
  } else if (EPI == EPI_RES_STATS) {
    constexpr int RS = BN * 4 + 16;
    constexpr int PART = BM * RS;
    if (wid < 4) {
#pragma unroll
      for (int i = 0; i < 4; ++i)
#pragma unroll
        for (int j = 0; j < NT; ++j)
          *(f32x4*)(smem + wx * PART + (wm * 64 + i * 16 + fr) * RS + (j * 16 + g * 4) * 4) = acc[i][j];
    }
    __syncthreads();
#pragma unroll
    for (int c = tid; c < BM * CPR; c += NTHR) {
      const int row = c / CPR, ch = c % CPR;
      const f32x4 a0 = *(const f32x4*)(smem + row * RS + ch * 32);
      const f32x4 a1 = *(const f32x4*)(smem + row * RS + ch * 32 + 16);
      const f32x4 b0 = *(const f32x4*)(smem + PART + row * RS + ch * 32);
      const f32x4 b1 = *(const f32x4*)(smem + PART + row * RS + ch * 32 + 16);
      const u32x4 rv = *(const u32x4*)(res + (size_t)(m0 + row) * N + n0 + ch * 8);
      u32x4 o;
      o[0] = pk2(a0[0] + b0[0] + bflo(rv[0]), a0[1] + b0[1] + bfhi(rv[0]));
      o[1] = pk2(a0[2] + b0[2] + bflo(rv[1]), a0[3] + b0[3] + bfhi(rv[1]));
      o[2] = pk2(a1[0] + b1[0] + bflo(rv[2]), a1[1] + b1[1] + bfhi(rv[2]));
      o[3] = pk2(a1[2] + b1[2] + bflo(rv[3]), a1[3] + b1[3] + bfhi(rv[3]));
      ST16(C + (size_t)(m0 + row) * N + n0 + ch * 8, o);
      float s = 0.f;
#pragma unroll
      for (int r = 0; r < 4; ++r) s += bflo(o[r]) + bfhi(o[r]);
      const float mean = sum8(s) * (1.0f / 64.0f);
      float m2 = 0.f;
#pragma unroll
      for (int r = 0; r < 4; ++r) {
        const float d0 = bflo(o[r]) - mean, d1 = bfhi(o[r]) - mean;
        m2 += d0 * d0 + d1 * d1;
      }
      m2 = sum8(m2);
      if (ch == 0) *(f32x2*)(aux0 + ((size_t)(m0 + row) * 16 + bn) * 2) = f32x2{mean, m2};
    }
  } else {
    constexpr int RS = BN * 4 + 16;
    if (wid < 4 && wm == hk) {
#pragma unroll
      for (int i = 0; i < 4; ++i)
#pragma unroll
        for (int j = 0; j < NT; ++j)
          *(f32x4*)(smem + (i * 16 + fr) * RS + (tcol[j] + g * 4) * 4) = acc[i][j];
    }
    __syncthreads();
#pragma unroll
    for (int c = tid; c < 64 * CPR; c += NTHR) {
      const int row = c / CPR, ch = c % CPR;
      const f32x4 a0 = *(const f32x4*)(smem + row * RS + ch * 32);
      const f32x4 a1 = *(const f32x4*)(smem + row * RS + ch * 32 + 16);
      const u32x4 bb = *(const u32x4*)(bias + n0 + ch * 8);
      const size_t go = (size_t)(m0 + hk * 64 + row) * N + n0 + ch * 8;
      const u32x4 rv = *(const u32x4*)(res + go);
      u32x4 o;
      o[0] = pk2(a0[0] + bflo(bb[0]) + bflo(rv[0]), a0[1] + bfhi(bb[0]) + bfhi(rv[0]));
      o[1] = pk2(a0[2] + bflo(bb[1]) + bflo(rv[1]), a0[3] + bfhi(bb[1]) + bfhi(rv[1]));
      o[2] = pk2(a1[0] + bflo(bb[2]) + bflo(rv[2]), a1[1] + bfhi(bb[2]) + bfhi(rv[2]));
      o[3] = pk2(a1[2] + bflo(bb[3]) + bflo(rv[3]), a1[3] + bfhi(bb[3]) + bfhi(rv[3]));
      ST16(C + go, o);
    }
  }
  asm volatile("s_waitcnt vmcnt(0)" ::: "memory");
}

__global__ __launch_bounds__(512, 1) void qkv_attn_kernel(const u16* __restrict__ A, const u16* __restrict__ W,
                                                         u16* __restrict__ o, const char* __restrict__ wo,
                                                         const u16* __restrict__ w1, float* __restrict__ kvg,
                                                         float* __restrict__ cspart, unsigned* __restrict__ flags) {
  gemm_body<192, 3, EPI_QKV, 8, 57344, 0>((const char*)w1, wo, A, W, o, nullptr, w1, kvg, cspart, flags, 3 * DM, DM);
}
__global__ __launch_bounds__(512, 1) void gemm_wo_kernel(const u16* __restrict__ A, const u16* __restrict__ W,
                                                        u16* __restrict__ C, const u16* __restrict__ res,
                                                        float* __restrict__ stats) {
  gemm_body<64, 6, EPI_RES_STATS, 4>(nullptr, nullptr, A, W, C, nullptr, res, stats, nullptr, nullptr, DM, DM);
}
__global__ __launch_bounds__(512, 1) void gemm_w1_kernel(const u16* __restrict__ A, const u16* __restrict__ W,
                                                        u16* __restrict__ C, const u16* __restrict__ bias,
                                                        float* __restrict__ stats, float* __restrict__ cspart,
                                                        const char* __restrict__ t0) {
  gemm_body<224, 3, EPI_LN_GELU, 8, 57344, 0>(t0, t0, A, W, C, bias, nullptr, stats, cspart, nullptr, FF, DM);
}
__global__ __launch_bounds__(512, 1) void gemm_w2_kernel(const u16* __restrict__ A, const u16* __restrict__ W,
                                                        u16* __restrict__ C, const u16* __restrict__ bias,
                                                        const u16* __restrict__ res, float* __restrict__ xchg,
                                                        unsigned* __restrict__ flags) {
  gemm_body<128, 4, EPI_SPLIT_BIAS_RES, 4>(nullptr, nullptr, A, W, C, bias, res, xchg, nullptr, flags, DM, FF);
}
extern "C" __global__ void zz_text_end() {}

constexpr int lds_bytes(int BN, int NBUF, bool side) { return NBUF * (128 * 64 * 2 + 64 * BN * 2) + (side ? 5120 : 0); }

extern "C" void kernel_launch(void* const* d_in, const int* in_sizes, int n_in, void* d_out, int out_size,
                              void* d_ws, size_t ws_size, hipStream_t stream) {
  const u16* x = (const u16*)d_in[0];
  const u16* wqkv = (const u16*)d_in[1];
  const u16* wo = (const u16*)d_in[2];
  const u16* w1 = (const u16*)d_in[4];
  const u16* b1 = (const u16*)d_in[5];
  const u16* w2 = (const u16*)d_in[6];
  const u16* b2 = (const u16*)d_in[7];
  u16* out = (u16*)d_out;
  char* ws = (char*)d_ws;
  u16* xn = (u16*)(ws);
  float* xchg = (float*)(ws);
  u16* h = (u16*)(ws + (8u << 20));
  u16* qkv = (u16*)(ws + (12u << 20));
  u16* o = (u16*)(ws + (20u << 20));
  u16* hh = (u16*)(ws + (12u << 20));
  float* stats = (float*)(ws + (26u << 20));
  float* cspart = (float*)(ws + (26u << 20) + (256u << 10));
  unsigned* flags = (unsigned*)(ws + (26u << 20) + (512u << 10));

  (void)hipFuncSetAttribute((const void*)qkv_attn_kernel, hipFuncAttributeMaxDynamicSharedMemorySize, lds_bytes(192, 3, true));
  (void)hipFuncSetAttribute((const void*)gemm_wo_kernel, hipFuncAttributeMaxDynamicSharedMemorySize, lds_bytes(64, 6, false));
  (void)hipFuncSetAttribute((const void*)gemm_w1_kernel, hipFuncAttributeMaxDynamicSharedMemorySize, lds_bytes(224, 3, true));
  (void)hipFuncSetAttribute((const void*)gemm_w2_kernel, hipFuncAttributeMaxDynamicSharedMemorySize, lds_bytes(128, 4, false));
  prep_kernel<<<S_LEN / 4, 256, 0, stream>>>(x, xn, (const char*)wqkv, (const char*)b1, (const char*)b2, flags);
  qkv_attn_kernel<<<256, 512, lds_bytes(192, 3, true), stream>>>(xn, wqkv, o, (const char*)wo, w1, (float*)qkv, cspart, flags + 512 * 32);
  gemm_wo_kernel<<<256, 512, lds_bytes(64, 6, false), stream>>>(o, wo, h, x, stats);
  gemm_w1_kernel<<<256, 512, lds_bytes(224, 3, true), stream>>>(h, w1, hh, b1, stats, cspart, (const char*)w2);
  gemm_w2_kernel<<<256, 512, lds_bytes(128, 4, false), stream>>>(hh, w2, out, b2, h, xchg, flags);
}
```
